```python
import torch

def _softmax(x, axis=-1):
    return torch.softmax(x, dim=axis)
def _gelu(x):
    return torch.nn.functional.gelu(x, approximate="tanh")
def _silu(x):
    return torch.nn.functional.silu(x)
def _sigmoid(x):
    return torch.sigmoid(x)
def _layernorm(x, axis=-1, eps=1e-5):
    m = x.mean(dim=axis, keepdim=True)
    v = x.var(dim=axis, keepdim=True, unbiased=False)
    return (x - m) / torch.sqrt(v + eps)
def _scatter_add(tgt, idx, src, axis=0):
    return tgt.clone().index_add_(axis, idx, src)
def _segment_sum(src, seg, n):
    out = torch.zeros((n,) + src.shape[1:], dtype=src.dtype)
    return out.index_add_(0, seg, src)
def _pad(x, pw):
    flat = tuple(v for pair in reversed(pw) for v in pair)
    return torch.nn.functional.pad(x, flat)

kernel_name = 'sequential_preLN__mha_block__dense_activation_gelu__prompt'

def setup_inputs():

    _w = (lambda a: (lambda t: t.to(torch.bfloat16) if t.is_floating_point() else t)(torch.as_tensor(a)))
    def _A_setup():
        B, S, D, H = 1, 2048, 512, 32
        return {
            "x": _w((lambda *s: torch.randn(*s, dtype=torch.bfloat16) * 0.05)(B, S, D)),
            "wqkv": _w((lambda *s: torch.randn(*s, dtype=torch.bfloat16) * 0.05)(D, 3 * D)),
            "wo": _w((lambda *s: torch.randn(*s, dtype=torch.bfloat16) * 0.05)(D, D)),
        }
    _A_in = _A_setup()

    def _B_setup():
        B, S, D, F = 1, 2048, 512, 1792
        return {
            "x": _w((lambda *s: torch.randn(*s, dtype=torch.bfloat16) * 0.05)(B, S, D)),
            "w1": _w((lambda *s: torch.randn(*s, dtype=torch.bfloat16) * 0.8)(D, F) * (D ** -0.5)),
            "b1": _w((lambda *s: torch.randn(*s, dtype=torch.bfloat16) * 0.2)(F) * 0.01),
            "w2": _w((lambda *s: torch.randn(*s, dtype=torch.bfloat16) * 0.2)(F, D) * (F ** -0.5)),
            "b2": _w((lambda *s: torch.randn(*s, dtype=torch.bfloat16) * 0.2)(D) * 0.01),
        }
    _B_in = _B_setup()

    return {
        "A_x": _A_in["x"],
        "A_wqkv": _A_in["wqkv"],
        "A_wo": _A_in["wo"],
        "B_x": _B_in["x"],
        "B_w1": _B_in["w1"],
        "B_b1": _B_in["b1"],
        "B_w2": _B_in["w2"],
        "B_b2": _B_in["b2"],
    }

def reference(A_x, A_wqkv, A_wo, B_x, B_w1, B_b1, B_w2, B_b2):
    def _A_ref(x, wqkv, wo):
        B, S, D = x.shape
        H = 32
        Dh = D // H
        qkv = (lambda x, *d: x.permute(*d))((x @ wqkv).reshape(B, S, 3, H, Dh), 2, 0, 3, 1, 4)
        q, k, v = qkv[0], qkv[1], qkv[2]
        scores = torch.einsum("bhqd,bhkd->bhqk", q, k) * (Dh ** -0.5)
        p = _softmax(scores, axis=-1)
        o = torch.einsum("bhqk,bhkd->bhqd", p, v)
        return (lambda x, *d: x.permute(*d))(o, 0, 2, 1, 3).reshape(B, S, D) @ wo

    def _B_ref(x, w1, b1, w2, b2):
        h = torch.einsum("bsd,df->bsf", x, w1) + b1
        h = _gelu(h)
        return torch.einsum("bsf,fd->bsd", h, w2) + b2

    x = A_x
    h = x + _A_ref(_layernorm(x, axis=-1), A_wqkv, A_wo)
    h2 = h + _B_ref(_layernorm(h, axis=-1), B_w1, B_b1, B_w2, B_b2)
    return h2
```

```cpp
#include <hip/hip_runtime.h>
#include <hip/hip_bf16.h>
#include <stdint.h>

typedef __bf16 bf16x8 __attribute__((ext_vector_type(8)));
typedef __bf16 bf16x4 __attribute__((ext_vector_type(4)));
typedef __bf16 bf16x2 __attribute__((ext_vector_type(2)));
typedef float f32x16 __attribute__((ext_vector_type(16)));
typedef float f32x4 __attribute__((ext_vector_type(4)));
typedef float f32x2 __attribute__((ext_vector_type(2)));
typedef unsigned u32x4 __attribute__((ext_vector_type(4)));
typedef unsigned u32x2 __attribute__((ext_vector_type(2)));
#define LAS __attribute__((address_space(3)))
#define GAS __attribute__((address_space(1)))
#define DEV __device__ __forceinline__

constexpr int S_ = 2048, D_ = 512, F_ = 1792;
constexpr size_t WS_Q = 0;
constexpr size_t WS_K = WS_Q + (2u << 20);
constexpr size_t WS_V = WS_K + (2u << 20);
constexpr size_t WS_O = WS_V + (2u << 20);
constexpr size_t WS_H = WS_O + (2u << 20);
constexpr size_t WS_G = WS_H + (2u << 20);
constexpr size_t WS_WO = WS_G + (7u << 20);
constexpr size_t WS_W1 = WS_WO + (512u << 10);
constexpr size_t WS_W2 = WS_W1 + (1792u << 10);
constexpr size_t WS_NRM = WS_W2 + (1792u << 10);
constexpr size_t WS_ST = WS_NRM + 16384;

DEV void dma16(const void* g, void* l) { __builtin_amdgcn_global_load_lds((const GAS void*)g, (LAS void*)l, 16, 0, 0); }
DEV void dma4(const void* g, void* l) { __builtin_amdgcn_global_load_lds((const GAS void*)g, (LAS void*)l, 4, 0, 0); }
DEV f32x16 mfma32(bf16x8 a, bf16x8 b, f32x16 c) { return __builtin_amdgcn_mfma_f32_32x32x16_bf16(a, b, c, 0, 0, 0); }
DEV bf16x4 trread(const char* l) { return __builtin_amdgcn_ds_read_tr16_b64_v4bf16((LAS bf16x4*)l); }
DEV bf16x8 cat8(bf16x4 a, bf16x4 b) { return __builtin_shufflevector(a, b, 0, 1, 2, 3, 4, 5, 6, 7); }
DEV bf16x2 pk2(float a, float b) { f32x2 v = {a, b}; return __builtin_convertvector(v, bf16x2); }
DEV unsigned pk2u(float a, float b) { return __builtin_bit_cast(unsigned, pk2(a, b)); }
DEV float dot2(bf16x2 a, bf16x2 b, float c) { return __builtin_amdgcn_fdot2_f32_bf16(a, b, c, false); }
DEV float bflo(unsigned u) { return __builtin_bit_cast(float, u << 16); }
DEV float bfhi(unsigned u) { return __builtin_bit_cast(float, u & 0xffff0000u); }
DEV float sum8(bf16x8 v, float c) {
  const bf16x2 one = {(__bf16)1.0f, (__bf16)1.0f};
  c = dot2(__builtin_shufflevector(v, v, 0, 1), one, c); c = dot2(__builtin_shufflevector(v, v, 2, 3), one, c);
  c = dot2(__builtin_shufflevector(v, v, 4, 5), one, c); c = dot2(__builtin_shufflevector(v, v, 6, 7), one, c);
  return c;
}
DEV float sq8(bf16x8 v, float c) {
  bf16x2 a = __builtin_shufflevector(v, v, 0, 1), b = __builtin_shufflevector(v, v, 2, 3), d = __builtin_shufflevector(v, v, 4, 5), e = __builtin_shufflevector(v, v, 6, 7);
  c = dot2(a, a, c); c = dot2(b, b, c); c = dot2(d, d, c); c = dot2(e, e, c);
  return c;
}
DEV float xhalf(float v) { return __shfl_xor(v, 32); }
#define WAITVM(n) asm volatile("s_waitcnt vmcnt(" #n ")" ::: "memory")
#define WAITLG() asm volatile("s_waitcnt lgkmcnt(0)" ::: "memory")
DEV void bar() { asm volatile("s_waitcnt lgkmcnt(0)" ::: "memory"); __builtin_amdgcn_s_barrier(); asm volatile("" ::: "memory"); }

constexpr int K1_STAGE = 28672, K1_NS = 5, K1_MISC = K1_STAGE * K1_NS;
constexpr int K1_LDS = K1_MISC + 512 + 1024;
__global__ __launch_bounds__(256, 1) void k_qkv(const char* __restrict__ x, const char* __restrict__ wqkv, char* __restrict__ ws) {
  extern __shared__ __attribute__((aligned(16))) char lds[];
  const int b = blockIdx.x, xcd = b & 7, idx = b >> 3;
  const int tb = (xcd & 3) * 4 + (idx & 3), p = (xcd >> 2) * 8 + (idx >> 2);
  const int tid = threadIdx.x, lane = tid & 63, w = __builtin_amdgcn_readfirstlane(tid >> 6);
  const int r = lane & 31, h = lane >> 5;
  const int rot = (tb + p) & 7;
  const int xr = lane >> 3;
  const char* xsrc = x + (size_t)(tb * 128 + w * 32 + xr) * 1024;
  const int xc0 = ((lane & 7) ^ (xr >> 1)) * 16, xc1 = ((lane & 7) ^ (4 + (xr >> 1))) * 16;
  const char* wsrc = wqkv + (size_t)(w * 16 + (lane >> 2)) * 3072 + p * 64 + (lane & 3) * 16;
  auto issue = [&](int st) {
    const int kk = (st + rot) & 7;
    char* slot = lds + (st % K1_NS) * K1_STAGE;
    const char* xs = xsrc + kk * 128;
    dma16(xs + xc0, slot + w * 4096);
    dma16(xs + 8 * 1024 + xc1, slot + w * 4096 + 1024);
    dma16(xs + 16 * 1024 + xc0, slot + w * 4096 + 2048);
    dma16(xs + 24 * 1024 + xc1, slot + w * 4096 + 3072);
    const char* wsb = wsrc + (size_t)kk * 64 * 3072;
    dma16(wsb, slot + 16384 + w * 1024);
    dma16(wsb + 1024, slot + 16384 + 4096 + w * 1024);
    dma16(wsb + 2048, slot + 16384 + 8192 + w * 1024);
  };
  issue(0); issue(1); issue(2); issue(3);
  float* cs = (float*)(lds + K1_MISC);
  if (tid < 96) cs[tid] = 0.f;
  f32x16 aq = {}, ak = {}, av = {};
  float s1 = 0.f, s2 = 0.f, cq = 0.f, ck = 0.f, cv = 0.f;
  const int xoff = w * 4096 + r * 128;
  const int xsw = (r >> 1) & 7;
  const int woff = 16384 + (8 * h + ((lane & 15) >> 2)) * 64 + ((lane >> 4) & 1) * 32 + (lane & 3) * 8;
#pragma unroll 1
  for (int st = 0; st < 8; ++st) {
    if (st <= 4) WAITVM(21); else if (st == 5) WAITVM(14); else if (st == 6) WAITVM(7); else WAITVM(0);
    bar();
    if (st < 4) issue(st + 4);
    const char* slot = lds + (st % K1_NS) * K1_STAGE;
#pragma unroll
    for (int ks = 0; ks < 4; ++ks) {
      bf16x8 xf = *(const bf16x8*)(slot + xoff + (((2 * ks + h) ^ xsw) * 16));
      const char* wp = slot + woff + ks * 1024;
      bf16x8 fq = cat8(trread(wp), trread(wp + 256));
      bf16x8 fk = cat8(trread(wp + 4096), trread(wp + 4096 + 256));
      bf16x8 fv = cat8(trread(wp + 8192), trread(wp + 8192 + 256));
      aq = mfma32(fq, xf, aq);
      ak = mfma32(fk, xf, ak);
      av = mfma32(xf, fv, av);
      s1 = sum8(xf, s1); s2 = sq8(xf, s2);
      if (ks == w) { cq = sum8(fq, cq); ck = sum8(fk, ck); cv = sum8(fv, cv); }
    }
  }
  atomicAdd(&cs[r], cq); atomicAdd(&cs[32 + r], ck); atomicAdd(&cs[64 + r], cv);
  s1 += xhalf(s1); s2 += xhalf(s2);
  const float mean = s1 * (1.f / 512.f);
  const float var = fmaxf(s2 * (1.f / 512.f) - mean * mean, 0.f);
  const float rstd = rsqrtf(var + 1e-5f);
  float* rs = (float*)(lds + K1_MISC + 512) + w * 64;
  if (h == 0) rs[r] = rstd; else rs[32 + r] = -mean * rstd;
  bar();
  const int tile = tb * 4 + w;
  float* nrm = (float*)(ws + WS_NRM);
  {
    const float qs = rstd * (0.25f * 1.44269504088896f), mq = -mean * qs, mk = -mean * rstd;
#pragma unroll
    for (int a = 0; a < 2; ++a) {
      float q[8], k[8];
#pragma unroll
      for (int g = 0; g < 2; ++g) {
        f32x4 c4q = *(const f32x4*)(cs + 16 * a + 8 * g + 4 * h), c4k = *(const f32x4*)(cs + 32 + 16 * a + 8 * g + 4 * h);
#pragma unroll
        for (int e = 0; e < 4; ++e) { q[4 * g + e] = aq[8 * a + 4 * g + e] * qs + mq * c4q[e]; k[4 * g + e] = ak[8 * a + 4 * g + e] * rstd + mk * c4k[e]; }
      }
      u32x4 qo = {pk2u(q[0], q[1]), pk2u(q[2], q[3]), pk2u(q[4], q[5]), pk2u(q[6], q[7])};
      u32x4 ko = {pk2u(k[0], k[1]), pk2u(k[2], k[3]), pk2u(k[4], k[5]), pk2u(k[6], k[7])};
      const size_t off = ((size_t)((2 * p + a) * 64 + tile) * 64 + lane) * 16;
      *(u32x4*)(ws + WS_Q + off) = qo;
      *(u32x4*)(ws + WS_K + off) = ko;
      float qn = sq8(__builtin_bit_cast(bf16x8, qo), 0.f), kn = sq8(__builtin_bit_cast(bf16x8, ko), 0.f);
      qn += xhalf(qn); kn += xhalf(kn);
      float v = h ? kn : qn;
#pragma unroll
      for (int o = 1; o < 32; o <<= 1) v = fmaxf(v, __shfl_xor(v, o));
      if (r == 0) nrm[h * 2048 + (2 * p + a) * 64 + tile] = v;
    }
  }
  {
    const float cvv = cs[64 + r];
#pragma unroll
    for (int s = 0; s < 2; ++s) {
      float v[8];
#pragma unroll
      for (int g = 0; g < 2; ++g) {
        f32x4 r4 = *(const f32x4*)(rs + 16 * s + 8 * g + 4 * h), m4 = *(const f32x4*)(rs + 32 + 16 * s + 8 * g + 4 * h);
#pragma unroll
        for (int e = 0; e < 4; ++e) v[4 * g + e] = av[8 * s + 4 * g + e] * r4[e] + m4[e] * cvv;
      }
      u32x4 vo = {pk2u(v[0], v[1]), pk2u(v[2], v[3]), pk2u(v[4], v[5]), pk2u(v[6], v[7])};
      const size_t off = (size_t)((2 * p + (r >> 4)) * 64 + tile) * 1024 + ((s * 2 + h) * 16 + (r & 15)) * 16;
      *(u32x4*)(ws + WS_V + off) = vo;
    }
  }
}

constexpr int A_K = 0, A_V = 65536, A_VS = 1040, A_Q = A_V + 64 * A_VS, A_N = A_Q + 8192, A_W = A_N + 8 * 512, A_LDS = A_W + 16384;
__global__ __launch_bounds__(512, 2) void k_attn(const char* __restrict__ wo, const char* __restrict__ w1, const char* __restrict__ w2, char* __restrict__ ws) {
  extern __shared__ __attribute__((aligned(16))) char lds[];
  const int b = blockIdx.x, xcd = b & 7, idx = b >> 3;
  const int head = xcd * 4 + (idx & 3), qb = idx >> 2;
  const int tid = threadIdx.x, lane = tid & 63, w = __builtin_amdgcn_readfirstlane(tid >> 6);
  const int r = lane & 31, h = lane >> 5;
  const char* kg = ws + WS_K + (size_t)head * 65536 + w * 1024 + lane * 16;
  const char* vg = ws + WS_V + (size_t)head * 65536 + w * 1024 + lane * 16;
  auto issue = [&](int c) {
    dma16(kg + c * 8192, lds + A_K + (c * 8 + w) * 1024);
    dma16(vg + c * 8192, lds + A_V + (c * 8 + w) * A_VS);
  };
  dma16(ws + WS_Q + ((size_t)head * 64 + qb * 8 + w) * 1024 + lane * 16, lds + A_Q + w * 1024);
  dma4(ws + WS_NRM + 8192 + head * 256 + lane * 4, lds + A_N + w * 512);
  dma4(ws + WS_NRM + head * 256 + qb * 32 + (lane & 7) * 4, lds + A_N + w * 512 + 256);
  issue(0); issue(1); issue(2); issue(3);
  if (tid < 64) *(u32x4*)(lds + A_V + tid * A_VS + 1024) = (u32x4){0x3f803f80u, 0x3f803f80u, 0x3f803f80u, 0x3f803f80u};
  const char* sw; char* sd; int sld, skt;
  if (b < 32)       { const int u = b,       nb = u & 7, kb = u >> 3;  sw = wo + (size_t)kb * 128 * 1024 + nb * 128; sld = 1024; skt = 32;  sd = ws + WS_WO + ((size_t)nb * 2 * 32 + kb * 8) * 1024; }
  else if (b < 144) { const int u = b - 32,  nb = u >> 2, kb = u & 3;  sw = w1 + (size_t)kb * 128 * 3584 + nb * 128; sld = 3584; skt = 32;  sd = ws + WS_W1 + ((size_t)nb * 2 * 32 + kb * 8) * 1024; }
  else              { const int u = b - 144, nb = u & 7, kb = u >> 3;  sw = w2 + (size_t)kb * 128 * 1024 + nb * 128; sld = 1024; skt = 112; sd = ws + WS_W2 + ((size_t)nb * 2 * 112 + kb * 8) * 1024; }

  WAITVM(8);
  const bf16x8 qf = *(const bf16x8*)(lds + A_Q + w * 1024 + lane * 16);
  float kn = *(const float*)(lds + A_N + w * 512 + lane * 4), qn = *(const float*)(lds + A_N + w * 512 + 256 + lane * 4);
#pragma unroll
  for (int o = 1; o < 64; o <<= 1) { kn = fmaxf(kn, __shfl_xor(kn, o)); qn = fmaxf(qn, __shfl_xor(qn, o)); }
  const float bound = sqrtf(kn * qn) * 1.001f;
  const bool safe = __builtin_amdgcn_readfirstlane((int)(bound <= 50.f)) != 0;
  float shift = bound;
  const int koff = A_K + lane * 16;
  if (!safe) {
    issue(4); issue(5); issue(6); issue(7);
    WAITVM(0);
    bar();
    float m = -INFINITY;
#pragma unroll 1
    for (int t = 0; t < 64; ++t) {
      const bf16x8 kf = *(const bf16x8*)(lds + koff + t * 1024);
      f32x16 z = {};
      f32x16 s = mfma32(kf, qf, z);
#pragma unroll
      for (int i = 0; i < 16; ++i) m = fmaxf(m, s[i]);
    }
    shift = fmaxf(m, xhalf(m));
  }
  f32x16 cneg;
#pragma unroll
  for (int i = 0; i < 16; ++i) cneg[i] = -shift;
  f32x16 o = {};
  const int v0 = A_V + ((r < 16) ? (h * 16 + r) * 16 : 1024), v1 = A_V + ((r < 16) ? 512 + (h * 16 + r) * 16 : 1024);
#pragma unroll 1
  for (int c = 0; c < 8; ++c) {
    if (c < 4) {
      WAITVM(6);
      bar();
      if (safe) issue(c + 4);
      if (c == 3) {
        const char* s0 = sw + (size_t)(w * 16 + (lane >> 3)) * sld + (lane & 7) * 16;
        dma16(s0, lds + A_W + w * 2048);
        dma16(s0 + (size_t)8 * sld, lds + A_W + w * 2048 + 1024);
      }
    } else if (c == 4) {
      WAITVM(2);
      bar();
    } else if (c == 6) {
      WAITVM(0);
      const char* wp = lds + A_W + w * 2048 + (4 * h + ((lane & 15) >> 2)) * 128 + ((lane >> 4) & 1) * 32 + (lane & 3) * 8;
      bf16x8 f0 = cat8(trread(wp), trread(wp + 1024));
      bf16x8 f1 = cat8(trread(wp + 64), trread(wp + 1024 + 64));
      *(bf16x8*)(sd + w * 1024 + lane * 16) = f0;
      *(bf16x8*)(sd + (size_t)(skt + w) * 1024 + lane * 16) = f1;
    }
    const char* kb = lds + koff + c * 8192;
    const char* vb0 = lds + v0 + c * 8 * A_VS;
    const char* vb1 = lds + v1 + c * 8 * A_VS;
#pragma unroll 2
    for (int t = 0; t < 8; ++t) {
      const bf16x8 kf = *(const bf16x8*)(kb + t * 1024);
      const bf16x8 va = *(const bf16x8*)(vb0 + t * A_VS), vb = *(const bf16x8*)(vb1 + t * A_VS);
      f32x16 s = mfma32(kf, qf, cneg);
      unsigned pp[8];
#pragma unroll
      for (int i = 0; i < 8; ++i) pp[i] = pk2u(__builtin_amdgcn_exp2f(s[2 * i]), __builtin_amdgcn_exp2f(s[2 * i + 1]));
      u32x4 p0 = {pp[0], pp[1], pp[2], pp[3]}, p1 = {pp[4], pp[5], pp[6], pp[7]};
      o = mfma32(va, __builtin_bit_cast(bf16x8, p0), o);
      o = mfma32(vb, __builtin_bit_cast(bf16x8, p1), o);
    }
  }
  const float inv = 1.f / o[8];
  u32x4 ov = {pk2u(o[0] * inv, o[1] * inv), pk2u(o[2] * inv, o[3] * inv), pk2u(o[4] * inv, o[5] * inv), pk2u(o[6] * inv, o[7] * inv)};
  *(u32x4*)(ws + WS_O + ((size_t)(qb * 8 + w) * 32 + head) * 1024 + lane * 16) = ov;
}

template <int KS  , bool LAST>
__global__ __launch_bounds__(256, 1) void k_proj(const char* __restrict__ act, const char* __restrict__ wt, const char* __restrict__ res,
                                                 const char* __restrict__ bias, char* __restrict__ ws, char* __restrict__ out) {
  extern __shared__ __attribute__((aligned(16))) char lds[];
  constexpr int PW = KS / 4;
  constexpr int NB = PW <= 8 ? PW : PW / 2;
  const int b = blockIdx.x, xcd = b & 7, idx = b >> 3;
  const int mt = xcd * 8 + (idx & 3) * 2, nt = (idx >> 2) * 2;
  const int tid = threadIdx.x, lane = tid & 63, w = __builtin_amdgcn_readfirstlane(tid >> 6);
  const int r = lane & 31, h = lane >> 5;
  const char* ap = act + ((size_t)mt * KS + w * PW) * 1024 + lane * 16;
  const char* wp = wt + ((size_t)nt * KS + w * PW) * 1024 + lane * 16;
  bf16x8 fa0[NB], fa1[NB], fw0[NB], fw1[NB];
#pragma unroll
  for (int i = 0; i < NB; ++i) {
    fa0[i] = *(const bf16x8*)(ap + i * 1024); fw0[i] = *(const bf16x8*)(wp + i * 1024);
    fa1[i] = *(const bf16x8*)(ap + (KS + i) * 1024); fw1[i] = *(const bf16x8*)(wp + (KS + i) * 1024);
  }
  const int omt = mt + (w & 1), ont = nt + (w >> 1);
  u32x2 rx[4];
  if (LAST) {
    const u32x4 a = *(const u32x4*)(res + ((size_t)omt * 32 + ont * 2) * 1024 + lane * 16), c = *(const u32x4*)(res + ((size_t)omt * 32 + ont * 2 + 1) * 1024 + lane * 16);
    rx[0] = (u32x2){a.x, a.y}; rx[1] = (u32x2){a.z, a.w}; rx[2] = (u32x2){c.x, c.y}; rx[3] = (u32x2){c.z, c.w};
  } else {
#pragma unroll
    for (int g = 0; g < 4; ++g) rx[g] = *(const u32x2*)(res + (size_t)(omt * 32 + r) * 1024 + (ont * 32 + 8 * g + 4 * h) * 2);
  }
  u32x2 bx[4];
  if (LAST) {
#pragma unroll
    for (int g = 0; g < 4; ++g) bx[g] = *(const u32x2*)(bias + (ont * 32 + 8 * g + 4 * h) * 2);
  }
  f32x16 acc[4] = {};
#pragma unroll
  for (int i = 0; i < PW; ++i) {
    const int s = i % NB;
    acc[0] = mfma32(fw0[s], fa0[s], acc[0]);
    acc[1] = mfma32(fw0[s], fa1[s], acc[1]);
    acc[2] = mfma32(fw1[s], fa0[s], acc[2]);
    acc[3] = mfma32(fw1[s], fa1[s], acc[3]);
    if (i + NB < PW) {
      fa0[s] = *(const bf16x8*)(ap + (i + NB) * 1024); fw0[s] = *(const bf16x8*)(wp + (i + NB) * 1024);
      fa1[s] = *(const bf16x8*)(ap + (KS + i + NB) * 1024); fw1[s] = *(const bf16x8*)(wp + (KS + i + NB) * 1024);
    }
  }
#pragma unroll
  for (int t = 0; t < 4; ++t) {
    if (t != w) {
#pragma unroll
      for (int g = 0; g < 4; ++g)
        *(f32x4*)(lds + (t * 4 + w) * 4096 + g * 1024 + lane * 16) = (f32x4){acc[t][4 * g], acc[t][4 * g + 1], acc[t][4 * g + 2], acc[t][4 * g + 3]};
    }
  }
  f32x16 v = w == 0 ? acc[0] : w == 1 ? acc[1] : w == 2 ? acc[2] : acc[3];
  __syncthreads();
#pragma unroll
  for (int j = 1; j < 4; ++j) {
    const int src = (w + j) & 3;
#pragma unroll
    for (int g = 0; g < 4; ++g) {
      const f32x4 q = *(const f32x4*)(lds + (w * 4 + src) * 4096 + g * 1024 + lane * 16);
      v[4 * g] += q[0]; v[4 * g + 1] += q[1]; v[4 * g + 2] += q[2]; v[4 * g + 3] += q[3];
    }
  }
  unsigned pk[8];
#pragma unroll
  for (int g = 0; g < 4; ++g) {
    float a0 = v[4 * g] + bflo(rx[g].x), a1 = v[4 * g + 1] + bfhi(rx[g].x), a2 = v[4 * g + 2] + bflo(rx[g].y), a3 = v[4 * g + 3] + bfhi(rx[g].y);
    if (LAST) { a0 += bflo(bx[g].x); a1 += bfhi(bx[g].x); a2 += bflo(bx[g].y); a3 += bfhi(bx[g].y); }
    pk[2 * g] = pk2u(a0, a1); pk[2 * g + 1] = pk2u(a2, a3);
  }
  if (LAST) {
    char* op = out + (size_t)(omt * 32 + r) * 1024 + ont * 64 + h * 8;
#pragma unroll
    for (int g = 0; g < 4; ++g) *(u32x2*)(op + g * 16) = (u32x2){pk[2 * g], pk[2 * g + 1]};
  } else {
    u32x4 h0 = {pk[0], pk[1], pk[2], pk[3]}, h1 = {pk[4], pk[5], pk[6], pk[7]};
    char* hp = ws + WS_H + ((size_t)omt * 32 + ont * 2) * 1024 + lane * 16;
    *(u32x4*)hp = h0; *(u32x4*)(hp + 1024) = h1;
    float s1 = sum8(__builtin_bit_cast(bf16x8, h1), sum8(__builtin_bit_cast(bf16x8, h0), 0.f));
    float s2 = sq8(__builtin_bit_cast(bf16x8, h1), sq8(__builtin_bit_cast(bf16x8, h0), 0.f));
    s1 += xhalf(s1); s2 += xhalf(s2);
    if (h == 0) *(f32x2*)(ws + WS_ST + ((size_t)(omt * 16 + ont) * 32 + r) * 8) = (f32x2){s1, s2};
  }
}

constexpr int F1_MISC = 131072, F1_LDS = F1_MISC + 4 * 256;
DEV float gelu_t(float u) {
  const float c0 = -2.f * 0.7978845608028654f * 1.44269504088896f, c1 = c0 * 0.044715f;
  const float e = __builtin_amdgcn_exp2f(u * (c0 + c1 * u * u));
  return u * __builtin_amdgcn_rcpf(1.f + e);
}
__global__ __launch_bounds__(256, 1) void k_ffn1(const char* __restrict__ b1, char* __restrict__ ws) {
  extern __shared__ __attribute__((aligned(16))) char lds[];
  const int b = blockIdx.x, xcd = b & 7, idx = b >> 3;
  const int tid = threadIdx.x, lane = tid & 63, w = __builtin_amdgcn_readfirstlane(tid >> 6);
  const int mt = xcd * 8 + (idx & 1) * 4, nt = (idx >> 1) * 4 + w;
  const int r = lane & 31, h = lane >> 5;
  const char* wp = ws + WS_W1 + (size_t)nt * 32768 + lane * 16;
  bf16x8 fw[32];
#pragma unroll
  for (int i = 0; i < 32; ++i) fw[i] = *(const bf16x8*)(wp + i * 1024);
  u32x2 bx[4];
#pragma unroll
  for (int g = 0; g < 4; ++g) bx[g] = *(const u32x2*)(b1 + (nt * 32 + 8 * g + 4 * h) * 2);
  float mu[4], rsd[4];
#pragma unroll
  for (int m = 0; m < 4; ++m) {
    float s1 = 0.f, s2 = 0.f;
#pragma unroll
    for (int j = 0; j < 8; ++j) { const f32x2 q = *(const f32x2*)(ws + WS_ST + ((size_t)((mt + m) * 16 + 8 * h + j) * 32 + r) * 8); s1 += q.x; s2 += q.y; }
    s1 += xhalf(s1); s2 += xhalf(s2);
    const float mean = s1 * (1.f / 512.f), var = fmaxf(s2 * (1.f / 512.f) - mean * mean, 0.f);
    rsd[m] = rsqrtf(var + 1e-5f); mu[m] = -mean * rsd[m];
  }
  const char* hp = ws + WS_H + (size_t)(mt + w) * 32768 + lane * 16;
#pragma unroll
  for (int i = 0; i < 32; ++i) dma16(hp + i * 1024, lds + (i * 4 + w) * 1024);
  float cs = 0.f;
#pragma unroll
  for (int i = 0; i < 32; ++i) cs = sum8(fw[i], cs);
  cs += xhalf(cs);
  float* csl = (float*)(lds + F1_MISC) + w * 64;
  csl[lane] = cs;
  f32x16 acc[4] = {};
  WAITVM(0);
  bar();
#pragma unroll
  for (int i = 0; i < 32; ++i) {
#pragma unroll
    for (int m = 0; m < 4; ++m) {
      const bf16x8 af = *(const bf16x8*)(lds + (i * 4 + m) * 1024 + lane * 16);
      acc[m] = mfma32(fw[i], af, acc[m]);
    }
  }
  float cb[16], bb[16];
#pragma unroll
  for (int g = 0; g < 4; ++g) {
    const f32x4 c4 = *(const f32x4*)(csl + 8 * g + 4 * h);
    cb[4 * g] = c4[0]; cb[4 * g + 1] = c4[1]; cb[4 * g + 2] = c4[2]; cb[4 * g + 3] = c4[3];
    bb[4 * g] = bflo(bx[g].x); bb[4 * g + 1] = bfhi(bx[g].x); bb[4 * g + 2] = bflo(bx[g].y); bb[4 * g + 3] = bfhi(bx[g].y);
  }
#pragma unroll
  for (int m = 0; m < 4; ++m) {
    unsigned pk[8];
#pragma unroll
    for (int i = 0; i < 8; ++i) {
      const float u0 = acc[m][2 * i] * rsd[m] + (mu[m] * cb[2 * i] + bb[2 * i]);
      const float u1 = acc[m][2 * i + 1] * rsd[m] + (mu[m] * cb[2 * i + 1] + bb[2 * i + 1]);
      pk[i] = pk2u(gelu_t(u0), gelu_t(u1));
    }
    char* gp = ws + WS_G + ((size_t)(mt + m) * 112 + nt * 2) * 1024 + lane * 16;
    *(u32x4*)gp = (u32x4){pk[0], pk[1], pk[2], pk[3]};
    *(u32x4*)(gp + 1024) = (u32x4){pk[4], pk[5], pk[6], pk[7]};
  }
}

extern "C" void kernel_launch(void* const* d_in, const int* in_sizes, int n_in,
                              void* d_out, int out_size, void* d_ws, size_t ws_size,
                              hipStream_t stream) {
  const char* x = (const char*)d_in[0];
  const char* wqkv = (const char*)d_in[1];
  const char* wo = (const char*)d_in[2];
  const char* w1 = (const char*)d_in[4];
  const char* b1 = (const char*)d_in[5];
  const char* w2 = (const char*)d_in[6];
  const char* b2 = (const char*)d_in[7];
  char* ws = (char*)d_ws;
  char* out = (char*)d_out;
  (void)hipFuncSetAttribute((const void*)k_qkv, hipFuncAttributeMaxDynamicSharedMemorySize, K1_LDS);
  (void)hipFuncSetAttribute((const void*)k_attn, hipFuncAttributeMaxDynamicSharedMemorySize, A_LDS);
  (void)hipFuncSetAttribute((const void*)k_ffn1, hipFuncAttributeMaxDynamicSharedMemorySize, F1_LDS);
  (void)hipFuncSetAttribute((const void*)k_proj<32, false>, hipFuncAttributeMaxDynamicSharedMemorySize, 65536);
  (void)hipFuncSetAttribute((const void*)k_proj<112, true>, hipFuncAttributeMaxDynamicSharedMemorySize, 65536);
  k_qkv<<<256, 256, K1_LDS, stream>>>(x, wqkv, ws);
  k_attn<<<256, 512, A_LDS, stream>>>(wo, w1, w2, ws);
  k_proj<32, false><<<256, 256, 65536, stream>>>(ws + WS_O, ws + WS_WO, x, b2, ws, out);
  k_ffn1<<<224, 256, F1_LDS, stream>>>(b1, ws);
  k_proj<112, true><<<256, 256, 65536, stream>>>(ws + WS_G, ws + WS_W2, ws + WS_H, b2, ws, out);
}
```

```cpp
#include <hip/hip_runtime.h>
#include <stdint.h>
constexpr size_t WS_K = 0, WS_V = 2097152, WS_O = 4194304, WS_H = 6291456, WS_G = 8388608, WS_WO = 15728640, WS_W1 = 16252928, WS_W2 = 18087936;
constexpr size_t WS_FL = 19922944, WS_DQ = 20971520, WS_ST = 23068672;
#define MEGA_STAMPS { "end" }
#define MEGA_VNAT 1
#define MEGA_NSTAMP 0
#define MEGA_NCY 0
#define MEGA_NP 0
#define MEGA_DIFF 0
#define MEGA_PN {  "" }
__global__ __launch_bounds__(512, 2) void mega(const void* x, const void* wqkv, const void* wo, const void* w1, const void* b1,
                                               const void* w2, const void* b2, void* ws, void* out) {
  asm volatile(
    "Lstart_%=:\n"
    "s_load_dwordx16 s[64:79], s[0:1], 0\n"
    "s_load_dwordx2 s[80:81], s[0:1], 64\n"
    "s_getpc_b64 s[32:33]\n"
    "s_setprio 3\n"
    "v_lshrrev_b32 v2, 6, v0\n"
    "v_and_b32 v1, 63, v0\n"
    "v_readfirstlane_b32 s3, v2\n"
    "s_and_b32 s43, s2, 7\n"
    "s_lshr_b32 s44, s2, 3\n"
    "s_and_b32 s45, s43, 3\n"
    "s_and_b32 s46, s44, 3\n"
    "s_lshl2_add_u32 s36, s45, s46\n"
    "s_lshr_b32 s45, s43, 2\n"
    "s_lshr_b32 s46, s44, 2\n"
    "s_lshl3_add_u32 s37, s45, s46\n"
    "s_and_b32 s33, s33, 0xffff\n"
    "s_mov_b32 s34, Lhot_%=-Lstart_%=-64\n"
    "s_mov_b32 s35, 0x20000\n"
    "s_lshl3_add_u32 s45, s44, s3\n"
    "s_lshl_b32 s45, s45, 7\n"
    "v_and_b32 v3, 1, v0\n"
    "v_lshlrev_b32 v3, 15, v3\n"
    "buffer_load_dword v254, v3, s[32:35], s45 offen\n"
    "s_and_b32 s47, s3, 1\n"
    "s_lshl_b32 s48, s47, 2\n"
    "v_lshrrev_b32 v6, 4, v1\n"
    "v_or_b32 v6, s48, v6\n"
    "v_and_b32 v7, 7, v1\n"
    "v_xor_b32 v6, v6, v7\n"
    "v_lshrrev_b32 v7, 3, v1\n"
    "v_lshlrev_b32 v7, 10, v7\n"
    "v_lshl_or_b32 v2, v6, 4, v7\n"
    "s_lshl_b32 s48, s3, 14\n"
    "s_lshl_b32 s49, s36, 17\n"
    "s_add_u32 s48, s48, s49\n"
    "v_add_u32 v2, s48, v2\n"
    "v_add_u32 v3, 7168, v2\n"
    "v_mul_u32_u24 v6, 43, v1\n"
    "v_lshrrev_b32 v6, 9, v6\n"
    "v_mul_u32_u24 v7, 12, v6\n"
    "v_sub_u32 v7, v1, v7\n"
    "v_lshrrev_b32 v8, 2, v7\n"
    "v_lshrrev_b32 v10, 1, v7\n"
    "v_and_b32 v9, 2, v10\n"
    "v_and_b32 v10, 3, v7\n"
    "v_xor_b32 v9, v9, v10\n"
    "v_lshlrev_b32 v8, 10, v8\n"
    "v_lshl_or_b32 v8, v9, 4, v8\n"
    "v_mul_u32_u24 v6, 3072, v6\n"
    "s_mul_i32 s48, s3, 24576\n"
    "s_lshl_b32 s49, s37, 6\n"
    "s_add_u32 s48, s48, s49\n"
    "v_add3_u32 v4, v8, v6, s48\n"
    "v_add_u32 v5, 11520, v4\n"
    "s_lshl_b32 s50, s36, 2\n"
    "s_add_u32 s50, s50, s2\n"
    "s_lshl_b32 s51, s3, 11\n"
    "s_mul_i32 s52, s3, 1536\n"
    "s_add_u32 s52, s52, 81920\n"
    "v_lshlrev_b32 v22, 4, v1\n"
    "s_lshl4_add_u32 s84, s36, s37\n"
    "v_and_b32 v220, 0x200, v22\n"
    "s_lshl2_add_u32 s84, s84, s3\n"
    "v_add_u32 v220, v220, v22\n"
    "s_lshl_b32 s85, s84, 11\n"
    "s_and_b32 s38, s3, 3\n"
    "s_lshr_b32 s39, s3, 2\n"
    "s_lshl1_add_u32 s45, s37, s39\n"
    "s_lshl4_add_u32 s45, s45, s36\n"
    "s_lshl2_add_u32 s45, s45, s38\n"
    "s_lshl_b32 s89, s45, 10\n"
    "v_mov_b32 v240, -1\n"
    "v_mov_b32 v241, -1\n"
    "v_mov_b32 v242, -1\n"
    "v_mov_b32 v243, -1\n"
    "s_waitcnt lgkmcnt(0)\n"
    "s_mov_b32 s4, s64\n"
    "s_and_b32 s5, s65, 0xffff\n"
    "s_mov_b32 s6, 2097152\n"
    "s_mov_b32 s7, 0x20000\n"
    "s_mov_b32 s8, s66\n"
    "s_and_b32 s9, s67, 0xffff\n"
    "s_mov_b32 s10, 1572864\n"
    "s_mov_b32 s11, 0x20000\n"
    "s_mov_b32 s12, s78\n"
    "s_and_b32 s13, s79, 0xffff\n"
    "s_mov_b32 s14, 24117248\n"
    "s_mov_b32 s15, 0x20000\n"
    "s_mov_b32 s54, 19922944\n"
    "buffer_load_dwordx2 v[252:253], off, s[12:15], s54 sc1\n"
    "s_and_b32 s54, s50, 7\n"
    "s_add_u32 m0, s51, 0\n"
    "s_lshl_b32 s55, s54, 7\n"
    "buffer_load_dwordx4 v2, s[4:7], s55 offen lds\n"
    "buffer_load_dwordx4 v3, s[4:7], s55 offen offset:1024 lds\n"
    "s_mul_i32 s56, s54, 196608\n"
    "s_add_u32 m0, s52, 0\n"
    "s_add_u32 s50, s50, 1\n"
    "s_mov_b32 exec_hi, 0xffff\n"
    "buffer_load_dwordx4 v4, s[8:11], s56 offen lds\n"
    "s_mov_b32 exec_hi, -1\n"
    "s_mov_b32 exec_hi, 0xffff\n"
    "buffer_load_dwordx4 v5, s[8:11], s56 offen offset:768 lds\n"
    "s_mov_b32 exec_hi, -1\n"
    "s_setprio 2\n"
    "s_mov_b32 s62, 0x3f803f80\n"
    "s_mov_b32 s60, 0\n"
    "s_mov_b32 s61, -1\n"
    "s_mov_b32 s0, 0x200000\n"
    "s_mov_b32 s42, 0xffff0000\n"
    "s_xor_b32 s44, s38, s39\n"
    "v_and_b32 v20, 31, v1\n"
    "v_lshrrev_b32 v21, 5, v1\n"
    "v_bfe_u32 v24, v20, 1, 2\n"
    "v_lshrrev_b32 v25, 4, v20\n"
    "v_lshl_or_b32 v24, v25, 2, v24\n"
    "v_xor_b32 v24, v24, v21\n"
    "v_lshlrev_b32 v26, 7, v20\n"
    "v_lshl_or_b32 v24, v24, 4, v26\n"
    "s_cmp_lt_u32 s3, 4\n"
    "s_cselect_b64 exec, -1, 0\n"
    "buffer_load_dwordx4 v[212:215], v220, s[4:7], s85 offen offset:0\n"
    "buffer_load_dwordx4 v[216:219], v220, s[4:7], s85 offen offset:512\n"
    "s_mov_b64 exec, -1\n"
    "s_lshl_b32 s43, s38, 12\n"
    "s_lshl_b32 s45, s44, 5\n"
    "s_or_b32 s43, s43, s45\n"
    "v_xor_b32 v6, s43, v24\n"
    "v_xor_b32 v7, 32, v6\n"
    "v_xor_b32 v8, 64, v6\n"
    "v_xor_b32 v9, 0x60, v6\n"
    "v_add_u32 v10, 65536, v6\n"
    "v_add_u32 v11, 65536, v7\n"
    "v_add_u32 v12, 65536, v8\n"
    "v_add_u32 v13, 65536, v9\n"
    "v_bfe_u32 v26, v1, 2, 2\n"
    "v_and_b32 v27, 3, v1\n"
    "v_lshlrev_b32 v27, 3, v27\n"
    "v_xor_b32 v28, s39, v25\n"
    "s_and_b32 s54, s50, 7\n"
    "s_add_u32 m0, s51, 16384\n"
    "s_lshl_b32 s55, s54, 7\n"
    "buffer_load_dwordx4 v2, s[4:7], s55 offen lds\n"
    "v_lshl_or_b32 v27, v28, 5, v27\n"
    "v_lshl_add_u32 v28, v21, 3, v26\n"
    "v_mul_u32_u24 v28, 192, v28\n"
    "v_add_u32 v27, v27, v28\n"
    "v_lshl_add_u32 v28, v25, 6, v27\n"
    "s_xor_b32 s45, s44, 0\n"
    "s_mul_i32 s45, s45, 3072\n"
    "s_add_u32 s45, s45, 81920\n"
    "v_add_u32 v14, s45, v28\n"
    "s_add_u32 s45, s45, 128\n"
    "v_add_u32 v18, s45, v27\n"
    "s_xor_b32 s45, s44, 1\n"
    "s_mul_i32 s45, s45, 3072\n"
    "s_add_u32 s45, s45, 81920\n"
    "v_add_u32 v15, s45, v28\n"
    "buffer_load_dwordx4 v3, s[4:7], s55 offen offset:1024 lds\n"
    "s_mul_i32 s56, s54, 196608\n"
    "s_add_u32 m0, s52, 12288\n"
    "s_add_u32 s50, s50, 1\n"
    "s_xor_b32 s45, s44, 2\n"
    "s_mul_i32 s45, s45, 3072\n"
    "s_add_u32 s45, s45, 81920\n"
    "v_add_u32 v16, s45, v28\n"
    "s_add_u32 s45, s45, 128\n"
    "v_add_u32 v19, s45, v27\n"
    "s_xor_b32 s45, s44, 3\n"
    "s_mul_i32 s45, s45, 3072\n"
    "s_add_u32 s45, s45, 81920\n"
    "v_add_u32 v17, s45, v28\n"
    "v_xor_b32 v26, v26, v25\n"
    "v_and_b32 v26, 1, v26\n"
    "v_add_u32 v26, -1, v26\n"
    "v_and_b32 v104, s62, v26\n"
    "s_mov_b32 exec_hi, 0xffff\n"
    "buffer_load_dwordx4 v4, s[8:11], s56 offen lds\n"
    "s_mov_b32 exec_hi, -1\n"
    "v_mov_b32 v105, v104\n"
    "v_mov_b32 v106, v104\n"
    "v_mov_b32 v107, v104\n"
    "v_mov_b32 v116, 0\n"
    "v_add_u32 v184, s51, v22\n"
    "v_xor_b32 v185, 0x2000, v184\n"
    "s_lshl_b32 s43, s3, 8\n"
    "v_lshl_add_u32 v186, v1, 2, s43\n"
    "s_lshl_b32 s45, s39, 10\n"
    "v_add_u32 v189, s45, v22\n"
    "s_lshl_b32 s45, s38, 2\n"
    "v_add_u32 v188, s45, v189\n"
    "v_and_b32 v190, 0xfffffcff, v189\n"
    "v_lshlrev_b32 v26, 6, v25\n"
    "v_lshl_or_b32 v26, v21, 4, v26\n"
    "s_mov_b32 exec_hi, 0xffff\n"
    "buffer_load_dwordx4 v5, s[8:11], s56 offen offset:768 lds\n"
    "s_mov_b32 exec_hi, -1\n"
    "v_add_u32 v191, s43, v26\n"
    "s_lshl_b32 s43, s3, 10\n"
    "v_add_u32 v192, s43, v22\n"
    "s_lshl_b32 s43, s39, 12\n"
    "v_add_u32 v193, s43, v22\n"
    "v_xor_b32 v244, s36, v1\n"
    "v_and_b32 v244, 15, v244\n"
    "v_lshrrev_b32 v250, 4, v1\n"
    "s_lshl3_add_u32 s1, s37, s3\n"
    "v_lshlrev_b32 v250, 26, v250\n"
    "s_lshl_b32 s1, s1, 11\n"
    "v_lshl_or_b32 v244, v244, 7, v250\n"
    "s_add_u32 s1, s1, 20185088\n"
    "v_lshlrev_b32 v187, 5, v20\n"
    "s_and_b32 s86, s89, 0xffff\n"
    "s_and_b32 s54, s50, 7\n"
    "s_add_u32 m0, s51, 32768\n"
    "s_lshl_b32 s55, s54, 7\n"
    "buffer_load_dwordx4 v2, s[4:7], s55 offen lds\n"
    "v_lshl_or_b32 v187, v21, 4, v187\n"
    "s_add_u32 s86, s86, 20840448\n"
    "v_lshlrev_b32 v24, 2, v1\n"
    "v_add_u32 v24, 161792, v24\n"
    "ds_write_b32 v24, v116\n"
    "s_cmp_ge_u32 s2, 144\n"
    "s_cselect_b64 s[20:21], s[74:75], s[68:69]\n"
    "s_cselect_b32 s58, 112, 32\n"
    "s_cselect_b32 s57, 2359296, 0\n"
    "s_cselect_b32 s45, 18, 0\n"
    "s_cselect_b32 s22, 7, 2\n"
    "buffer_load_dwordx4 v3, s[4:7], s55 offen offset:1024 lds\n"
    "s_mul_i32 s56, s54, 196608\n"
    "s_add_u32 m0, s52, 24576\n"
    "s_add_u32 s50, s50, 1\n"
    "s_and_b32 s43, s2, 7\n"
    "s_lshr_b32 s44, s2, 3\n"
    "s_sub_u32 s44, s44, s45\n"
    "s_mov_b32 s59, 1024\n"
    "s_sub_u32 s46, s2, 32\n"
    "s_lshr_b32 s47, s46, 2\n"
    "s_and_b32 s45, s46, 3\n"
    "s_cmp_lt_u32 s46, 112\n"
    "s_cselect_b32 s43, s47, s43\n"
    "s_cselect_b32 s44, s45, s44\n"
    "s_cselect_b32 s59, 3584, s59\n"
    "s_cmp_lt_u32 s46, 112\n"
    "s_cselect_b64 s[20:21], s[70:71], s[20:21]\n"
    "s_cselect_b32 s57, 524288, s57\n"
    "s_cselect_b32 s22, 7, s22\n"
    "s_mov_b32 exec_hi, 0xffff\n"
    "buffer_load_dwordx4 v4, s[8:11], s56 offen lds\n"
    "s_mov_b32 exec_hi, -1\n"
    "s_and_b32 s21, s21, 0xffff\n"
    "s_mov_b32 s23, 0x20000\n"
    "s_lshl_b32 s22, s22, 18\n"
    "s_lshl_b32 s58, s58, 10\n"
    "s_lshl3_add_u32 s44, s44, s3\n"
    "s_lshl_b32 s45, s44, 4\n"
    "s_mul_i32 s45, s45, s59\n"
    "s_lshl_b32 s46, s43, 7\n"
    "s_add_u32 s63, s45, s46\n"
    "s_mul_i32 s47, s43, s58\n"
    "s_lshl_b32 s44, s44, 10\n"
    "s_lshl1_add_u32 s47, s47, s44\n"
    "s_add_u32 s57, s57, s47\n"
    "s_add_u32 s57, s57, 15728640\n"
    "v_lshrrev_b32 v100, 3, v1\n"
    "v_mul_u32_u24 v100, s59, v100\n"
    "s_lshl_b32 s45, s59, 3\n"
    "s_mov_b32 exec_hi, 0xffff\n"
    "buffer_load_dwordx4 v5, s[8:11], s56 offen offset:768 lds\n"
    "s_mov_b32 exec_hi, -1\n"
    "v_and_b32 v101, 7, v1\n"
    "s_sub_u32 s45, s45, 1024\n"
    "v_lshl_add_u32 v100, v101, 4, v100\n"
    "v_add_u32 v101, s45, v100\n"
    "s_mov_b32 s16, s80\n"
    "s_and_b32 s17, s81, 0xffff\n"
    "s_mov_b32 s18, 2097152\n"
    "s_mov_b32 s19, 0x20000\n"
    "s_mov_b32 s24, s72\n"
    "s_and_b32 s25, s73, 0xffff\n"
    "s_mov_b32 s26, 3584\n"
    "s_mov_b32 s27, 0x20000\n"
    "s_mov_b32 s28, s76\n"
    "s_and_b32 s29, s77, 0xffff\n"
    "s_mov_b32 s30, 1024\n"
    "s_mov_b32 s31, 0x20000\n"
    "s_waitcnt vmcnt(14)\n"
    "v_add_u32 v252, 1, v252\n"
    "v_readfirstlane_b32 s41, v253\n"
    "s_setprio 0\n"
    "v_readfirstlane_b32 s40, v252\n"
    "v_bfe_u32 v117, v1, 3, 2\n"
    "v_and_b32 v118, 2, v1\n"
    "v_lshlrev_b32 v117, 10, v117\n"
    "v_lshl_or_b32 v117, v118, 8, v117\n"
    "v_and_b32 v118, 1, v1\n"
    "v_lshl_or_b32 v117, v118, 2, v117\n"
    "v_and_b32 v118, 4, v1\n"
    "v_lshl_or_b32 v117, v118, 1, v117\n"
    "v_lshrrev_b32 v118, 4, v1\n"
    "v_and_b32 v119, 15, v1\n"
    "v_lshlrev_b32 v119, 4, v119\n"
    "v_lshl_or_b32 v119, v118, 9, v119\n"
    "v_lshrrev_b32 v118, 5, v1\n"
    "v_lshl_or_b32 v117, v118, 4, v117\n"
    "v_lshlrev_b32 v118, 3, v118\n"
    "s_waitcnt vmcnt(10)\n"
    "s_waitcnt lgkmcnt(0)\n"
    "s_barrier\n"
    "ds_read_b128 v[24:27], v6 offset:0\n"
    "ds_read_b64_tr_b16 v[40:41], v14 offset:0\n"
    "ds_read_b64_tr_b16 v[42:43], v14 offset:768\n"
    "ds_read_b64_tr_b16 v[56:57], v18 offset:0\n"
    "ds_read_b64_tr_b16 v[58:59], v18 offset:768\n"
    "ds_read_b128 v[28:31], v7 offset:0\n"
    "ds_read_b64_tr_b16 v[44:45], v15 offset:0\n"
    "ds_read_b64_tr_b16 v[46:47], v15 offset:768\n"
    "ds_read_b128 v[32:35], v8 offset:0\n"
    "ds_read_b64_tr_b16 v[48:49], v16 offset:0\n"
    "ds_read_b64_tr_b16 v[50:51], v16 offset:768\n"
    "ds_read_b64_tr_b16 v[60:61], v19 offset:0\n"
    "ds_read_b64_tr_b16 v[62:63], v19 offset:768\n"
    "ds_read_b128 v[36:39], v9 offset:0\n"
    "ds_read_b64_tr_b16 v[52:53], v17 offset:0\n"
    "ds_read_b64_tr_b16 v[54:55], v17 offset:768\n"
    "s_waitcnt lgkmcnt(13)\n"
    "v_mfma_f32_32x32x16_bf16 v[64:79], v[40:43], v[24:27], 0\n"
    "s_and_b32 s54, s50, 7\n"
    "s_add_u32 m0, s51, 49152\n"
    "s_lshl_b32 s55, s54, 7\n"
    "buffer_load_dwordx4 v2, s[4:7], s55 offen lds\n"
    "s_waitcnt lgkmcnt(11)\n"
    "v_mfma_f32_32x32x16_bf16 v[80:95], v[24:27], v[56:59], 0\n"
    "buffer_load_dwordx4 v3, s[4:7], s55 offen offset:1024 lds\n"
    "s_mul_i32 s56, s54, 196608\n"
    "s_add_u32 m0, s52, 36864\n"
    "s_add_u32 s50, s50, 1\n"
    "s_waitcnt lgkmcnt(8)\n"
    "v_mfma_f32_32x32x16_bf16 v[64:79], v[44:47], v[28:31], v[64:79]\n"
    "s_mov_b32 exec_hi, 0xffff\n"
    "buffer_load_dwordx4 v4, s[8:11], s56 offen lds\n"
    "s_mov_b32 exec_hi, -1\n"
    "v_mfma_f32_16x16x32_bf16 v[108:111], v[104:107], v[40:43], 0\n"
    "s_mov_b32 exec_hi, 0xffff\n"
    "buffer_load_dwordx4 v5, s[8:11], s56 offen offset:768 lds\n"
    "s_mov_b32 exec_hi, -1\n"
    "s_mov_b64 exec, 0\n"
    "s_cmp_lt_u32 s3, 4\n"
    "s_cbranch_scc0 xs_1_%=\n"
    "s_mov_b64 exec, -1\n"
    "s_waitcnt vmcnt(13)\n"
    "v_dot2_f32_bf16 v220, v212, s62, 0\n"
    "v_dot2_f32_bf16 v221, v212, v212, 0\n"
    "v_dot2_f32_bf16 v220, v213, s62, v220\n"
    "v_dot2_f32_bf16 v221, v213, v213, v221\n"
    "v_dot2_f32_bf16 v220, v214, s62, v220\n"
    "v_dot2_f32_bf16 v221, v214, v214, v221\n"
    "v_dot2_f32_bf16 v220, v215, s62, v220\n"
    "v_dot2_f32_bf16 v221, v215, v215, v221\n"
    "s_waitcnt vmcnt(12)\n"
    "v_dot2_f32_bf16 v220, v216, s62, v220\n"
    "v_dot2_f32_bf16 v221, v216, v216, v221\n"
    "v_dot2_f32_bf16 v220, v217, s62, v220\n"
    "v_dot2_f32_bf16 v221, v217, v217, v221\n"
    "v_dot2_f32_bf16 v220, v218, s62, v220\n"
    "v_dot2_f32_bf16 v221, v218, v218, v221\n"
    "v_dot2_f32_bf16 v220, v219, s62, v220\n"
    "v_dot2_f32_bf16 v221, v219, v219, v221\n"
    "s_lshl_b32 s84, s84, 6\n"
    "s_add_u32 s84, s84, 20840448\n"
    "v_mov_b32 v222, v253\n"
    "v_permlane16_swap_b32 v220, v221\n"
    "v_add_f32 v220, v220, v221\n"
    "v_mov_b32 v221, v252\n"
    "s_nop 0\n"
    "v_add_f32_dpp v220, v220, v220 row_ror:8 row_mask:0xf bank_mask:0xf\n"
    "s_nop 1\n"
    "v_add_f32_dpp v220, v220, v220 row_ror:4 row_mask:0xf bank_mask:0xf\n"
    "s_nop 1\n"
    "v_add_f32_dpp v220, v220, v220 row_ror:2 row_mask:0xf bank_mask:0xf\n"
    "s_nop 1\n"
    "v_add_f32_dpp v220, v220, v220 row_ror:1 row_mask:0xf bank_mask:0xf\n"
    "s_mov_b32 exec_lo, 0x10001\n"
    "s_mov_b32 exec_hi, 0x10001\n"
    "xs_1_%=:\n"
    "buffer_store_dwordx3 v[220:222], v1, s[12:15], s84 offen sc1\n"
    "s_mov_b64 exec, -1\n"
    "s_waitcnt vmcnt(9)\n"
    "s_waitcnt lgkmcnt(0)\n"
    "s_barrier\n"
    "v_mfma_f32_32x32x16_bf16 v[64:79], v[48:51], v[32:35], v[64:79]\n"
    "ds_read_b128 v[24:27], v6 offset:16384\n"
    "ds_read_b64_tr_b16 v[40:41], v14 offset:12288\n"
    "ds_read_b64_tr_b16 v[42:43], v14 offset:13056\n"
    "ds_read_b64_tr_b16 v[56:57], v18 offset:12288\n"
    "ds_read_b64_tr_b16 v[58:59], v18 offset:13056\n"
    "v_mfma_f32_32x32x16_bf16 v[80:95], v[32:35], v[60:63], v[80:95]\n"
    "ds_read_b128 v[28:31], v7 offset:16384\n"
    "ds_read_b64_tr_b16 v[44:45], v15 offset:12288\n"
    "ds_read_b64_tr_b16 v[46:47], v15 offset:13056\n"
    "v_mfma_f32_32x32x16_bf16 v[64:79], v[52:55], v[36:39], v[64:79]\n"
    "ds_read_b128 v[136:139], v8 offset:16384\n"
    "ds_read_b64_tr_b16 v[144:145], v16 offset:12288\n"
    "ds_read_b64_tr_b16 v[146:147], v16 offset:13056\n"
    "s_and_b32 s54, s50, 7\n"
    "s_add_u32 m0, s51, 65536\n"
    "s_lshl_b32 s55, s54, 7\n"
    "buffer_load_dwordx4 v2, s[4:7], s55 offen lds\n"
    "v_mfma_f32_16x16x32_bf16 v[112:115], v[104:107], v[60:63], 0\n"
    "ds_read_b64_tr_b16 v[152:153], v19 offset:12288\n"
    "ds_read_b64_tr_b16 v[154:155], v19 offset:13056\n"
    "s_waitcnt lgkmcnt(10)\n"
    "v_mfma_f32_32x32x16_bf16 v[64:79], v[40:43], v[24:27], v[64:79]\n"
    "ds_read_b128 v[140:143], v9 offset:16384\n"
    "ds_read_b64_tr_b16 v[148:149], v17 offset:12288\n"
    "ds_read_b64_tr_b16 v[150:151], v17 offset:13056\n"
    "buffer_load_dwordx4 v3, s[4:7], s55 offen offset:1024 lds\n"
    "s_mul_i32 s56, s54, 196608\n"
    "s_add_u32 m0, s52, 49152\n"
    "s_add_u32 s50, s50, 1\n"
    "s_waitcnt lgkmcnt(11)\n"
    "v_mfma_f32_32x32x16_bf16 v[80:95], v[24:27], v[56:59], v[80:95]\n"
    "s_mov_b32 exec_hi, 0xffff\n"
    "buffer_load_dwordx4 v4, s[8:11], s56 offen lds\n"
    "s_mov_b32 exec_hi, -1\n"
    "s_waitcnt lgkmcnt(8)\n"
    "v_mfma_f32_32x32x16_bf16 v[64:79], v[44:47], v[28:31], v[64:79]\n"
    "s_mov_b32 exec_hi, 0xffff\n"
    "buffer_load_dwordx4 v5, s[8:11], s56 offen offset:768 lds\n"
    "s_mov_b32 exec_hi, -1\n"
    "v_mfma_f32_16x16x32_bf16 v[108:111], v[104:107], v[40:43], v[108:111]\n"
    "s_waitcnt vmcnt(9)\n"
    "s_waitcnt lgkmcnt(0)\n"
    "s_barrier\n"
    "v_mfma_f32_32x32x16_bf16 v[64:79], v[144:147], v[136:139], v[64:79]\n"
    "ds_read_b128 v[24:27], v6 offset:32768\n"
    "ds_read_b64_tr_b16 v[40:41], v14 offset:24576\n"
    "ds_read_b64_tr_b16 v[42:43], v14 offset:25344\n"
    "ds_read_b64_tr_b16 v[56:57], v18 offset:24576\n"
    "ds_read_b64_tr_b16 v[58:59], v18 offset:25344\n"
    "v_mfma_f32_32x32x16_bf16 v[80:95], v[136:139], v[152:155], v[80:95]\n"
    "ds_read_b128 v[28:31], v7 offset:32768\n"
    "ds_read_b64_tr_b16 v[44:45], v15 offset:24576\n"
    "ds_read_b64_tr_b16 v[46:47], v15 offset:25344\n"
    "v_and_b32 v156, 11, v1\n"
    "v_mov_b32 v157, s62\n"
    "v_cmp_eq_u32 vcc, 0, v156\n"
    "s_nop 1\n"
    "v_cndmask_b32 v156, 0, v157, vcc\n"
    "v_mfma_f32_32x32x16_bf16 v[64:79], v[148:151], v[140:143], v[64:79]\n"
    "ds_read_b128 v[32:35], v8 offset:32768\n"
    "ds_read_b64_tr_b16 v[48:49], v16 offset:24576\n"
    "ds_read_b64_tr_b16 v[50:51], v16 offset:25344\n"
    "s_and_b32 s54, s50, 7\n"
    "s_add_u32 m0, s51, 0\n"
    "s_lshl_b32 s55, s54, 7\n"
    "buffer_load_dwordx4 v2, s[4:7], s55 offen lds\n"
    "v_mov_b32 v157, v156\n"
    "v_mfma_f32_16x16x32_bf16 v[112:115], v[104:107], v[152:155], v[112:115]\n"
    "ds_read_b64_tr_b16 v[60:61], v19 offset:24576\n"
    "ds_read_b64_tr_b16 v[62:63], v19 offset:25344\n"
    "v_mov_b32 v158, v156\n"
    "s_waitcnt lgkmcnt(10)\n"
    "v_mfma_f32_32x32x16_bf16 v[64:79], v[40:43], v[24:27], v[64:79]\n"
    "ds_read_b128 v[36:39], v9 offset:32768\n"
    "ds_read_b64_tr_b16 v[52:53], v17 offset:24576\n"
    "ds_read_b64_tr_b16 v[54:55], v17 offset:25344\n"
    "buffer_load_dwordx4 v3, s[4:7], s55 offen offset:1024 lds\n"
    "s_mul_i32 s56, s54, 196608\n"
    "s_add_u32 m0, s52, 0\n"
    "s_add_u32 s50, s50, 1\n"
    "s_waitcnt lgkmcnt(11)\n"
    "v_mfma_f32_32x32x16_bf16 v[80:95], v[24:27], v[56:59], v[80:95]\n"
    "s_mov_b32 exec_hi, 0xffff\n"
    "buffer_load_dwordx4 v4, s[8:11], s56 offen lds\n"
    "s_mov_b32 exec_hi, -1\n"
    "v_mov_b32 v159, v156\n"
    "s_waitcnt lgkmcnt(8)\n"
    "v_mfma_f32_32x32x16_bf16 v[64:79], v[44:47], v[28:31], v[64:79]\n"
    "s_mov_b32 exec_hi, 0xffff\n"
    "buffer_load_dwordx4 v5, s[8:11], s56 offen offset:768 lds\n"
    "s_mov_b32 exec_hi, -1\n"
    "v_mov_b32 v164, v156\n"
    "v_mfma_f32_16x16x32_bf16 v[108:111], v[104:107], v[40:43], v[108:111]\n"
    "s_waitcnt vmcnt(9)\n"
    "s_waitcnt lgkmcnt(0)\n"
    "s_barrier\n"
    "v_mfma_f32_32x32x16_bf16 v[64:79], v[48:51], v[32:35], v[64:79]\n"
    "ds_read_b128 v[24:27], v6 offset:49152\n"
    "ds_read_b64_tr_b16 v[40:41], v14 offset:36864\n"
    "ds_read_b64_tr_b16 v[42:43], v14 offset:37632\n"
    "ds_read_b64_tr_b16 v[56:57], v18 offset:36864\n"
    "ds_read_b64_tr_b16 v[58:59], v18 offset:37632\n"
    "buffer_store_dwordx4 v[240:243], v22, s[12:15], s89 offen sc1\n"
    "v_mfma_f32_32x32x16_bf16 v[80:95], v[32:35], v[60:63], v[80:95]\n"
    "ds_read_b128 v[28:31], v7 offset:49152\n"
    "ds_read_b64_tr_b16 v[44:45], v15 offset:36864\n"
    "ds_read_b64_tr_b16 v[46:47], v15 offset:37632\n"
    "v_mfma_f32_32x32x16_bf16 v[64:79], v[52:55], v[36:39], v[64:79]\n"
    "ds_read_b128 v[136:139], v8 offset:49152\n"
    "ds_read_b64_tr_b16 v[144:145], v16 offset:36864\n"
    "ds_read_b64_tr_b16 v[146:147], v16 offset:37632\n"
    "s_and_b32 s54, s50, 7\n"
    "s_add_u32 m0, s51, 16384\n"
    "s_lshl_b32 s55, s54, 7\n"
    "buffer_load_dwordx4 v2, s[4:7], s55 offen lds\n"
    "v_mfma_f32_16x16x32_bf16 v[112:115], v[104:107], v[60:63], v[112:115]\n"
    "ds_read_b64_tr_b16 v[152:153], v19 offset:36864\n"
    "ds_read_b64_tr_b16 v[154:155], v19 offset:37632\n"
    "s_waitcnt lgkmcnt(10)\n"
    "v_mfma_f32_32x32x16_bf16 v[64:79], v[40:43], v[24:27], v[64:79]\n"
    "ds_read_b128 v[140:143], v9 offset:49152\n"
    "ds_read_b64_tr_b16 v[148:149], v17 offset:36864\n"
    "ds_read_b64_tr_b16 v[150:151], v17 offset:37632\n"
    "buffer_load_dwordx4 v3, s[4:7], s55 offen offset:1024 lds\n"
    "s_mul_i32 s56, s54, 196608\n"
    "s_add_u32 m0, s52, 12288\n"
    "s_add_u32 s50, s50, 1\n"
    "s_add_u32 s87, s89, 2097152\n"
    "buffer_store_dwordx4 v[240:243], v22, s[12:15], s87 offen sc1\n"
    "s_waitcnt lgkmcnt(11)\n"
    "v_mfma_f32_32x32x16_bf16 v[80:95], v[24:27], v[56:59], v[80:95]\n"
    "s_mov_b32 exec_hi, 0xffff\n"
    "buffer_load_dwordx4 v4, s[8:11], s56 offen lds\n"
    "s_mov_b32 exec_hi, -1\n"
    "s_waitcnt lgkmcnt(8)\n"
    "v_mfma_f32_32x32x16_bf16 v[64:79], v[44:47], v[28:31], v[64:79]\n"
    "s_mov_b32 exec_hi, 0xffff\n"
    "buffer_load_dwordx4 v5, s[8:11], s56 offen offset:768 lds\n"
    "s_mov_b32 exec_hi, -1\n"
    "v_mfma_f32_16x16x32_bf16 v[108:111], v[104:107], v[40:43], v[108:111]\n"
    "s_waitcnt vmcnt(10)\n"
    "s_waitcnt lgkmcnt(0)\n"
    "s_barrier\n"
    "v_mfma_f32_32x32x16_bf16 v[64:79], v[144:147], v[136:139], v[64:79]\n"
    "ds_read_b128 v[24:27], v10\n"
    "ds_read_b64_tr_b16 v[40:41], v14 offset:49152\n"
    "ds_read_b64_tr_b16 v[42:43], v14 offset:49920\n"
    "ds_read_b64_tr_b16 v[56:57], v18 offset:49152\n"
    "ds_read_b64_tr_b16 v[58:59], v18 offset:49920\n"
    "v_mfma_f32_32x32x16_bf16 v[80:95], v[136:139], v[152:155], v[80:95]\n"
    "ds_read_b128 v[28:31], v11\n"
    "ds_read_b64_tr_b16 v[44:45], v15 offset:49152\n"
    "ds_read_b64_tr_b16 v[46:47], v15 offset:49920\n"
    "v_mov_b32 v165, v156\n"
    "v_mfma_f32_32x32x16_bf16 v[64:79], v[148:151], v[140:143], v[64:79]\n"
    "ds_read_b128 v[32:35], v12\n"
    "ds_read_b64_tr_b16 v[48:49], v16 offset:49152\n"
    "ds_read_b64_tr_b16 v[50:51], v16 offset:49920\n"
    "s_and_b32 s54, s50, 7\n"
    "s_add_u32 m0, s51, 32768\n"
    "s_lshl_b32 s55, s54, 7\n"
    "buffer_load_dwordx4 v2, s[4:7], s55 offen lds\n"
    "v_mov_b32 v166, v156\n"
    "v_mfma_f32_16x16x32_bf16 v[112:115], v[104:107], v[152:155], v[112:115]\n"
    "ds_read_b64_tr_b16 v[60:61], v19 offset:49152\n"
    "ds_read_b64_tr_b16 v[62:63], v19 offset:49920\n"
    "v_mov_b32 v167, v156\n"
    "s_waitcnt lgkmcnt(10)\n"
    "v_mfma_f32_32x32x16_bf16 v[64:79], v[40:43], v[24:27], v[64:79]\n"
    "ds_read_b128 v[36:39], v13\n"
    "ds_read_b64_tr_b16 v[52:53], v17 offset:49152\n"
    "ds_read_b64_tr_b16 v[54:55], v17 offset:49920\n"
    "buffer_load_dwordx4 v3, s[4:7], s55 offen offset:1024 lds\n"
    "s_mul_i32 s56, s54, 196608\n"
    "s_add_u32 m0, s52, 24576\n"
    "s_add_u32 s50, s50, 1\n"
    "s_waitcnt lgkmcnt(11)\n"
    "v_mfma_f32_32x32x16_bf16 v[80:95], v[24:27], v[56:59], v[80:95]\n"
    "s_mov_b32 exec_hi, 0xffff\n"
    "buffer_load_dwordx4 v4, s[8:11], s56 offen lds\n"
    "s_mov_b32 exec_hi, -1\n"
    "v_mov_b32 v168, v156\n"
    "s_waitcnt lgkmcnt(8)\n"
    "v_mfma_f32_32x32x16_bf16 v[64:79], v[44:47], v[28:31], v[64:79]\n"
    "s_mov_b32 exec_hi, 0xffff\n"
    "buffer_load_dwordx4 v5, s[8:11], s56 offen offset:768 lds\n"
    "s_mov_b32 exec_hi, -1\n"
    "v_mov_b32 v169, v156\n"
    "v_mfma_f32_16x16x32_bf16 v[108:111], v[104:107], v[40:43], v[108:111]\n"
    "s_waitcnt vmcnt(10)\n"
    "s_waitcnt lgkmcnt(0)\n"
    "s_barrier\n"
    "v_mfma_f32_32x32x16_bf16 v[64:79], v[48:51], v[32:35], v[64:79]\n"
    "buffer_load_dwordx3 v[204:206], v187, s[12:15], s86 offen sc1\n"
    "ds_read_b128 v[24:27], v6 offset:0\n"
    "ds_read_b64_tr_b16 v[40:41], v14 offset:0\n"
    "ds_read_b64_tr_b16 v[42:43], v14 offset:768\n"
    "ds_read_b64_tr_b16 v[56:57], v18 offset:0\n"
    "ds_read_b64_tr_b16 v[58:59], v18 offset:768\n"
    "v_mfma_f32_32x32x16_bf16 v[80:95], v[32:35], v[60:63], v[80:95]\n"
    "ds_read_b128 v[28:31], v7 offset:0\n"
    "ds_read_b64_tr_b16 v[44:45], v15 offset:0\n"
    "ds_read_b64_tr_b16 v[46:47], v15 offset:768\n"
    "s_waitcnt vmcnt(7)\n"
    "s_mov_b64 exec, 1\n"
    "buffer_store_dwordx2 v[252:253], v244, s[12:15], s1 offen offset:8 sc1\n"
    "s_mov_b64 exec, -1\n"
    "v_mov_b32 v170, v156\n"
    "v_mfma_f32_32x32x16_bf16 v[64:79], v[52:55], v[36:39], v[64:79]\n"
    "ds_read_b128 v[136:139], v8 offset:0\n"
    "ds_read_b64_tr_b16 v[144:145], v16 offset:0\n"
    "ds_read_b64_tr_b16 v[146:147], v16 offset:768\n"
    "s_add_u32 m0, s51, 143360\n"
    "s_nop 0\n"
    "buffer_load_dwordx4 v100, s[20:23], s63 offen offset:0 lds\n"
    "v_mov_b32 v171, v156\n"
    "v_mfma_f32_16x16x32_bf16 v[112:115], v[104:107], v[60:63], v[112:115]\n"
    "ds_read_b64_tr_b16 v[152:153], v19 offset:0\n"
    "ds_read_b64_tr_b16 v[154:155], v19 offset:768\n"
    "v_mov_b32 v176, v156\n"
    "s_waitcnt lgkmcnt(10)\n"
    "v_mfma_f32_32x32x16_bf16 v[64:79], v[40:43], v[24:27], v[64:79]\n"
    "ds_read_b128 v[140:143], v9 offset:0\n"
    "ds_read_b64_tr_b16 v[148:149], v17 offset:0\n"
    "ds_read_b64_tr_b16 v[150:151], v17 offset:768\n"
    "s_waitcnt lgkmcnt(11)\n"
    "v_mfma_f32_32x32x16_bf16 v[80:95], v[24:27], v[56:59], v[80:95]\n"
    "s_add_u32 m0, s51, 143360\n"
    "s_nop 0\n"
    "buffer_load_dwordx4 v101, s[20:23], s63 offen offset:1024 lds\n"
    "v_mov_b32 v177, v156\n"
    "s_waitcnt lgkmcnt(8)\n"
    "v_mfma_f32_32x32x16_bf16 v[64:79], v[44:47], v[28:31], v[64:79]\n"
    "v_mov_b32 v178, v156\n"
    "v_mfma_f32_16x16x32_bf16 v[108:111], v[104:107], v[40:43], v[108:111]\n"
    "s_waitcnt vmcnt(8)\n"
    "s_waitcnt lgkmcnt(0)\n"
    "s_barrier\n"
    "v_mfma_f32_32x32x16_bf16 v[64:79], v[144:147], v[136:139], v[64:79]\n"
    "ds_read_b128 v[24:27], v6 offset:16384\n"
    "ds_read_b64_tr_b16 v[40:41], v14 offset:12288\n"
    "ds_read_b64_tr_b16 v[42:43], v14 offset:13056\n"
    "ds_read_b64_tr_b16 v[56:57], v18 offset:12288\n"
    "ds_read_b64_tr_b16 v[58:59], v18 offset:13056\n"
    "v_mfma_f32_32x32x16_bf16 v[80:95], v[136:139], v[152:155], v[80:95]\n"
    "ds_read_b128 v[28:31], v7 offset:16384\n"
    "ds_read_b64_tr_b16 v[44:45], v15 offset:12288\n"
    "ds_read_b64_tr_b16 v[46:47], v15 offset:13056\n"
    "s_waitcnt vmcnt(3)\n"
    "v_cmp_ne_u32 vcc, s40, v205\n"
    "v_cmp_ne_u32 s[44:45], s41, v206\n"
    "s_or_b64 vcc, vcc, s[44:45]\n"
    "s_cbranch_scc1 xp_2_%=\n"
    "xb_3_%=:\n"
    "v_mov_b32 v205, v204\n"
    "v_mov_b32 v198, 0\n"
    "v_mov_b32 v199, 0\n"
    "v_mov_b32 v179, v156\n"
    "v_mfma_f32_32x32x16_bf16 v[64:79], v[148:151], v[140:143], v[64:79]\n"
    "ds_read_b128 v[32:35], v8 offset:16384\n"
    "ds_read_b64_tr_b16 v[48:49], v16 offset:12288\n"
    "ds_read_b64_tr_b16 v[50:51], v16 offset:13056\n"
    "v_permlane32_swap_b32 v204, v205\n"
    "v_mul_f32 v208, 0xbb000000, v204\n"
    "v_mul_f32 v205, 0x3b000000, v205\n"
    "v_fma_f32 v205, -v208, v208, v205\n"
    "v_mov_b32 v180, v156\n"
    "v_mfma_f32_16x16x32_bf16 v[112:115], v[104:107], v[152:155], v[112:115]\n"
    "ds_read_b64_tr_b16 v[60:61], v19 offset:12288\n"
    "ds_read_b64_tr_b16 v[62:63], v19 offset:13056\n"
    "v_max_f32 v205, 0, v205\n"
    "v_add_f32 v205, 0x3727c5ac, v205\n"
    "v_rsq_f32 v206, v205\n"
    "v_mov_b32 v202, 0\n"
    "v_mov_b32 v181, v156\n"
    "s_waitcnt lgkmcnt(10)\n"
    "v_mfma_f32_32x32x16_bf16 v[64:79], v[40:43], v[24:27], v[64:79]\n"
    "ds_read_b128 v[36:39], v9 offset:16384\n"
    "ds_read_b64_tr_b16 v[52:53], v17 offset:12288\n"
    "ds_read_b64_tr_b16 v[54:55], v17 offset:13056\n"
    "v_mul_f32 v204, v208, v206\n"
    "v_cndmask_b32 v204, v206, v204, s[60:61]\n"
    "ds_write_b32 v186, v204 offset:12288\n"
    "v_cndmask_b32 v208, v208, 0, s[60:61]\n"
    "s_waitcnt lgkmcnt(12)\n"
    "v_mfma_f32_32x32x16_bf16 v[80:95], v[24:27], v[56:59], v[80:95]\n"
    "ds_read_b128 v[212:215], v191 offset:12288\n"
    "ds_read_b128 v[216:219], v191 offset:12320\n"
    "ds_read_b128 v[220:223], v191 offset:12416\n"
    "ds_read_b128 v[224:227], v191 offset:12448\n"
    "v_mov_b32 v182, v156\n"
    "s_waitcnt lgkmcnt(13)\n"
    "v_mfma_f32_32x32x16_bf16 v[64:79], v[44:47], v[28:31], v[64:79]\n"
    "v_cvt_pk_bf16_f32 v200, v208, v208\n"
    "v_mov_b32 v203, 0\n"
    "v_and_b32 v201, s42, v200\n"
    "v_mul_f32 v210, 0x3eb8aa3b, v206\n"
    "v_mov_b32 v183, v156\n"
    "v_mfma_f32_16x16x32_bf16 v[108:111], v[104:107], v[40:43], v[108:111]\n"
    "v_sub_f32 v201, v208, v201\n"
    "v_cvt_pk_bf16_f32 v200, v208, v201\n"
    "v_mov_b32 v201, v200\n"
    "s_waitcnt lgkmcnt(0)\n"
    "s_barrier\n"
    "v_mfma_f32_32x32x16_bf16 v[64:79], v[48:51], v[32:35], v[64:79]\n"
    "ds_read_b128 v[24:27], v6 offset:32768\n"
    "ds_read_b64_tr_b16 v[40:41], v14 offset:24576\n"
    "ds_read_b64_tr_b16 v[42:43], v14 offset:25344\n"
    "ds_read_b64_tr_b16 v[56:57], v18 offset:24576\n"
    "ds_read_b64_tr_b16 v[58:59], v18 offset:25344\n"
    "v_mfma_f32_32x32x16_bf16 v[80:95], v[32:35], v[60:63], v[80:95]\n"
    "ds_read_b128 v[136:139], v8 offset:32768\n"
    "ds_read_b64_tr_b16 v[144:145], v16 offset:24576\n"
    "ds_read_b64_tr_b16 v[146:147], v16 offset:25344\n"
    "ds_read_b64_tr_b16 v[152:153], v19 offset:24576\n"
    "ds_read_b64_tr_b16 v[154:155], v19 offset:25344\n"
    "buffer_load_dwordx2 v[250:251], v244, s[12:15], s1 offen offset:8 sc1\n"
    "v_mfma_f32_32x32x16_bf16 v[64:79], v[52:55], v[36:39], v[64:79]\n"
    "ds_read_b128 v[140:143], v9 offset:32768\n"
    "ds_read_b64_tr_b16 v[148:149], v17 offset:24576\n"
    "ds_read_b64_tr_b16 v[150:151], v17 offset:25344\n"
    "v_mfma_f32_16x16x32_bf16 v[112:115], v[104:107], v[60:63], v[112:115]\n"
    "ds_read_b128 v[28:31], v7 offset:32768\n"
    "ds_read_b64_tr_b16 v[44:45], v15 offset:24576\n"
    "ds_read_b64_tr_b16 v[46:47], v15 offset:25344\n"
    "v_mov_b32 v2, v22\n"
    "s_waitcnt lgkmcnt(11)\n"
    "v_mfma_f32_32x32x16_bf16 v[80:95], v[24:27], v[56:59], v[80:95]\n"
    "v_mfma_f32_16x16x32_bf16 v[108:111], v[104:107], v[40:43], v[108:111]\n"
    "v_mov_b32 v8, v156\n"
    "v_mov_b32 v9, v156\n"
    "v_mov_b32 v10, v156\n"
    "v_mov_b32 v11, v156\n"
    "s_waitcnt lgkmcnt(6)\n"
    "v_mfma_f32_32x32x16_bf16 v[80:95], v[136:139], v[152:155], v[80:95]\n"
    "v_mov_b32 v12, v156\n"
    "v_mov_b32 v13, v156\n"
    "v_mov_b32 v14, v156\n"
    "v_mov_b32 v15, v156\n"
    "v_mfma_f32_16x16x32_bf16 v[112:115], v[104:107], v[152:155], v[112:115]\n"
    "v_mfma_f32_32x32x16_bf16 v[64:79], v[144:147], v[136:139], v[64:79]\n"
    "v_mov_b32 v152, v156\n"
    "v_mov_b32 v153, v156\n"
    "v_mov_b32 v154, v156\n"
    "v_mov_b32 v155, v156\n"
    "s_nop 1\n"
    "v_permlane16_swap_b32 v80, v88\n"
    "v_permlane16_swap_b32 v81, v89\n"
    "v_permlane16_swap_b32 v82, v90\n"
    "v_permlane16_swap_b32 v83, v91\n"
    "v_permlane16_swap_b32 v84, v92\n"
    "v_permlane16_swap_b32 v85, v93\n"
    "v_permlane16_swap_b32 v86, v94\n"
    "v_permlane16_swap_b32 v87, v95\n"
    "ds_write_b128 v184, v[88:91] offset:49152\n"
    "ds_write_b128 v184, v[92:95] offset:50176\n"
    "v_cndmask_b32 v108, v108, v112, s[60:61]\n"
    "ds_write_b32 v188, v108 offset:8192\n"
    "s_waitcnt lgkmcnt(6)\n"
    "v_mfma_f32_32x32x16_bf16 v[64:79], v[148:151], v[140:143], v[64:79]\n"
    "v_mov_b32 v144, v156\n"
    "v_mov_b32 v145, v156\n"
    "v_mov_b32 v146, v156\n"
    "v_mov_b32 v147, v156\n"
    "s_waitcnt lgkmcnt(0)\n"
    "s_barrier\n"
    "ds_read_b128 v[108:111], v189 offset:8192\n"
    "ds_read_b128 v[112:115], v190 offset:8704\n"
    "ds_read_b128 v[88:91], v185 offset:49152\n"
    "ds_read_b128 v[92:95], v185 offset:50176\n"
    "v_mfma_f32_32x32x16_bf16 v[64:79], v[40:43], v[24:27], v[64:79]\n"
    "v_mfma_f32_32x32x16_bf16 v[64:79], v[44:47], v[28:31], v[64:79]\n"
    "s_add_u32 s47, s89, 2097152\n"
    "s_waitcnt lgkmcnt(3)\n"
    "v_add_f32 v108, v108, v109\n"
    "v_add_f32 v110, v110, v111\n"
    "v_add_f32 v108, v108, v110\n"
    "v_cvt_pk_bf16_f32 v196, v108, v108\n"
    "s_waitcnt lgkmcnt(2)\n"
    "v_add_f32 v112, v112, v113\n"
    "v_and_b32 v197, s42, v196\n"
    "v_add_f32 v114, v114, v115\n"
    "v_sub_f32 v197, v108, v197\n"
    "v_add_f32 v114, v112, v114\n"
    "v_cvt_pk_bf16_f32 v197, v197, v197\n"
    "s_waitcnt lgkmcnt(1)\n"
    "v_add_f32 v80, v80, v88\n"
    "v_add_f32 v81, v81, v89\n"
    "v_mfma_f32_32x32x16_bf16 v[64:79], v[196:199], v[200:203], v[64:79]\n"
    "v_add_f32 v82, v82, v90\n"
    "v_add_f32 v83, v83, v91\n"
    "s_waitcnt lgkmcnt(0)\n"
    "v_add_f32 v84, v84, v92\n"
    "v_add_f32 v85, v85, v93\n"
    "v_add_f32 v86, v86, v94\n"
    "v_add_f32 v87, v87, v95\n"
    "v_mov_b32 v112, 0xdddd\n"
    "v_pk_mul_f32 v[220:221], v[220:221], v[114:115] op_sel_hi:[1,0]\n"
    "v_pk_mul_f32 v[222:223], v[222:223], v[114:115] op_sel_hi:[1,0]\n"
    "v_pk_mul_f32 v[224:225], v[224:225], v[114:115] op_sel_hi:[1,0]\n"
    "v_pk_mul_f32 v[226:227], v[226:227], v[114:115] op_sel_hi:[1,0]\n"
    "v_pk_fma_f32 v[220:221], v[80:81], v[212:213], v[220:221]\n"
    "v_pk_fma_f32 v[222:223], v[82:83], v[214:215], v[222:223]\n"
    "v_pk_fma_f32 v[224:225], v[84:85], v[216:217], v[224:225]\n"
    "v_pk_fma_f32 v[226:227], v[86:87], v[218:219], v[226:227]\n"
    "v_cvt_pk_bf16_f32 v140, v220, v221\n"
    "v_cvt_pk_bf16_f32 v141, v222, v223\n"
    "v_cvt_pk_bf16_f32 v142, v224, v225\n"
    "v_cvt_pk_bf16_f32 v143, v226, v227\n"
    "v_min_u32 v140, -2, v140\n"
    "v_min_u32 v141, -2, v141\n"
    "v_min_u32 v142, -2, v142\n"
    "v_min_u32 v143, -2, v143\n"
    "s_nop 0\n"
    "v_permlane32_swap_b32 v140, v142\n"
    "v_permlane32_swap_b32 v141, v143\n"
    "buffer_store_dwordx4 v[140:143], v2, s[12:15], s47 offen sc1\n"
    "v_pk_mul_f32 v[72:73], v[72:73], v[206:207] op_sel_hi:[1,0]\n"
    "v_pk_mul_f32 v[74:75], v[74:75], v[206:207] op_sel_hi:[1,0]\n"
    "v_pk_mul_f32 v[76:77], v[76:77], v[206:207] op_sel_hi:[1,0]\n"
    "v_pk_mul_f32 v[78:79], v[78:79], v[206:207] op_sel_hi:[1,0]\n"
    "v_cvt_pk_bf16_f32 v136, v72, v73\n"
    "v_cvt_pk_bf16_f32 v137, v74, v75\n"
    "v_cvt_pk_bf16_f32 v138, v76, v77\n"
    "v_cvt_pk_bf16_f32 v139, v78, v79\n"
    "v_min_u32 v136, -2, v136\n"
    "v_min_u32 v137, -2, v137\n"
    "v_min_u32 v138, -2, v138\n"
    "v_min_u32 v139, -2, v139\n"
    "buffer_store_dwordx4 v[136:139], v2, s[12:15], s89 offen sc1\n"
    "v_pk_mul_f32 v[64:65], v[64:65], v[210:211] op_sel_hi:[1,0]\n"
    "v_pk_mul_f32 v[66:67], v[66:67], v[210:211] op_sel_hi:[1,0]\n"
    "v_pk_mul_f32 v[68:69], v[68:69], v[210:211] op_sel_hi:[1,0]\n"
    "v_pk_mul_f32 v[70:71], v[70:71], v[210:211] op_sel_hi:[1,0]\n"
    "v_cvt_pk_bf16_f32 v44, v64, v65\n"
    "v_cvt_pk_bf16_f32 v45, v66, v67\n"
    "v_cvt_pk_bf16_f32 v46, v68, v69\n"
    "v_cvt_pk_bf16_f32 v47, v70, v71\n"
    "ds_write_b128 v192, v[44:47]\n"
    "v_mfma_f32_32x32x8_f16 v[16:31], v[198:199], v[198:199], 0\n"
    "s_waitcnt lgkmcnt(0)\n"
    "s_barrier\n"
    "ds_read_b128 v[120:123], v193 offset:0\n"
    "ds_read_b128 v[124:127], v193 offset:1024\n"
    "ds_read_b128 v[128:131], v193 offset:2048\n"
    "ds_read_b128 v[132:135], v193 offset:3072\n"
    "v_permlane16_swap_b32 v140, v144\n"
    "v_permlane16_swap_b32 v141, v145\n"
    "v_permlane16_swap_b32 v142, v146\n"
    "v_permlane16_swap_b32 v143, v147\n"
    "s_setprio 3\n"
    "s_waitcnt lgkmcnt(3)\n"
    "v_mfma_f32_32x32x16_bf16 v[80:95], v[136:139], v[120:123], 0\n"
    "s_mov_b32 s46, 0xffff\n"
    "s_mov_b32 s47, 0xffff\n"
    "v_add_u32 v248, 2097152, v2\n"
    "s_nop 8\n"
    "v_exp_f32 v80, v80\n"
    "v_exp_f32 v81, v81\n"
    "s_waitcnt vmcnt(2)\n"
    "v_cmp_eq_u64 s[48:49], s[40:41], v[250:251]\n"
    "s_andn2_b32 s54, 62, s48\n"
    "s_cbranch_scc1 kvp_14_%=\n"
    "kvb_15_%=:\n"
    "v_mfma_f32_32x32x8_f16 v[32:47], v[198:199], v[198:199], 0\n"
    "v_exp_f32 v82, v82\n"
    "v_exp_f32 v83, v83\n"
    "s_setprio 2\n"
    "v_exp_f32 v84, v84\n"
    "s_waitcnt lgkmcnt(2)\n"
    "v_mfma_f32_32x32x16_bf16 v[96:111], v[136:139], v[124:127], 0\n"
    "v_exp_f32 v85, v85\n"
    "v_exp_f32 v86, v86\n"
    "v_exp_f32 v87, v87\n"
    "v_exp_f32 v88, v88\n"
    "v_exp_f32 v89, v89\n"
    "v_smfmac_f32_32x32x32_bf16 v[16:31], v[140:143], v[80:87], v112\n"
    "v_exp_f32 v90, v90\n"
    "v_exp_f32 v91, v91\n"
    "v_exp_f32 v92, v92\n"
    "v_exp_f32 v93, v93\n"
    "v_exp_f32 v94, v94\n"
    "v_exp_f32 v95, v95\n"
    "v_exp_f32 v96, v96\n"
    "v_exp_f32 v97, v97\n"
    "v_smfmac_f32_32x32x32_bf16 v[16:31], v[144:147], v[88:95], v112\n"
    "v_exp_f32 v98, v98\n"
    "v_exp_f32 v99, v99\n"
    "s_setprio 1\n"
    "v_exp_f32 v100, v100\n"
    "s_waitcnt lgkmcnt(1)\n"
    "v_mfma_f32_32x32x16_bf16 v[80:95], v[136:139], v[128:131], 0\n"
    "v_exp_f32 v101, v101\n"
    "s_xor_b32 s53, s89, 4096\n"
    "buffer_load_dwordx4 v[148:151], v2, s[12:15], s53 offen sc1\n"
    "v_exp_f32 v102, v102\n"
    "v_exp_f32 v103, v103\n"
    "v_exp_f32 v104, v104\n"
    "v_exp_f32 v105, v105\n"
    "v_smfmac_f32_32x32x32_bf16 v[32:47], v[140:143], v[96:103], v112\n"
    "v_exp_f32 v106, v106\n"
    "v_exp_f32 v107, v107\n"
    "s_xor_b32 s53, s89, 4096\n"
    "s_mov_b64 exec, s[46:47]\n"
    "buffer_load_dwordx4 v[152:155], v248, s[12:15], s53 offen offset:0 sc1\n"
    "s_mov_b64 exec, -1\n"
    "v_exp_f32 v108, v108\n"
    "v_exp_f32 v109, v109\n"
    "v_mfma_f32_32x32x8_f16 v[48:63], v[198:199], v[198:199], 0\n"
    "v_exp_f32 v110, v110\n"
    "v_exp_f32 v111, v111\n"
    "v_exp_f32 v80, v80\n"
    "v_exp_f32 v81, v81\n"
    "s_xor_b32 s53, s89, 4096\n"
    "s_mov_b64 exec, s[46:47]\n"
    "buffer_load_dwordx4 v[156:159], v248, s[12:15], s53 offen offset:256 sc1\n"
    "s_mov_b64 exec, -1\n"
    "v_smfmac_f32_32x32x32_bf16 v[32:47], v[144:147], v[104:111], v112\n"
    "s_waitcnt vmcnt(3)\n"
    "s_mov_b64 exec, 1\n"
    "buffer_store_dwordx2 v[252:253], v244, s[12:15], s1 offen sc1\n"
    "s_mov_b64 exec, -1\n"
    "v_exp_f32 v82, v82\n"
    "v_exp_f32 v83, v83\n"
    "v_exp_f32 v84, v84\n"
    "s_waitcnt lgkmcnt(0)\n"
    "v_mfma_f32_32x32x16_bf16 v[96:111], v[136:139], v[132:135], 0\n"
    "v_exp_f32 v85, v85\n"
    "v_exp_f32 v86, v86\n"
    "v_exp_f32 v87, v87\n"
    "v_exp_f32 v88, v88\n"
    "v_exp_f32 v89, v89\n"
    "v_smfmac_f32_32x32x32_bf16 v[48:63], v[140:143], v[80:87], v112\n"
    "v_exp_f32 v90, v90\n"
    "s_setprio 0\n"
    "v_exp_f32 v91, v91\n"
    "s_xor_b32 s53, s89, 8192\n"
    "buffer_load_dwordx4 v[4:7], v2, s[12:15], s53 offen sc1\n"
    "v_exp_f32 v92, v92\n"
    "v_exp_f32 v93, v93\n"
    "v_mfma_f32_32x32x8_f16 v[64:79], v[198:199], v[198:199], 0\n"
    "v_exp_f32 v94, v94\n"
    "v_exp_f32 v95, v95\n"
    "v_exp_f32 v96, v96\n"
    "s_lshl_b32 s54, s39, 8\n"
    "v_xor_b32 v249, s54, v2\n"
    "s_mov_b32 s64, s36\n"
    "s_mov_b32 s65, s37\n"
    "v_exp_f32 v97, v97\n"
    "s_xor_b32 s53, s89, 8192\n"
    "s_mov_b64 exec, s[46:47]\n"
    "buffer_load_dwordx4 v[8:11], v248, s[12:15], s53 offen offset:0 sc1\n"
    "s_mov_b64 exec, -1\n"
    "v_smfmac_f32_32x32x32_bf16 v[48:63], v[144:147], v[88:95], v112\n"
    "s_lshl2_add_u32 s54, s65, s38\n"
    "s_lshl1_add_u32 s54, s54, s39\n"
    "s_lshl_b32 s54, s54, 14\n"
    "s_add_u32 s66, s54, 16252928\n"
    "v_exp_f32 v98, v98\n"
    "v_exp_f32 v99, v99\n"
    "s_and_b32 s54, s65, 14\n"
    "s_mul_i32 s54, s54, 112\n"
    "s_mul_i32 s55, s3, 14\n"
    "s_add_u32 s54, s54, s55\n"
    "s_lshl_b32 s54, s54, 10\n"
    "s_add_u32 s67, s54, 18087936\n"
    "s_setprio 3\n"
    "v_exp_f32 v100, v100\n"
    "s_lshl_b32 s54, s36, 7\n"
    "s_lshl1_add_u32 s55, s37, s39\n"
    "s_add_u32 s54, s54, s55\n"
    "s_lshl_b32 s54, s54, 10\n"
    "s_add_u32 s68, s54, 4194304\n"
    "v_exp_f32 v101, v101\n"
    "s_xor_b32 s53, s89, 8192\n"
    "s_mov_b64 exec, s[46:47]\n"
    "buffer_load_dwordx4 v[12:15], v248, s[12:15], s53 offen offset:256 sc1\n"
    "s_mov_b64 exec, -1\n"
    "v_exp_f32 v102, v102\n"
    "s_lshl_b32 s54, s64, 7\n"
    "s_lshl1_add_u32 s54, s65, s54\n"
    "s_lshl_b32 s54, s54, 10\n"
    "s_add_u32 s69, s54, 6291456\n"
    "v_exp_f32 v103, v103\n"
    "v_exp_f32 v104, v104\n"
    "s_mul_i32 s54, s64, 448\n"
    "s_lshl3_add_u32 s54, s65, s54\n"
    "s_lshl_b32 s54, s54, 10\n"
    "s_add_u32 s71, s54, 8388608\n"
    "v_exp_f32 v105, v105\n"
    "v_smfmac_f32_32x32x32_bf16 v[64:79], v[140:143], v[96:103], v112\n"
    "s_cmp_lt_u32 s65, 14\n"
    "s_cselect_b32 s71, s71, 20447232\n"
    "s_cselect_b32 s66, s66, 0x40000000\n"
    "v_exp_f32 v106, v106\n"
    "v_exp_f32 v107, v107\n"
    "v_exp_f32 v108, v108\n"
    "v_exp_f32 v109, v109\n"
    "v_exp_f32 v110, v110\n"
    "v_exp_f32 v111, v111\n"
    "s_waitcnt vmcnt(6)\n"
    "v_max3_u32 v0, v148, v149, v150\n"
    "s_waitcnt vmcnt(5)\n"
    "v_max3_u32 v0, v151, v152, v0\n"
    "v_max3_u32 v0, v153, v154, v0\n"
    "s_waitcnt vmcnt(4)\n"
    "v_max3_u32 v0, v155, v156, v0\n"
    "v_max3_u32 v0, v157, v158, v0\n"
    "v_max_u32 v0, v159, v0\n"
    "v_cmp_eq_u32 vcc, -1, v0\n"
    "s_cbranch_vccnz kp_4_%=\n"
    "kb_5_%=:\n"
    "v_mfma_f32_32x32x16_bf16 v[80:95], v[148:151], v[120:123], 0\n"
    "s_nop 11\n"
    "v_exp_f32 v80, v80\n"
    "v_lshrrev_b32 v192, 5, v1\n"
    "v_exp_f32 v81, v81\n"
    "v_bfe_u32 v193, v1, 2, 2\n"
    "v_lshrrev_b32 v194, 2, v1\n"
    "v_and_b32 v195, 0x30, v2\n"
    "v_lshl_or_b32 v194, v194, 10, v195\n"
    "s_lshl3_add_u32 s54, s64, s3\n"
    "s_lshl_b32 s54, s54, 14\n"
    "s_lshl_b32 s55, s65, 6\n"
    "s_add_u32 s54, s54, s55\n"
    "s_lshl_b32 s55, s3, 10\n"
    "s_add_u32 m0, s55, 71680\n"
    "s_nop 0\n"
    "buffer_load_dwordx4 v194, s[4:7], s54 offen lds\n"
    "v_smfmac_f32_32x32x32_bf16 v[64:79], v[144:147], v[104:111], v112\n"
    "v_lshl_add_u32 v192, v192, 2, v193\n"
    "v_exp_f32 v82, v82\n"
    "v_exp_f32 v83, v83\n"
    "v_and_b32 v193, 3, v1\n"
    "s_setprio 2\n"
    "v_exp_f32 v84, v84\n"
    "v_lshlrev_b32 v193, 3, v193\n"
    "v_mfma_f32_32x32x16_bf16 v[96:111], v[148:151], v[124:127], 0\n"
    "v_exp_f32 v85, v85\n"
    "v_lshl_or_b32 v192, v192, 7, v193\n"
    "v_exp_f32 v86, v86\n"
    "v_and_b32 v193, 16, v1\n"
    "v_exp_f32 v87, v87\n"
    "s_add_u32 s54, s51, 143360\n"
    "v_exp_f32 v88, v88\n"
    "v_exp_f32 v89, v89\n"
    "v_lshl_add_u32 v193, v193, 1, s54\n"
    "v_smfmac_f32_32x32x32_bf16 v[16:31], v[152:155], v[80:87], v112\n"
    "v_add_u32 v192, v192, v193\n"
    "v_exp_f32 v90, v90\n"
    "v_exp_f32 v91, v91\n"
    "v_exp_f32 v92, v92\n"
    "v_exp_f32 v93, v93\n"
    "ds_read_b64_tr_b16 v[184:185], v192 offset:0\n"
    "ds_read_b64_tr_b16 v[186:187], v192 offset:1024\n"
    "ds_read_b64_tr_b16 v[188:189], v192 offset:64\n"
    "ds_read_b64_tr_b16 v[190:191], v192 offset:1088\n"
    "v_exp_f32 v94, v94\n"
    "v_exp_f32 v95, v95\n"
    "v_exp_f32 v96, v96\n"
    "v_exp_f32 v97, v97\n"
    "s_xor_b32 s53, s89, 12288\n"
    "buffer_load_dwordx4 v[160:163], v2, s[12:15], s53 offen sc1\n"
    "v_smfmac_f32_32x32x32_bf16 v[16:31], v[156:159], v[88:95], v112\n"
    "v_mov_b32 v196, -1\n"
    "v_exp_f32 v98, v98\n"
    "v_exp_f32 v99, v99\n"
    "v_mov_b32 v197, -1\n"
    "s_setprio 1\n"
    "v_exp_f32 v100, v100\n"
    "v_mov_b32 v198, -1\n"
    "v_mfma_f32_32x32x16_bf16 v[80:95], v[148:151], v[128:131], 0\n"
    "v_exp_f32 v101, v101\n"
    "v_mov_b32 v199, -1\n"
    "s_xor_b32 s53, s89, 12288\n"
    "s_mov_b64 exec, s[46:47]\n"
    "buffer_load_dwordx4 v[164:167], v248, s[12:15], s53 offen offset:0 sc1\n"
    "s_mov_b64 exec, -1\n"
    "v_exp_f32 v102, v102\n"
    "v_exp_f32 v103, v103\n"
    "v_exp_f32 v104, v104\n"
    "v_exp_f32 v105, v105\n"
    "v_smfmac_f32_32x32x32_bf16 v[32:47], v[152:155], v[96:103], v112\n"
    "v_exp_f32 v106, v106\n"
    "v_exp_f32 v107, v107\n"
    "s_xor_b32 s53, s89, 12288\n"
    "s_mov_b64 exec, s[46:47]\n"
    "buffer_load_dwordx4 v[168:171], v248, s[12:15], s53 offen offset:256 sc1\n"
    "s_mov_b64 exec, -1\n"
    "v_exp_f32 v108, v108\n"
    "v_exp_f32 v109, v109\n"
    "v_exp_f32 v110, v110\n"
    "v_exp_f32 v111, v111\n"
    "v_exp_f32 v80, v80\n"
    "v_exp_f32 v81, v81\n"
    "s_waitcnt lgkmcnt(2)\n"
    "buffer_store_dwordx4 v[184:187], v2, s[12:15], s57 offen sc1\n"
    "v_smfmac_f32_32x32x32_bf16 v[32:47], v[156:159], v[104:111], v112\n"
    "v_exp_f32 v82, v82\n"
    "v_exp_f32 v83, v83\n"
    "v_exp_f32 v84, v84\n"
    "v_mfma_f32_32x32x16_bf16 v[96:111], v[148:151], v[132:135], 0\n"
    "v_exp_f32 v85, v85\n"
    "v_exp_f32 v86, v86\n"
    "v_exp_f32 v87, v87\n"
    "v_exp_f32 v88, v88\n"
    "v_exp_f32 v89, v89\n"
    "s_waitcnt vmcnt(7)\n"
    "v_max3_u32 v0, v4, v5, v6\n"
    "v_smfmac_f32_32x32x32_bf16 v[48:63], v[152:155], v[80:87], v112\n"
    "v_exp_f32 v90, v90\n"
    "s_waitcnt vmcnt(6)\n"
    "v_max3_u32 v0, v7, v8, v0\n"
    "s_setprio 0\n"
    "v_exp_f32 v91, v91\n"
    "v_exp_f32 v92, v92\n"
    "v_max3_u32 v0, v9, v10, v0\n"
    "v_exp_f32 v93, v93\n"
    "v_exp_f32 v94, v94\n"
    "s_waitcnt vmcnt(5)\n"
    "v_max3_u32 v0, v11, v12, v0\n"
    "v_exp_f32 v95, v95\n"
    "v_exp_f32 v96, v96\n"
    "v_exp_f32 v97, v97\n"
    "v_max3_u32 v0, v13, v14, v0\n"
    "v_smfmac_f32_32x32x32_bf16 v[48:63], v[156:159], v[88:95], v112\n"
    "v_max_u32 v0, v15, v0\n"
    "v_exp_f32 v98, v98\n"
    "v_exp_f32 v99, v99\n"
    "s_setprio 3\n"
    "v_cmp_eq_u32 vcc, -1, v0\n"
    "s_cbranch_vccnz kp_6_%=\n"
    "kb_7_%=:\n"
    "v_exp_f32 v100, v100\n"
    "v_mfma_f32_32x32x16_bf16 v[80:95], v[4:7], v[120:123], 0\n"
    "v_exp_f32 v101, v101\n"
    "v_exp_f32 v102, v102\n"
    "v_exp_f32 v103, v103\n"
    "v_exp_f32 v104, v104\n"
    "v_exp_f32 v105, v105\n"
    "v_smfmac_f32_32x32x32_bf16 v[64:79], v[152:155], v[96:103], v112\n"
    "v_exp_f32 v106, v106\n"
    "v_exp_f32 v107, v107\n"
    "s_add_u32 s54, s57, s58\n"
    "s_waitcnt lgkmcnt(0)\n"
    "buffer_store_dwordx4 v[188:191], v2, s[12:15], s54 offen sc1\n"
    "v_exp_f32 v108, v108\n"
    "v_exp_f32 v109, v109\n"
    "v_exp_f32 v110, v110\n"
    "v_exp_f32 v111, v111\n"
    "v_exp_f32 v80, v80\n"
    "v_exp_f32 v81, v81\n"
    "s_xor_b32 s53, s89, 16384\n"
    "buffer_load_dwordx4 v[172:175], v2, s[12:15], s53 offen sc1\n"
    "v_smfmac_f32_32x32x32_bf16 v[64:79], v[156:159], v[104:111], v112\n"
    "v_exp_f32 v82, v82\n"
    "v_exp_f32 v83, v83\n"
    "v_exp_f32 v84, v84\n"
    "v_mfma_f32_32x32x16_bf16 v[96:111], v[4:7], v[124:127], 0\n"
    "v_exp_f32 v85, v85\n"
    "s_xor_b32 s53, s89, 16384\n"
    "s_mov_b64 exec, s[46:47]\n"
    "buffer_load_dwordx4 v[176:179], v248, s[12:15], s53 offen offset:0 sc1\n"
    "s_mov_b64 exec, -1\n"
    "v_exp_f32 v86, v86\n"
    "v_exp_f32 v87, v87\n"
    "v_exp_f32 v88, v88\n"
    "v_exp_f32 v89, v89\n"
    "v_smfmac_f32_32x32x32_bf16 v[16:31], v[8:11], v[80:87], v112\n"
    "v_exp_f32 v90, v90\n"
    "v_exp_f32 v91, v91\n"
    "v_exp_f32 v92, v92\n"
    "v_exp_f32 v93, v93\n"
    "v_exp_f32 v94, v94\n"
    "v_exp_f32 v95, v95\n"
    "v_exp_f32 v96, v96\n"
    "v_exp_f32 v97, v97\n"
    "s_xor_b32 s53, s89, 16384\n"
    "s_mov_b64 exec, s[46:47]\n"
    "buffer_load_dwordx4 v[180:183], v248, s[12:15], s53 offen offset:256 sc1\n"
    "s_mov_b64 exec, -1\n"
    "v_smfmac_f32_32x32x32_bf16 v[16:31], v[12:15], v[88:95], v112\n"
    "v_exp_f32 v98, v98\n"
    "v_exp_f32 v99, v99\n"
    "v_exp_f32 v100, v100\n"
    "v_mfma_f32_32x32x16_bf16 v[80:95], v[4:7], v[128:131], 0\n"
    "v_exp_f32 v101, v101\n"
    "s_lshl_b32 s54, s38, 15\n"
    "s_add_u32 s54, s54, s68\n"
    "buffer_store_dwordx4 v[196:199], v2, s[12:15], s54 offen sc1\n"
    "v_exp_f32 v102, v102\n"
    "v_exp_f32 v103, v103\n"
    "v_exp_f32 v104, v104\n"
    "v_exp_f32 v105, v105\n"
    "v_smfmac_f32_32x32x32_bf16 v[32:47], v[8:11], v[96:103], v112\n"
    "v_exp_f32 v106, v106\n"
    "v_exp_f32 v107, v107\n"
    "v_exp_f32 v108, v108\n"
    "v_exp_f32 v109, v109\n"
    "v_exp_f32 v110, v110\n"
    "v_exp_f32 v111, v111\n"
    "v_exp_f32 v80, v80\n"
    "v_exp_f32 v81, v81\n"
    "s_xor_b32 s53, s89, 20480\n"
    "buffer_load_dwordx4 v[136:139], v2, s[12:15], s53 offen sc1\n"
    "v_smfmac_f32_32x32x32_bf16 v[32:47], v[12:15], v[104:111], v112\n"
    "v_exp_f32 v82, v82\n"
    "v_exp_f32 v83, v83\n"
    "s_setprio 2\n"
    "v_exp_f32 v84, v84\n"
    "v_mfma_f32_32x32x16_bf16 v[96:111], v[4:7], v[132:135], 0\n"
    "v_exp_f32 v85, v85\n"
    "s_xor_b32 s53, s89, 20480\n"
    "s_mov_b64 exec, s[46:47]\n"
    "buffer_load_dwordx4 v[140:143], v248, s[12:15], s53 offen offset:0 sc1\n"
    "s_mov_b64 exec, -1\n"
    "v_exp_f32 v86, v86\n"
    "v_exp_f32 v87, v87\n"
    "v_exp_f32 v88, v88\n"
    "v_exp_f32 v89, v89\n"
    "s_waitcnt vmcnt(10)\n"
    "v_max3_u32 v0, v160, v161, v162\n"
    "v_smfmac_f32_32x32x32_bf16 v[48:63], v[8:11], v[80:87], v112\n"
    "v_exp_f32 v90, v90\n"
    "s_waitcnt vmcnt(9)\n"
    "v_max3_u32 v0, v163, v164, v0\n"
    "v_exp_f32 v91, v91\n"
    "s_xor_b32 s53, s89, 20480\n"
    "s_mov_b64 exec, s[46:47]\n"
    "buffer_load_dwordx4 v[144:147], v248, s[12:15], s53 offen offset:256 sc1\n"
    "s_mov_b64 exec, -1\n"
    "v_exp_f32 v92, v92\n"
    "v_max3_u32 v0, v165, v166, v0\n"
    "v_exp_f32 v93, v93\n"
    "v_exp_f32 v94, v94\n"
    "s_waitcnt vmcnt(9)\n"
    "v_max3_u32 v0, v167, v168, v0\n"
    "v_exp_f32 v95, v95\n"
    "v_exp_f32 v96, v96\n"
    "v_exp_f32 v97, v97\n"
    "v_max3_u32 v0, v169, v170, v0\n"
    "s_lshl_b32 s54, s38, 15\n"
    "s_lshl_b32 s55, s39, 10\n"
    "s_add_u32 s54, s54, s55\n"
    "s_add_u32 s54, s54, s69\n"
    "buffer_store_dwordx4 v[196:199], v2, s[12:15], s54 offen sc1\n"
    "v_smfmac_f32_32x32x32_bf16 v[48:63], v[12:15], v[88:95], v112\n"
    "v_max_u32 v0, v171, v0\n"
    "v_exp_f32 v98, v98\n"
    "v_exp_f32 v99, v99\n"
    "v_cmp_eq_u32 vcc, -1, v0\n"
    "s_cbranch_vccnz kp_8_%=\n"
    "kb_9_%=:\n"
    "v_exp_f32 v100, v100\n"
    "v_mfma_f32_32x32x16_bf16 v[80:95], v[160:163], v[120:123], 0\n"
    "v_exp_f32 v101, v101\n"
    "v_exp_f32 v102, v102\n"
    "v_exp_f32 v103, v103\n"
    "v_exp_f32 v104, v104\n"
    "v_exp_f32 v105, v105\n"
    "v_smfmac_f32_32x32x32_bf16 v[64:79], v[8:11], v[96:103], v112\n"
    "v_exp_f32 v106, v106\n"
    "v_exp_f32 v107, v107\n"
    "v_exp_f32 v108, v108\n"
    "v_exp_f32 v109, v109\n"
    "v_exp_f32 v110, v110\n"
    "v_exp_f32 v111, v111\n"
    "v_exp_f32 v80, v80\n"
    "buffer_load_dwordx2 v[250:251], v244, s[12:15], s1 offen offset:0 sc1\n"
    "v_exp_f32 v81, v81\n"
    "v_smfmac_f32_32x32x32_bf16 v[64:79], v[12:15], v[104:111], v112\n"
    "v_exp_f32 v82, v82\n"
    "v_exp_f32 v83, v83\n"
    "s_setprio 1\n"
    "v_exp_f32 v84, v84\n"
    "v_mfma_f32_32x32x16_bf16 v[96:111], v[160:163], v[124:127], 0\n"
    "v_exp_f32 v85, v85\n"
    "v_exp_f32 v86, v86\n"
    "v_exp_f32 v87, v87\n"
    "v_exp_f32 v88, v88\n"
    "v_exp_f32 v89, v89\n"
    "v_smfmac_f32_32x32x32_bf16 v[16:31], v[164:167], v[80:87], v112\n"
    "v_exp_f32 v90, v90\n"
    "v_exp_f32 v91, v91\n"
    "s_lshl_b32 s54, s3, 10\n"
    "s_add_u32 s54, s54, s71\n"
    "s_add_u32 s54, s54, 0\n"
    "buffer_store_dwordx4 v[196:199], v2, s[12:15], s54 offen sc1\n"
    "v_exp_f32 v92, v92\n"
    "v_exp_f32 v93, v93\n"
    "v_exp_f32 v94, v94\n"
    "v_exp_f32 v95, v95\n"
    "v_exp_f32 v96, v96\n"
    "v_exp_f32 v97, v97\n"
    "v_smfmac_f32_32x32x32_bf16 v[16:31], v[168:171], v[88:95], v112\n"
    "v_exp_f32 v98, v98\n"
    "v_exp_f32 v99, v99\n"
    "v_exp_f32 v100, v100\n"
    "v_mfma_f32_32x32x16_bf16 v[80:95], v[160:163], v[128:131], 0\n"
    "v_exp_f32 v101, v101\n"
    "v_exp_f32 v102, v102\n"
    "v_exp_f32 v103, v103\n"
    "v_exp_f32 v104, v104\n"
    "v_exp_f32 v105, v105\n"
    "v_smfmac_f32_32x32x32_bf16 v[32:47], v[164:167], v[96:103], v112\n"
    "v_exp_f32 v106, v106\n"
    "v_exp_f32 v107, v107\n"
    "v_exp_f32 v108, v108\n"
    "v_exp_f32 v109, v109\n"
    "v_exp_f32 v110, v110\n"
    "v_exp_f32 v111, v111\n"
    "v_exp_f32 v80, v80\n"
    "v_exp_f32 v81, v81\n"
    "v_smfmac_f32_32x32x32_bf16 v[32:47], v[168:171], v[104:111], v112\n"
    "v_exp_f32 v82, v82\n"
    "v_exp_f32 v83, v83\n"
    "v_exp_f32 v84, v84\n"
    "v_mfma_f32_32x32x16_bf16 v[96:111], v[160:163], v[132:135], 0\n"
    "v_exp_f32 v85, v85\n"
    "s_lshl_b32 s54, s3, 10\n"
    "s_add_u32 s54, s54, s71\n"
    "s_add_u32 s54, s54, 114688\n"
    "buffer_store_dwordx4 v[196:199], v2, s[12:15], s54 offen sc1\n"
    "v_exp_f32 v86, v86\n"
    "v_exp_f32 v87, v87\n"
    "v_exp_f32 v88, v88\n"
    "v_exp_f32 v89, v89\n"
    "s_waitcnt vmcnt(10)\n"
    "v_max3_u32 v0, v172, v173, v174\n"
    "v_smfmac_f32_32x32x32_bf16 v[48:63], v[164:167], v[80:87], v112\n"
    "v_exp_f32 v90, v90\n"
    "s_waitcnt vmcnt(9)\n"
    "v_max3_u32 v0, v175, v176, v0\n"
    "s_setprio 0\n"
    "v_exp_f32 v91, v91\n"
    "v_exp_f32 v92, v92\n"
    "v_max3_u32 v0, v177, v178, v0\n"
    "v_exp_f32 v93, v93\n"
    "v_exp_f32 v94, v94\n"
    "s_waitcnt vmcnt(8)\n"
    "v_max3_u32 v0, v179, v180, v0\n"
    "v_exp_f32 v95, v95\n"
    "v_exp_f32 v96, v96\n"
    "v_exp_f32 v97, v97\n"
    "v_max3_u32 v0, v181, v182, v0\n"
    "v_smfmac_f32_32x32x32_bf16 v[48:63], v[168:171], v[88:95], v112\n"
    "v_max_u32 v0, v183, v0\n"
    "v_exp_f32 v98, v98\n"
    "v_exp_f32 v99, v99\n"
    "s_setprio 3\n"
    "v_cmp_eq_u32 vcc, -1, v0\n"
    "s_cbranch_vccnz kp_10_%=\n"
    "kb_11_%=:\n"
    "v_exp_f32 v100, v100\n"
    "v_mfma_f32_32x32x16_bf16 v[80:95], v[172:175], v[120:123], 0\n"
    "v_exp_f32 v101, v101\n"
    "v_exp_f32 v102, v102\n"
    "v_exp_f32 v103, v103\n"
    "v_exp_f32 v104, v104\n"
    "v_exp_f32 v105, v105\n"
    "v_smfmac_f32_32x32x32_bf16 v[64:79], v[164:167], v[96:103], v112\n"
    "v_exp_f32 v106, v106\n"
    "v_exp_f32 v107, v107\n"
    "v_exp_f32 v108, v108\n"
    "v_exp_f32 v109, v109\n"
    "v_exp_f32 v110, v110\n"
    "v_exp_f32 v111, v111\n"
    "v_exp_f32 v80, v80\n"
    "s_waitcnt vmcnt(2)\n"
    "v_cmp_eq_u64 s[48:49], s[40:41], v[250:251]\n"
    "s_andn2_b32 s54, 65535, s48\n"
    "s_cbranch_scc1 kvp_17_%=\n"
    "kvb_18_%=:\n"
    "v_exp_f32 v81, v81\n"
    "s_xor_b32 s53, s89, 24576\n"
    "buffer_load_dwordx4 v[148:151], v2, s[12:15], s53 offen sc1\n"
    "v_smfmac_f32_32x32x32_bf16 v[64:79], v[168:171], v[104:111], v112\n"
    "v_exp_f32 v82, v82\n"
    "v_exp_f32 v83, v83\n"
    "v_exp_f32 v84, v84\n"
    "v_mfma_f32_32x32x16_bf16 v[96:111], v[172:175], v[124:127], 0\n"
    "v_exp_f32 v85, v85\n"
    "s_lshl_b32 s54, s3, 10\n"
    "s_add_u32 s54, s54, s71\n"
    "s_add_u32 s54, s54, 229376\n"
    "buffer_store_dwordx4 v[196:199], v2, s[12:15], s54 offen sc1\n"
    "v_exp_f32 v86, v86\n"
    "v_exp_f32 v87, v87\n"
    "v_exp_f32 v88, v88\n"
    "v_exp_f32 v89, v89\n"
    "v_smfmac_f32_32x32x32_bf16 v[16:31], v[176:179], v[80:87], v112\n"
    "v_exp_f32 v90, v90\n"
    "v_exp_f32 v91, v91\n"
    "s_xor_b32 s53, s89, 24576\n"
    "s_mov_b64 exec, s[46:47]\n"
    "buffer_load_dwordx4 v[152:155], v248, s[12:15], s53 offen offset:0 sc1\n"
    "s_mov_b64 exec, -1\n"
    "v_exp_f32 v92, v92\n"
    "v_exp_f32 v93, v93\n"
    "v_exp_f32 v94, v94\n"
    "v_exp_f32 v95, v95\n"
    "v_exp_f32 v96, v96\n"
    "v_exp_f32 v97, v97\n"
    "v_smfmac_f32_32x32x32_bf16 v[16:31], v[180:183], v[88:95], v112\n"
    "v_exp_f32 v98, v98\n"
    "v_exp_f32 v99, v99\n"
    "v_exp_f32 v100, v100\n"
    "v_mfma_f32_32x32x16_bf16 v[80:95], v[172:175], v[128:131], 0\n"
    "v_exp_f32 v101, v101\n"
    "s_xor_b32 s53, s89, 24576\n"
    "s_mov_b64 exec, s[46:47]\n"
    "buffer_load_dwordx4 v[156:159], v248, s[12:15], s53 offen offset:256 sc1\n"
    "s_mov_b64 exec, -1\n"
    "v_exp_f32 v102, v102\n"
    "v_exp_f32 v103, v103\n"
    "v_exp_f32 v104, v104\n"
    "v_exp_f32 v105, v105\n"
    "v_smfmac_f32_32x32x32_bf16 v[32:47], v[176:179], v[96:103], v112\n"
    "v_exp_f32 v106, v106\n"
    "v_exp_f32 v107, v107\n"
    "s_xor_b32 s53, s89, 28672\n"
    "buffer_load_dwordx4 v[4:7], v2, s[12:15], s53 offen sc1\n"
    "v_exp_f32 v108, v108\n"
    "v_exp_f32 v109, v109\n"
    "v_exp_f32 v110, v110\n"
    "v_exp_f32 v111, v111\n"
    "v_exp_f32 v80, v80\n"
    "v_exp_f32 v81, v81\n"
    "s_lshl_b32 s54, s3, 10\n"
    "s_add_u32 s54, s54, s71\n"
    "s_add_u32 s54, s54, 344064\n"
    "buffer_store_dwordx4 v[196:199], v2, s[12:15], s54 offen sc1\n"
    "v_smfmac_f32_32x32x32_bf16 v[32:47], v[180:183], v[104:111], v112\n"
    "v_exp_f32 v82, v82\n"
    "v_exp_f32 v83, v83\n"
    "s_setprio 2\n"
    "v_exp_f32 v84, v84\n"
    "v_mfma_f32_32x32x16_bf16 v[96:111], v[172:175], v[132:135], 0\n"
    "v_exp_f32 v85, v85\n"
    "v_exp_f32 v86, v86\n"
    "v_exp_f32 v87, v87\n"
    "v_exp_f32 v88, v88\n"
    "v_exp_f32 v89, v89\n"
    "v_max3_u32 v0, v136, v137, v138\n"
    "v_smfmac_f32_32x32x32_bf16 v[48:63], v[176:179], v[80:87], v112\n"
    "v_exp_f32 v90, v90\n"
    "v_max3_u32 v0, v139, v140, v0\n"
    "v_exp_f32 v91, v91\n"
    "s_xor_b32 s53, s89, 28672\n"
    "s_mov_b64 exec, s[46:47]\n"
    "buffer_load_dwordx4 v[8:11], v248, s[12:15], s53 offen offset:0 sc1\n"
    "s_mov_b64 exec, -1\n"
    "v_exp_f32 v92, v92\n"
    "v_max3_u32 v0, v141, v142, v0\n"
    "v_exp_f32 v93, v93\n"
    "v_exp_f32 v94, v94\n"
    "v_max3_u32 v0, v143, v144, v0\n"
    "v_exp_f32 v95, v95\n"
    "v_exp_f32 v96, v96\n"
    "v_exp_f32 v97, v97\n"
    "v_max3_u32 v0, v145, v146, v0\n"
    "s_xor_b32 s53, s89, 28672\n"
    "s_mov_b64 exec, s[46:47]\n"
    "buffer_load_dwordx4 v[12:15], v248, s[12:15], s53 offen offset:256 sc1\n"
    "s_mov_b64 exec, -1\n"
    "v_smfmac_f32_32x32x32_bf16 v[48:63], v[180:183], v[88:95], v112\n"
    "v_max_u32 v0, v147, v0\n"
    "v_exp_f32 v98, v98\n"
    "v_exp_f32 v99, v99\n"
    "v_cmp_eq_u32 vcc, -1, v0\n"
    "s_cbranch_vccnz kp_12_%=\n"
    "kb_13_%=:\n"
    "v_exp_f32 v100, v100\n"
    "v_mfma_f32_32x32x16_bf16 v[80:95], v[136:139], v[120:123], 0\n"
    "v_exp_f32 v101, v101\n"
    "s_xor_b32 s53, s89, 32768\n"
    "buffer_load_dwordx4 v[160:163], v2, s[12:15], s53 offen sc1\n"
    "v_exp_f32 v102, v102\n"
    "v_exp_f32 v103, v103\n"
    "v_exp_f32 v104, v104\n"
    "v_exp_f32 v105, v105\n"
    "v_smfmac_f32_32x32x32_bf16 v[64:79], v[176:179], v[96:103], v112\n"
    "v_exp_f32 v106, v106\n"
    "v_exp_f32 v107, v107\n"
    "s_xor_b32 s53, s89, 32768\n"
    "s_mov_b64 exec, s[46:47]\n"
    "buffer_load_dwordx4 v[164:167], v248, s[12:15], s53 offen offset:0 sc1\n"
    "s_mov_b64 exec, -1\n"
    "v_exp_f32 v108, v108\n"
    "v_exp_f32 v109, v109\n"
    "v_exp_f32 v110, v110\n"
    "v_exp_f32 v111, v111\n"
    "v_exp_f32 v80, v80\n"
    "v_exp_f32 v81, v81\n"
    "s_xor_b32 s53, s89, 32768\n"
    "s_mov_b64 exec, s[46:47]\n"
    "buffer_load_dwordx4 v[168:171], v248, s[12:15], s53 offen offset:256 sc1\n"
    "s_mov_b64 exec, -1\n"
    "v_smfmac_f32_32x32x32_bf16 v[64:79], v[180:183], v[104:111], v112\n"
    "v_exp_f32 v82, v82\n"
    "v_exp_f32 v83, v83\n"
    "s_setprio 1\n"
    "v_exp_f32 v84, v84\n"
    "v_mfma_f32_32x32x16_bf16 v[96:111], v[136:139], v[124:127], 0\n"
    "v_exp_f32 v85, v85\n"
    "s_xor_b32 s53, s89, 40960\n"
    "buffer_load_dwordx4 v[172:175], v2, s[12:15], s53 offen sc1\n"
    "v_exp_f32 v86, v86\n"
    "v_exp_f32 v87, v87\n"
    "v_exp_f32 v88, v88\n"
    "v_exp_f32 v89, v89\n"
    "v_smfmac_f32_32x32x32_bf16 v[16:31], v[140:143], v[80:87], v112\n"
    "v_exp_f32 v90, v90\n"
    "v_exp_f32 v91, v91\n"
    "s_xor_b32 s53, s89, 40960\n"
    "s_mov_b64 exec, s[46:47]\n"
    "buffer_load_dwordx4 v[176:179], v248, s[12:15], s53 offen offset:0 sc1\n"
    "s_mov_b64 exec, -1\n"
    "v_exp_f32 v92, v92\n"
    "v_exp_f32 v93, v93\n"
    "v_exp_f32 v94, v94\n"
    "v_exp_f32 v95, v95\n"
    "v_exp_f32 v96, v96\n"
    "v_exp_f32 v97, v97\n"
    "s_xor_b32 s53, s89, 40960\n"
    "s_mov_b64 exec, s[46:47]\n"
    "buffer_load_dwordx4 v[180:183], v248, s[12:15], s53 offen offset:256 sc1\n"
    "s_mov_b64 exec, -1\n"
    "v_smfmac_f32_32x32x32_bf16 v[16:31], v[144:147], v[88:95], v112\n"
    "v_exp_f32 v98, v98\n"
    "v_exp_f32 v99, v99\n"
    "v_exp_f32 v100, v100\n"
    "v_mfma_f32_32x32x16_bf16 v[80:95], v[136:139], v[128:131], 0\n"
    "v_exp_f32 v101, v101\n"
    "v_exp_f32 v102, v102\n"
    "v_exp_f32 v103, v103\n"
    "v_exp_f32 v104, v104\n"
    "v_exp_f32 v105, v105\n"
    "v_smfmac_f32_32x32x32_bf16 v[32:47], v[140:143], v[96:103], v112\n"
    "v_exp_f32 v106, v106\n"
    "v_exp_f32 v107, v107\n"
    "v_exp_f32 v108, v108\n"
    "v_exp_f32 v109, v109\n"
    "v_exp_f32 v110, v110\n"
    "v_exp_f32 v111, v111\n"
    "v_exp_f32 v80, v80\n"
    "v_exp_f32 v81, v81\n"
    "v_smfmac_f32_32x32x32_bf16 v[32:47], v[144:147], v[104:111], v112\n"
    "v_exp_f32 v82, v82\n"
    "v_exp_f32 v83, v83\n"
    "v_exp_f32 v84, v84\n"
    "v_mfma_f32_32x32x16_bf16 v[96:111], v[136:139], v[132:135], 0\n"
    "v_exp_f32 v85, v85\n"
    "s_xor_b32 s53, s89, 36864\n"
    "buffer_load_dwordx4 v[136:139], v2, s[12:15], s53 offen sc1\n"
    "v_exp_f32 v86, v86\n"
    "v_exp_f32 v87, v87\n"
    "v_exp_f32 v88, v88\n"
    "v_exp_f32 v89, v89\n"
    "v_smfmac_f32_32x32x32_bf16 v[48:63], v[140:143], v[80:87], v112\n"
    "v_exp_f32 v90, v90\n"
    "s_setprio 0\n"
    "v_exp_f32 v91, v91\n"
    "v_exp_f32 v92, v92\n"
    "v_exp_f32 v93, v93\n"
    "v_exp_f32 v94, v94\n"
    "v_exp_f32 v95, v95\n"
    "v_exp_f32 v96, v96\n"
    "v_exp_f32 v97, v97\n"
    "v_smfmac_f32_32x32x32_bf16 v[48:63], v[144:147], v[88:95], v112\n"
    "v_exp_f32 v98, v98\n"
    "v_exp_f32 v99, v99\n"
    "s_setprio 3\n"
    "v_exp_f32 v100, v100\n"
    "s_waitcnt vmcnt(14)\n"
    "v_mfma_f32_32x32x16_bf16 v[80:95], v[148:151], v[120:123], 0\n"
    "v_exp_f32 v101, v101\n"
    "v_exp_f32 v102, v102\n"
    "v_exp_f32 v103, v103\n"
    "v_exp_f32 v104, v104\n"
    "v_exp_f32 v105, v105\n"
    "v_smfmac_f32_32x32x32_bf16 v[64:79], v[140:143], v[96:103], v112\n"
    "v_exp_f32 v106, v106\n"
    "v_exp_f32 v107, v107\n"
    "s_xor_b32 s53, s89, 36864\n"
    "s_mov_b64 exec, s[46:47]\n"
    "buffer_load_dwordx4 v[140:143], v248, s[12:15], s53 offen offset:0 sc1\n"
    "s_mov_b64 exec, -1\n"
    "v_exp_f32 v108, v108\n"
    "v_exp_f32 v109, v109\n"
    "v_exp_f32 v110, v110\n"
    "v_exp_f32 v111, v111\n"
    "v_exp_f32 v80, v80\n"
    "v_exp_f32 v81, v81\n"
    "v_smfmac_f32_32x32x32_bf16 v[64:79], v[144:147], v[104:111], v112\n"
    "v_exp_f32 v82, v82\n"
    "v_mov_b32 v184, 161792\n"
    "v_mov_b32 v185, 1\n"
    "s_waitcnt vmcnt(10)\n"
    "s_mov_b64 exec, 1\n"
    "ds_add_rtn_u32 v186, v184, v185\n"
    "s_mov_b64 exec, -1\n"
    "v_exp_f32 v83, v83\n"
    "v_exp_f32 v84, v84\n"
    "v_mfma_f32_32x32x16_bf16 v[96:111], v[148:151], v[124:127], 0\n"
    "v_exp_f32 v85, v85\n"
    "s_xor_b32 s53, s89, 36864\n"
    "s_mov_b64 exec, s[46:47]\n"
    "buffer_load_dwordx4 v[144:147], v248, s[12:15], s53 offen offset:256 sc1\n"
    "s_mov_b64 exec, -1\n"
    "v_exp_f32 v86, v86\n"
    "v_exp_f32 v87, v87\n"
    "v_exp_f32 v88, v88\n"
    "v_exp_f32 v89, v89\n"
    "v_smfmac_f32_32x32x32_bf16 v[16:31], v[152:155], v[80:87], v112\n"
    "v_exp_f32 v90, v90\n"
    "v_exp_f32 v91, v91\n"
    "s_waitcnt lgkmcnt(0)\n"
    "v_readfirstlane_b32 s54, v186\n"
    "s_cmp_lg_u32 s54, 7\n"
    "s_cbranch_scc1 rf_20_%=\n"
    "s_lshl_b32 s54, s36, 5\n"
    "s_add_u32 s54, s54, s37\n"
    "s_lshl_b32 s54, s54, 7\n"
    "v_writelane_b32 v184, s54, 0\n"
    "s_lshl_b32 s54, s64, 5\n"
    "s_add_u32 s54, s54, s65\n"
    "s_lshl_b32 s54, s54, 7\n"
    "s_add_u32 s54, s54, 2048\n"
    "v_writelane_b32 v184, s54, 1\n"
    "s_lshl_b32 s54, s2, 7\n"
    "s_add_u32 s54, s54, 131072\n"
    "v_writelane_b32 v184, s54, 2\n"
    "s_mov_b32 s54, 19988480\n"
    "s_mov_b64 exec, 7\n"
    "buffer_store_dwordx2 v[252:253], v184, s[12:15], s54 offen sc1\n"
    "s_mov_b64 exec, -1\n"
    "rf_20_%=:\n"
    "v_exp_f32 v92, v92\n"
    "v_exp_f32 v93, v93\n"
    "v_exp_f32 v94, v94\n"
    "v_exp_f32 v95, v95\n"
    "v_exp_f32 v96, v96\n"
    "v_exp_f32 v97, v97\n"
    "v_smfmac_f32_32x32x32_bf16 v[16:31], v[156:159], v[88:95], v112\n"
    "v_exp_f32 v98, v98\n"
    "s_lshl_b32 s78, s3, 12\n"
    "s_lshl_b32 s54, s65, 15\n"
    "s_add_u32 s54, s54, s78\n"
    "s_add_u32 s72, s54, 15728640\n"
    "s_lshl_b32 s54, s64, 17\n"
    "s_add_u32 s54, s54, s78\n"
    "s_add_u32 s73, s54, 4194304\n"
    "s_add_u32 s74, s54, 6291456\n"
    "s_lshl1_add_u32 s54, s38, s39\n"
    "s_lshl_b32 s54, s54, 10\n"
    "s_add_u32 s76, s54, s71\n"
    "s_lshl_b32 s54, s64, 5\n"
    "s_add_u32 s54, s54, s3\n"
    "s_mul_i32 s54, s54, 14336\n"
    "s_and_b32 s55, s65, 1\n"
    "s_lshl_b32 s55, s55, 8\n"
    "s_add_u32 s54, s54, s55\n"
    "s_add_u32 s75, s54, 8388608\n"
    "s_lshl_b32 s54, s38, 8\n"
    "s_add_u32 s68, s68, s54\n"
    "s_lshl_b32 s54, s3, 8\n"
    "s_add_u32 s69, s69, s54\n"
    "s_mul_i32 s57, s3, 5120\n"
    "s_mov_b32 s8, 0xaaaaaaaa\n"
    "s_mov_b32 s9, 0xaaaaaaaa\n"
    "s_mov_b32 s10, 0xcccccccc\n"
    "s_mov_b32 s11, 0xcccccccc\n"
    "s_mov_b32 s88, 0x3eeff558\n"
    "v_exp_f32 v99, v99\n"
    "v_exp_f32 v100, v100\n"
    "v_mfma_f32_32x32x16_bf16 v[80:95], v[148:151], v[128:131], 0\n"
    "v_exp_f32 v101, v101\n"
    "v_exp_f32 v102, v102\n"
    "v_exp_f32 v103, v103\n"
    "v_exp_f32 v104, v104\n"
    "v_exp_f32 v105, v105\n"
    "v_smfmac_f32_32x32x32_bf16 v[32:47], v[152:155], v[96:103], v112\n"
    "v_exp_f32 v106, v106\n"
    "v_exp_f32 v107, v107\n"
    "v_exp_f32 v108, v108\n"
    "v_exp_f32 v109, v109\n"
    "v_exp_f32 v110, v110\n"
    "v_exp_f32 v111, v111\n"
    "v_exp_f32 v80, v80\n"
    "v_exp_f32 v81, v81\n"
    "v_smfmac_f32_32x32x32_bf16 v[32:47], v[156:159], v[104:111], v112\n"
    "v_exp_f32 v82, v82\n"
    "v_exp_f32 v83, v83\n"
    "s_setprio 2\n"
    "v_exp_f32 v84, v84\n"
    "v_mfma_f32_32x32x16_bf16 v[96:111], v[148:151], v[132:135], 0\n"
    "v_exp_f32 v85, v85\n"
    "s_xor_b32 s53, s89, 45056\n"
    "buffer_load_dwordx4 v[148:151], v2, s[12:15], s53 offen sc1\n"
    "v_exp_f32 v86, v86\n"
    "v_exp_f32 v87, v87\n"
    "v_exp_f32 v88, v88\n"
    "v_exp_f32 v89, v89\n"
    "v_smfmac_f32_32x32x32_bf16 v[48:63], v[152:155], v[80:87], v112\n"
    "v_exp_f32 v90, v90\n"
    "v_exp_f32 v91, v91\n"
    "v_exp_f32 v92, v92\n"
    "v_exp_f32 v93, v93\n"
    "v_exp_f32 v94, v94\n"
    "v_exp_f32 v95, v95\n"
    "v_exp_f32 v96, v96\n"
    "v_exp_f32 v97, v97\n"
    "v_smfmac_f32_32x32x32_bf16 v[48:63], v[156:159], v[88:95], v112\n"
    "v_exp_f32 v98, v98\n"
    "s_add_u32 s20, s38, 1\n"
    "s_and_b32 s20, s20, 3\n"
    "s_lshl2_add_u32 s20, s39, s20\n"
    "s_lshl_b32 s20, s20, 12\n"
    "v_exp_f32 v99, v99\n"
    "v_exp_f32 v100, v100\n"
    "v_mfma_f32_32x32x16_bf16 v[80:95], v[4:7], v[120:123], 0\n"
    "v_exp_f32 v101, v101\n"
    "v_exp_f32 v102, v102\n"
    "v_exp_f32 v103, v103\n"
    "s_add_u32 s21, s38, 2\n"
    "s_and_b32 s21, s21, 3\n"
    "s_lshl2_add_u32 s21, s39, s21\n"
    "s_lshl_b32 s21, s21, 12\n"
    "v_exp_f32 v104, v104\n"
    "v_exp_f32 v105, v105\n"
    "v_smfmac_f32_32x32x32_bf16 v[64:79], v[152:155], v[96:103], v112\n"
    "v_exp_f32 v106, v106\n"
    "v_exp_f32 v107, v107\n"
    "s_xor_b32 s53, s89, 45056\n"
    "s_mov_b64 exec, s[46:47]\n"
    "buffer_load_dwordx4 v[152:155], v248, s[12:15], s53 offen offset:0 sc1\n"
    "s_mov_b64 exec, -1\n"
    "v_exp_f32 v108, v108\n"
    "v_exp_f32 v109, v109\n"
    "s_add_u32 s22, s38, 3\n"
    "s_and_b32 s22, s22, 3\n"
    "s_lshl2_add_u32 s22, s39, s22\n"
    "s_lshl_b32 s22, s22, 12\n"
    "v_exp_f32 v110, v110\n"
    "v_exp_f32 v111, v111\n"
    "v_exp_f32 v80, v80\n"
    "v_exp_f32 v81, v81\n"
    "v_smfmac_f32_32x32x32_bf16 v[64:79], v[156:159], v[104:111], v112\n"
    "v_exp_f32 v82, v82\n"
    "s_lshl_b32 s23, s39, 10\n"
    "s_lshl2_add_u32 s23, s38, s23\n"
    "s_add_u32 s23, s23, 67584\n"
    "v_exp_f32 v83, v83\n"
    "s_setprio 1\n"
    "v_exp_f32 v84, v84\n"
    "v_mfma_f32_32x32x16_bf16 v[96:111], v[4:7], v[124:127], 0\n"
    "v_exp_f32 v85, v85\n"
    "s_xor_b32 s53, s89, 45056\n"
    "s_mov_b64 exec, s[46:47]\n"
    "buffer_load_dwordx4 v[156:159], v248, s[12:15], s53 offen offset:256 sc1\n"
    "s_mov_b64 exec, -1\n"
    "v_exp_f32 v86, v86\n"
    "v_exp_f32 v87, v87\n"
    "s_add_u32 s4, s51, 71680\n"
    "s_xor_b32 s5, s51, 8192\n"
    "s_add_u32 s5, s5, 71680\n"
    "v_exp_f32 v88, v88\n"
    "v_exp_f32 v89, v89\n"
    "s_waitcnt vmcnt(13)\n"
    "v_smfmac_f32_32x32x32_bf16 v[16:31], v[8:11], v[80:87], v112\n"
    "v_exp_f32 v90, v90\n"
    "v_exp_f32 v91, v91\n"
    "v_exp_f32 v92, v92\n"
    "v_exp_f32 v93, v93\n"
    "s_lshr_b32 s6, s65, 1\n"
    "s_and_b32 s7, s65, 1\n"
    "s_lshl4_add_u32 s6, s6, s7\n"
    "s_lshl3_add_u32 s6, s6, s3\n"
    "s_lshl_b32 s6, s6, 5\n"
    "v_exp_f32 v94, v94\n"
    "v_exp_f32 v95, v95\n"
    "v_exp_f32 v96, v96\n"
    "v_exp_f32 v97, v97\n"
    "s_waitcnt vmcnt(12)\n"
    "v_smfmac_f32_32x32x32_bf16 v[16:31], v[12:15], v[88:95], v112\n"
    "v_exp_f32 v98, v98\n"
    "s_lshl2_add_u32 s7, s65, s38\n"
    "s_lshl1_add_u32 s7, s7, s39\n"
    "s_lshl_b32 s7, s7, 5\n"
    "v_exp_f32 v99, v99\n"
    "v_exp_f32 v100, v100\n"
    "v_mfma_f32_32x32x16_bf16 v[80:95], v[4:7], v[128:131], 0\n"
    "v_exp_f32 v101, v101\n"
    "v_exp_f32 v102, v102\n"
    "v_exp_f32 v103, v103\n"
    "v_exp_f32 v104, v104\n"
    "v_exp_f32 v105, v105\n"
    "v_smfmac_f32_32x32x32_bf16 v[32:47], v[8:11], v[96:103], v112\n"
    "v_exp_f32 v106, v106\n"
    "v_exp_f32 v107, v107\n"
    "v_exp_f32 v108, v108\n"
    "v_exp_f32 v109, v109\n"
    "v_exp_f32 v110, v110\n"
    "v_exp_f32 v111, v111\n"
    "v_exp_f32 v80, v80\n"
    "v_exp_f32 v81, v81\n"
    "v_smfmac_f32_32x32x32_bf16 v[32:47], v[12:15], v[104:111], v112\n"
    "v_exp_f32 v82, v82\n"
    "v_exp_f32 v83, v83\n"
    "v_exp_f32 v84, v84\n"
    "v_mfma_f32_32x32x16_bf16 v[96:111], v[4:7], v[132:135], 0\n"
    "v_exp_f32 v85, v85\n"
    "s_xor_b32 s53, s89, 49152\n"
    "buffer_load_dwordx4 v[4:7], v2, s[12:15], s53 offen sc1\n"
    "v_exp_f32 v86, v86\n"
    "v_exp_f32 v87, v87\n"
    "v_exp_f32 v88, v88\n"
    "v_exp_f32 v89, v89\n"
    "v_smfmac_f32_32x32x32_bf16 v[48:63], v[8:11], v[80:87], v112\n"
    "v_exp_f32 v90, v90\n"
    "s_setprio 0\n"
    "v_exp_f32 v91, v91\n"
    "v_exp_f32 v92, v92\n"
    "v_exp_f32 v93, v93\n"
    "v_exp_f32 v94, v94\n"
    "v_exp_f32 v95, v95\n"
    "v_exp_f32 v96, v96\n"
    "v_exp_f32 v97, v97\n"
    "v_smfmac_f32_32x32x32_bf16 v[48:63], v[12:15], v[88:95], v112\n"
    "v_exp_f32 v98, v98\n"
    "v_exp_f32 v99, v99\n"
    "s_setprio 3\n"
    "v_exp_f32 v100, v100\n"
    "s_waitcnt vmcnt(12)\n"
    "v_mfma_f32_32x32x16_bf16 v[80:95], v[160:163], v[120:123], 0\n"
    "v_exp_f32 v101, v101\n"
    "v_exp_f32 v102, v102\n"
    "v_exp_f32 v103, v103\n"
    "v_exp_f32 v104, v104\n"
    "v_exp_f32 v105, v105\n"
    "v_smfmac_f32_32x32x32_bf16 v[64:79], v[8:11], v[96:103], v112\n"
    "v_exp_f32 v106, v106\n"
    "v_exp_f32 v107, v107\n"
    "s_xor_b32 s53, s89, 49152\n"
    "s_mov_b64 exec, s[46:47]\n"
    "buffer_load_dwordx4 v[8:11], v248, s[12:15], s53 offen offset:0 sc1\n"
    "s_mov_b64 exec, -1\n"
    "v_exp_f32 v108, v108\n"
    "v_exp_f32 v109, v109\n"
    "v_exp_f32 v110, v110\n"
    "v_exp_f32 v111, v111\n"
    "v_exp_f32 v80, v80\n"
    "v_exp_f32 v81, v81\n"
    "v_smfmac_f32_32x32x32_bf16 v[64:79], v[12:15], v[104:111], v112\n"
    "v_exp_f32 v82, v82\n"
    "v_exp_f32 v83, v83\n"
    "v_exp_f32 v84, v84\n"
    "v_mfma_f32_32x32x16_bf16 v[96:111], v[160:163], v[124:127], 0\n"
    "v_exp_f32 v85, v85\n"
    "s_xor_b32 s53, s89, 49152\n"
    "s_mov_b64 exec, s[46:47]\n"
    "buffer_load_dwordx4 v[12:15], v248, s[12:15], s53 offen offset:256 sc1\n"
    "s_mov_b64 exec, -1\n"
    "v_exp_f32 v86, v86\n"
    "v_exp_f32 v87, v87\n"
    "v_exp_f32 v88, v88\n"
    "v_exp_f32 v89, v89\n"
    "s_waitcnt vmcnt(13)\n"
    "v_smfmac_f32_32x32x32_bf16 v[16:31], v[164:167], v[80:87], v112\n"
    "v_exp_f32 v90, v90\n"
    "v_exp_f32 v91, v91\n"
    "v_exp_f32 v92, v92\n"
    "v_exp_f32 v93, v93\n"
    "v_exp_f32 v94, v94\n"
    "v_exp_f32 v95, v95\n"
    "v_exp_f32 v96, v96\n"
    "v_exp_f32 v97, v97\n"
    "s_waitcnt vmcnt(12)\n"
    "v_smfmac_f32_32x32x32_bf16 v[16:31], v[168:171], v[88:95], v112\n"
    "v_exp_f32 v98, v98\n"
    "v_exp_f32 v99, v99\n"
    "v_exp_f32 v100, v100\n"
    "v_mfma_f32_32x32x16_bf16 v[80:95], v[160:163], v[128:131], 0\n"
    "v_exp_f32 v101, v101\n"
    "v_exp_f32 v102, v102\n"
    "v_exp_f32 v103, v103\n"
    "v_exp_f32 v104, v104\n"
    "v_exp_f32 v105, v105\n"
    "v_smfmac_f32_32x32x32_bf16 v[32:47], v[164:167], v[96:103], v112\n"
    "v_exp_f32 v106, v106\n"
    "v_exp_f32 v107, v107\n"
    "v_exp_f32 v108, v108\n"
    "v_exp_f32 v109, v109\n"
    "v_exp_f32 v110, v110\n"
    "v_exp_f32 v111, v111\n"
    "v_exp_f32 v80, v80\n"
    "v_exp_f32 v81, v81\n"
    "v_smfmac_f32_32x32x32_bf16 v[32:47], v[168:171], v[104:111], v112\n"
    "v_exp_f32 v82, v82\n"
    "v_exp_f32 v83, v83\n"
    "s_setprio 2\n"
    "v_exp_f32 v84, v84\n"
    "v_mfma_f32_32x32x16_bf16 v[96:111], v[160:163], v[132:135], 0\n"
    "v_exp_f32 v85, v85\n"
    "s_xor_b32 s53, s89, 53248\n"
    "buffer_load_dwordx4 v[160:163], v2, s[12:15], s53 offen sc1\n"
    "v_exp_f32 v86, v86\n"
    "v_exp_f32 v87, v87\n"
    "v_exp_f32 v88, v88\n"
    "v_exp_f32 v89, v89\n"
    "v_smfmac_f32_32x32x32_bf16 v[48:63], v[164:167], v[80:87], v112\n"
    "v_exp_f32 v90, v90\n"
    "v_exp_f32 v91, v91\n"
    "v_exp_f32 v92, v92\n"
    "v_exp_f32 v93, v93\n"
    "v_exp_f32 v94, v94\n"
    "v_exp_f32 v95, v95\n"
    "v_exp_f32 v96, v96\n"
    "v_exp_f32 v97, v97\n"
    "v_smfmac_f32_32x32x32_bf16 v[48:63], v[168:171], v[88:95], v112\n"
    "v_exp_f32 v98, v98\n"
    "s_lshl_b32 s54, s64, 12\n"
    "v_and_b32 v0, 31, v1\n"
    "v_lshl_add_u32 v0, v0, 7, s54\n"
    "s_lshl2_add_u32 s54, s65, s38\n"
    "s_and_b32 s54, s54, -2\n"
    "s_add_u32 s54, s54, s39\n"
    "s_lshl_b32 s54, s54, 8\n"
    "s_add_u32 s54, s54, 135168\n"
    "v_writelane_b32 v0, s54, 32\n"
    "s_add_u32 s54, s54, 128\n"
    "v_writelane_b32 v0, s54, 33\n"
    "s_lshr_b32 s55, s65, 1\n"
    "s_lshr_b32 s54, s3, 1\n"
    "s_lshl3_add_u32 s54, s54, s55\n"
    "s_lshl_b32 s54, s54, 7\n"
    "s_add_u32 s54, s54, 131072\n"
    "v_writelane_b32 v0, s54, 34\n"
    "s_mul_i32 s54, s3, 14\n"
    "s_and_b32 s54, s54, -8\n"
    "s_add_u32 s54, s54, s55\n"
    "s_lshl_b32 s54, s54, 7\n"
    "s_add_u32 s54, s54, 149504\n"
    "v_writelane_b32 v0, s54, 35\n"
    "s_add_u32 s54, s54, 1024\n"
    "v_writelane_b32 v0, s54, 36\n"
    "s_mul_i32 s54, s3, 14\n"
    "s_add_u32 s54, s54, 13\n"
    "s_and_b32 s54, s54, -8\n"
    "s_add_u32 s54, s54, s55\n"
    "s_lshl_b32 s54, s54, 7\n"
    "s_add_u32 s54, s54, 149504\n"
    "v_writelane_b32 v0, s54, 37\n"
    "v_exp_f32 v99, v99\n"
    "v_exp_f32 v100, v100\n"
    "s_waitcnt vmcnt(12)\n"
    "v_mfma_f32_32x32x16_bf16 v[80:95], v[172:175], v[120:123], 0\n"
    "v_exp_f32 v101, v101\n"
    "s_xor_b32 s53, s89, 57344\n"
    "buffer_load_dwordx4 v[172:175], v2, s[12:15], s53 offen sc1\n"
    "v_exp_f32 v102, v102\n"
    "v_exp_f32 v103, v103\n"
    "v_exp_f32 v104, v104\n"
    "v_exp_f32 v105, v105\n"
    "v_smfmac_f32_32x32x32_bf16 v[64:79], v[164:167], v[96:103], v112\n"
    "v_exp_f32 v106, v106\n"
    "v_exp_f32 v107, v107\n"
    "s_xor_b32 s53, s89, 53248\n"
    "s_mov_b64 exec, s[46:47]\n"
    "buffer_load_dwordx4 v[164:167], v248, s[12:15], s53 offen offset:0 sc1\n"
    "s_mov_b64 exec, -1\n"
    "v_exp_f32 v108, v108\n"
    "v_exp_f32 v109, v109\n"
    "v_exp_f32 v110, v110\n"
    "v_exp_f32 v111, v111\n"
    "v_exp_f32 v80, v80\n"
    "v_exp_f32 v81, v81\n"
    "v_smfmac_f32_32x32x32_bf16 v[64:79], v[168:171], v[104:111], v112\n"
    "v_exp_f32 v82, v82\n"
    "v_exp_f32 v83, v83\n"
    "s_setprio 1\n"
    "v_exp_f32 v84, v84\n"
    "s_waitcnt vmcnt(11)\n"
    "v_mfma_f32_32x32x16_bf16 v[96:111], v[136:139], v[120:123], 0\n"
    "v_exp_f32 v85, v85\n"
    "s_xor_b32 s53, s89, 53248\n"
    "s_mov_b64 exec, s[46:47]\n"
    "buffer_load_dwordx4 v[168:171], v248, s[12:15], s53 offen offset:256 sc1\n"
    "s_mov_b64 exec, -1\n"
    "v_exp_f32 v86, v86\n"
    "v_exp_f32 v87, v87\n"
    "v_exp_f32 v88, v88\n"
    "v_exp_f32 v89, v89\n"
    "v_smfmac_f32_32x32x32_bf16 v[16:31], v[176:179], v[80:87], v112\n"
    "v_exp_f32 v90, v90\n"
    "v_exp_f32 v91, v91\n"
    "s_xor_b32 s53, s89, 57344\n"
    "s_mov_b64 exec, s[46:47]\n"
    "buffer_load_dwordx4 v[176:179], v248, s[12:15], s53 offen offset:0 sc1\n"
    "s_mov_b64 exec, -1\n"
    "v_exp_f32 v92, v92\n"
    "v_exp_f32 v93, v93\n"
    "v_exp_f32 v94, v94\n"
    "v_exp_f32 v95, v95\n"
    "v_exp_f32 v96, v96\n"
    "v_exp_f32 v97, v97\n"
    "v_smfmac_f32_32x32x32_bf16 v[16:31], v[180:183], v[88:95], v112\n"
    "v_exp_f32 v98, v98\n"
    "v_exp_f32 v99, v99\n"
    "v_exp_f32 v100, v100\n"
    "v_mfma_f32_32x32x16_bf16 v[80:95], v[136:139], v[124:127], 0\n"
    "v_exp_f32 v101, v101\n"
    "s_xor_b32 s53, s89, 57344\n"
    "s_mov_b64 exec, s[46:47]\n"
    "buffer_load_dwordx4 v[180:183], v248, s[12:15], s53 offen offset:256 sc1\n"
    "s_mov_b64 exec, -1\n"
    "v_exp_f32 v102, v102\n"
    "v_exp_f32 v103, v103\n"
    "v_exp_f32 v104, v104\n"
    "v_exp_f32 v105, v105\n"
    "s_waitcnt vmcnt(13)\n"
    "v_smfmac_f32_32x32x32_bf16 v[16:31], v[140:143], v[96:103], v112\n"
    "s_mov_b32 s54, 19988480\n"
    "s_mov_b32 exec_hi, 63\n"
    "buffer_load_dwordx2 v[184:185], v0, s[12:15], s54 offen sc1\n"
    "s_mov_b32 exec_hi, -1\n"
    "v_exp_f32 v106, v106\n"
    "v_exp_f32 v107, v107\n"
    "v_exp_f32 v108, v108\n"
    "v_exp_f32 v109, v109\n"
    "v_exp_f32 v110, v110\n"
    "v_exp_f32 v111, v111\n"
    "v_exp_f32 v80, v80\n"
    "v_exp_f32 v81, v81\n"
    "s_lshr_b32 s54, s2, 3\n"
    "s_lshl3_add_u32 s54, s54, s3\n"
    "s_lshl_b32 s54, s54, 7\n"
    "s_add_u32 s54, s54, Ltail_%=-Lstart_%=\n"
    "s_mov_b64 exec, 1\n"
    "buffer_load_dword v228, off, s[32:35], s54\n"
    "s_mov_b64 exec, -1\n"
    "s_waitcnt vmcnt(14)\n"
    "v_smfmac_f32_32x32x32_bf16 v[16:31], v[144:147], v[104:111], v112\n"
    "v_exp_f32 v82, v82\n"
    "v_exp_f32 v83, v83\n"
    "v_exp_f32 v84, v84\n"
    "v_mfma_f32_32x32x16_bf16 v[96:111], v[136:139], v[128:131], 0\n"
    "v_exp_f32 v85, v85\n"
    "v_exp_f32 v86, v86\n"
    "v_exp_f32 v87, v87\n"
    "v_exp_f32 v88, v88\n"
    "v_exp_f32 v89, v89\n"
    "v_smfmac_f32_32x32x32_bf16 v[32:47], v[140:143], v[80:87], v112\n"
    "v_exp_f32 v90, v90\n"
    "s_setprio 0\n"
    "v_exp_f32 v91, v91\n"
    "v_exp_f32 v92, v92\n"
    "v_exp_f32 v93, v93\n"
    "v_exp_f32 v94, v94\n"
    "v_exp_f32 v95, v95\n"
    "v_exp_f32 v96, v96\n"
    "v_exp_f32 v97, v97\n"
    "v_smfmac_f32_32x32x32_bf16 v[32:47], v[144:147], v[88:95], v112\n"
    "v_exp_f32 v98, v98\n"
    "v_exp_f32 v99, v99\n"
    "s_setprio 3\n"
    "v_exp_f32 v100, v100\n"
    "v_mfma_f32_32x32x16_bf16 v[80:95], v[136:139], v[132:135], 0\n"
    "v_exp_f32 v101, v101\n"
    "s_xor_b32 s53, s89, 61440\n"
    "buffer_load_dwordx4 v[136:139], v2, s[12:15], s53 offen sc1\n"
    "v_exp_f32 v102, v102\n"
    "v_exp_f32 v103, v103\n"
    "v_exp_f32 v104, v104\n"
    "v_exp_f32 v105, v105\n"
    "v_smfmac_f32_32x32x32_bf16 v[48:63], v[140:143], v[96:103], v112\n"
    "v_exp_f32 v106, v106\n"
    "v_exp_f32 v107, v107\n"
    "v_exp_f32 v108, v108\n"
    "v_exp_f32 v109, v109\n"
    "v_exp_f32 v110, v110\n"
    "v_exp_f32 v111, v111\n"
    "v_exp_f32 v80, v80\n"
    "s_waitcnt vmcnt(2)\n"
    "v_cmp_ne_u64 vcc, s[40:41], v[184:185]\n"
    "s_and_b32 s54, vcc_hi, 63\n"
    "s_or_b32 s54, s54, vcc_lo\n"
    "s_cbranch_scc1 rp_21_%=\n"
    "rb_22_%=:\n"
    "v_exp_f32 v81, v81\n"
    "buffer_load_dwordx4 v[232:235], v2, s[12:15], s72 offen offset:0 sc1\n"
    "v_smfmac_f32_32x32x32_bf16 v[48:63], v[144:147], v[104:111], v112\n"
    "v_exp_f32 v82, v82\n"
    "v_exp_f32 v83, v83\n"
    "s_setprio 2\n"
    "v_exp_f32 v84, v84\n"
    "v_mfma_f32_32x32x16_bf16 v[96:111], v[148:151], v[120:123], 0\n"
    "v_exp_f32 v85, v85\n"
    "buffer_load_dwordx4 v[236:239], v2, s[12:15], s72 offen offset:1024 sc1\n"
    "v_exp_f32 v86, v86\n"
    "v_exp_f32 v87, v87\n"
    "v_exp_f32 v88, v88\n"
    "v_exp_f32 v89, v89\n"
    "v_smfmac_f32_32x32x32_bf16 v[64:79], v[140:143], v[80:87], v112\n"
    "v_exp_f32 v90, v90\n"
    "v_exp_f32 v91, v91\n"
    "s_xor_b32 s53, s89, 61440\n"
    "s_mov_b64 exec, s[46:47]\n"
    "buffer_load_dwordx4 v[140:143], v248, s[12:15], s53 offen offset:0 sc1\n"
    "s_mov_b64 exec, -1\n"
    "v_exp_f32 v92, v92\n"
    "v_exp_f32 v93, v93\n"
    "v_exp_f32 v94, v94\n"
    "v_exp_f32 v95, v95\n"
    "v_exp_f32 v96, v96\n"
    "v_exp_f32 v97, v97\n"
    "buffer_load_dwordx4 v[240:243], v2, s[12:15], s72 offen offset:2048 sc1\n"
    "v_smfmac_f32_32x32x32_bf16 v[64:79], v[144:147], v[88:95], v112\n"
    "v_exp_f32 v98, v98\n"
    "v_exp_f32 v99, v99\n"
    "s_setprio 1\n"
    "v_exp_f32 v100, v100\n"
    "v_mfma_f32_32x32x16_bf16 v[80:95], v[4:7], v[120:123], 0\n"
    "v_exp_f32 v101, v101\n"
    "s_xor_b32 s53, s89, 61440\n"
    "s_mov_b64 exec, s[46:47]\n"
    "buffer_load_dwordx4 v[144:147], v248, s[12:15], s53 offen offset:256 sc1\n"
    "s_mov_b64 exec, -1\n"
    "v_exp_f32 v102, v102\n"
    "v_exp_f32 v103, v103\n"
    "v_lshrrev_b32 v251, 4, v1\n"
    "v_and_b32 v250, 0xf0, v2\n"
    "v_lshlrev_b32 v250, 1, v250\n"
    "v_lshl_or_b32 v250, v251, 12, v250\n"
    "s_lshl_b32 s54, s3, 9\n"
    "v_add_u32 v254, s54, v250\n"
    "s_lshl_b32 s54, s39, 14\n"
    "s_lshl_b32 s55, s38, 9\n"
    "s_add_u32 s54, s54, s55\n"
    "s_add_u32 s54, s54, 88064\n"
    "v_add_u32 v250, s54, v250\n"
    "s_lshl_b32 s54, s39, 9\n"
    "s_lshl2_add_u32 s55, s38, s54\n"
    "s_add_u32 s55, s55, 65536\n"
    "v_and_b32 v251, 0x1f0, v2\n"
    "v_add_u32 v251, s55, v251\n"
    "v_lshlrev_b32 v253, 2, v1\n"
    "v_and_b32 v0, 0xf0, v253\n"
    "s_and_b32 s55, s38, 1\n"
    "s_lshl_b32 s55, s55, 8\n"
    "s_add_u32 s54, s54, s55\n"
    "s_add_u32 s54, s54, 65536\n"
    "v_add_u32 v0, s54, v0\n"
    "s_add_u32 s54, s78, 32768\n"
    "v_lshl_add_u32 v252, v1, 3, s54\n"
    "v_lshl_or_b32 v253, v1, 30, v253\n"
    "v_exp_f32 v104, v104\n"
    "v_exp_f32 v105, v105\n"
    "v_smfmac_f32_32x32x32_bf16 v[16:31], v[152:155], v[96:103], v112\n"
    "v_exp_f32 v106, v106\n"
    "v_exp_f32 v107, v107\n"
    "buffer_load_dwordx4 v[244:247], v2, s[12:15], s72 offen offset:3072 sc1\n"
    "v_exp_f32 v108, v108\n"
    "v_exp_f32 v109, v109\n"
    "v_exp_f32 v110, v110\n"
    "v_exp_f32 v111, v111\n"
    "v_exp_f32 v80, v80\n"
    "v_exp_f32 v81, v81\n"
    "s_add_u32 s84, s38, 0\n"
    "s_and_b32 s84, s84, 3\n"
    "s_lshl_b32 s84, s84, 12\n"
    "s_add_u32 s84, s84, s66\n"
    "buffer_load_dwordx4 v[184:187], v249, s[12:15], s84 offen offset:0 sc1\n"
    "v_smfmac_f32_32x32x32_bf16 v[16:31], v[156:159], v[104:111], v112\n"
    "v_exp_f32 v82, v82\n"
    "v_exp_f32 v83, v83\n"
    "v_exp_f32 v84, v84\n"
    "v_mfma_f32_32x32x16_bf16 v[96:111], v[160:163], v[120:123], 0\n"
    "v_exp_f32 v85, v85\n"
    "s_add_u32 s84, s38, 0\n"
    "s_and_b32 s84, s84, 3\n"
    "s_lshl_b32 s84, s84, 12\n"
    "s_add_u32 s84, s84, s66\n"
    "buffer_load_dwordx4 v[188:191], v249, s[12:15], s84 offen offset:1024 sc1\n"
    "v_exp_f32 v86, v86\n"
    "v_exp_f32 v87, v87\n"
    "v_exp_f32 v88, v88\n"
    "v_exp_f32 v89, v89\n"
    "v_smfmac_f32_32x32x32_bf16 v[16:31], v[8:11], v[80:87], v112\n"
    "v_exp_f32 v90, v90\n"
    "s_setprio 0\n"
    "v_exp_f32 v91, v91\n"
    "s_add_u32 s84, s38, 0\n"
    "s_and_b32 s84, s84, 3\n"
    "s_lshl_b32 s84, s84, 12\n"
    "s_add_u32 s84, s84, s66\n"
    "buffer_load_dwordx4 v[192:195], v249, s[12:15], s84 offen offset:2048 sc1\n"
    "v_exp_f32 v92, v92\n"
    "v_exp_f32 v93, v93\n"
    "v_exp_f32 v94, v94\n"
    "v_exp_f32 v95, v95\n"
    "v_exp_f32 v96, v96\n"
    "v_exp_f32 v97, v97\n"
    "s_add_u32 s84, s38, 0\n"
    "s_and_b32 s84, s84, 3\n"
    "s_lshl_b32 s84, s84, 12\n"
    "s_add_u32 s84, s84, s66\n"
    "buffer_load_dwordx4 v[196:199], v249, s[12:15], s84 offen offset:3072 sc1\n"
    "v_smfmac_f32_32x32x32_bf16 v[16:31], v[12:15], v[88:95], v112\n"
    "v_exp_f32 v98, v98\n"
    "v_exp_f32 v99, v99\n"
    "s_setprio 3\n"
    "v_exp_f32 v100, v100\n"
    "v_mfma_f32_32x32x16_bf16 v[80:95], v[172:175], v[120:123], 0\n"
    "v_exp_f32 v101, v101\n"
    "s_add_u32 s84, s38, 1\n"
    "s_and_b32 s84, s84, 3\n"
    "s_lshl_b32 s84, s84, 12\n"
    "s_add_u32 s84, s84, s66\n"
    "buffer_load_dwordx4 v[200:203], v249, s[12:15], s84 offen offset:0 sc1\n"
    "v_exp_f32 v102, v102\n"
    "v_exp_f32 v103, v103\n"
    "v_exp_f32 v104, v104\n"
    "v_exp_f32 v105, v105\n"
    "v_smfmac_f32_32x32x32_bf16 v[16:31], v[164:167], v[96:103], v112\n"
    "v_exp_f32 v106, v106\n"
    "v_exp_f32 v107, v107\n"
    "v_exp_f32 v108, v108\n"
    "v_exp_f32 v109, v109\n"
    "v_exp_f32 v110, v110\n"
    "v_exp_f32 v111, v111\n"
    "v_exp_f32 v80, v80\n"
    "v_exp_f32 v81, v81\n"
    "s_add_u32 s84, s38, 1\n"
    "s_and_b32 s84, s84, 3\n"
    "s_lshl_b32 s84, s84, 12\n"
    "s_add_u32 s84, s84, s66\n"
    "buffer_load_dwordx4 v[204:207], v249, s[12:15], s84 offen offset:1024 sc1\n"
    "v_smfmac_f32_32x32x32_bf16 v[16:31], v[168:171], v[104:111], v112\n"
    "v_exp_f32 v82, v82\n"
    "v_exp_f32 v83, v83\n"
    "s_setprio 2\n"
    "v_exp_f32 v84, v84\n"
    "s_waitcnt vmcnt(12)\n"
    "v_mfma_f32_32x32x16_bf16 v[96:111], v[136:139], v[120:123], 0\n"
    "v_exp_f32 v85, v85\n"
    "s_add_u32 s84, s38, 1\n"
    "s_and_b32 s84, s84, 3\n"
    "s_lshl_b32 s84, s84, 12\n"
    "s_add_u32 s84, s84, s66\n"
    "buffer_load_dwordx4 v[208:211], v249, s[12:15], s84 offen offset:2048 sc1\n"
    "v_exp_f32 v86, v86\n"
    "v_exp_f32 v87, v87\n"
    "v_exp_f32 v88, v88\n"
    "v_exp_f32 v89, v89\n"
    "v_smfmac_f32_32x32x32_bf16 v[16:31], v[176:179], v[80:87], v112\n"
    "v_exp_f32 v90, v90\n"
    "v_exp_f32 v91, v91\n"
    "s_add_u32 s84, s38, 1\n"
    "s_and_b32 s84, s84, 3\n"
    "s_lshl_b32 s84, s84, 12\n"
    "s_add_u32 s84, s84, s66\n"
    "buffer_load_dwordx4 v[212:215], v249, s[12:15], s84 offen offset:3072 sc1\n"
    "v_exp_f32 v92, v92\n"
    "v_exp_f32 v93, v93\n"
    "v_exp_f32 v94, v94\n"
    "v_exp_f32 v95, v95\n"
    "v_exp_f32 v96, v96\n"
    "v_exp_f32 v97, v97\n"
    "s_add_u32 s84, s38, 2\n"
    "s_and_b32 s84, s84, 3\n"
    "s_lshl_b32 s84, s84, 12\n"
    "s_add_u32 s84, s84, s66\n"
    "buffer_load_dwordx4 v[216:219], v249, s[12:15], s84 offen offset:0 sc1\n"
    "v_smfmac_f32_32x32x32_bf16 v[16:31], v[180:183], v[88:95], v112\n"
    "v_exp_f32 v98, v98\n"
    "v_exp_f32 v99, v99\n"
    "s_setprio 1\n"
    "v_exp_f32 v100, v100\n"
    "v_mfma_f32_32x32x16_bf16 v[80:95], v[136:139], v[124:127], 0\n"
    "v_exp_f32 v101, v101\n"
    "s_xor_b32 s53, s89, 40960\n"
    "buffer_load_dwordx4 v[136:139], v2, s[12:15], s53 offen sc1\n"
    "v_exp_f32 v102, v102\n"
    "v_exp_f32 v103, v103\n"
    "v_exp_f32 v104, v104\n"
    "v_exp_f32 v105, v105\n"
    "s_waitcnt vmcnt(13)\n"
    "v_smfmac_f32_32x32x32_bf16 v[16:31], v[140:143], v[96:103], v112\n"
    "v_exp_f32 v106, v106\n"
    "v_exp_f32 v107, v107\n"
    "s_add_u32 s84, s38, 2\n"
    "s_and_b32 s84, s84, 3\n"
    "s_lshl_b32 s84, s84, 12\n"
    "s_add_u32 s84, s84, s66\n"
    "buffer_load_dwordx4 v[220:223], v249, s[12:15], s84 offen offset:1024 sc1\n"
    "v_exp_f32 v108, v108\n"
    "v_exp_f32 v109, v109\n"
    "v_exp_f32 v110, v110\n"
    "v_exp_f32 v111, v111\n"
    "v_exp_f32 v80, v80\n"
    "v_exp_f32 v81, v81\n"
    "s_add_u32 s84, s38, 2\n"
    "s_and_b32 s84, s84, 3\n"
    "s_lshl_b32 s84, s84, 12\n"
    "s_add_u32 s84, s84, s66\n"
    "buffer_load_dwordx4 v[224:227], v249, s[12:15], s84 offen offset:2048 sc1\n"
    "s_waitcnt vmcnt(13)\n"
    "v_smfmac_f32_32x32x32_bf16 v[16:31], v[144:147], v[104:111], v112\n"
    "v_exp_f32 v82, v82\n"
    "v_exp_f32 v83, v83\n"
    "v_exp_f32 v84, v84\n"
    "v_mfma_f32_32x32x16_bf16 v[96:111], v[172:175], v[124:127], 0\n"
    "v_exp_f32 v85, v85\n"
    "s_add_u32 s84, s38, 2\n"
    "s_and_b32 s84, s84, 3\n"
    "s_lshl_b32 s84, s84, 12\n"
    "s_add_u32 s84, s84, s66\n"
    "buffer_load_dwordx4 v[228:231], v249, s[12:15], s84 offen offset:3072 sc1\n"
    "v_exp_f32 v86, v86\n"
    "v_exp_f32 v87, v87\n"
    "v_exp_f32 v88, v88\n"
    "ds_write_b128 v250, v[16:19] offset:0\n"
    "v_exp_f32 v89, v89\n"
    "v_smfmac_f32_32x32x32_bf16 v[32:47], v[140:143], v[80:87], v112\n"
    "v_exp_f32 v90, v90\n"
    "ds_write_b128 v250, v[20:23] offset:16\n"
    "s_setprio 0\n"
    "v_exp_f32 v91, v91\n"
    "s_xor_b32 s53, s89, 40960\n"
    "s_mov_b64 exec, s[46:47]\n"
    "buffer_load_dwordx4 v[140:143], v248, s[12:15], s53 offen offset:0 sc1\n"
    "s_mov_b64 exec, -1\n"
    "v_exp_f32 v92, v92\n"
    "v_exp_f32 v93, v93\n"
    "ds_write_b32 v251, v24 offset:0\n"
    "v_exp_f32 v94, v94\n"
    "v_exp_f32 v95, v95\n"
    "v_exp_f32 v96, v96\n"
    "v_exp_f32 v97, v97\n"
    "s_add_u32 m0, s57, 120832\n"
    "s_add_u32 s87, s67, 123136\n"
    "buffer_load_dwordx4 v119, s[12:15], s87 offen sc1 lds\n"
    "v_smfmac_f32_32x32x32_bf16 v[32:47], v[144:147], v[88:95], v112\n"
    "s_waitcnt lgkmcnt(0)\n"
    "s_barrier\n"
    "ds_read_b128 v[24:27], v0 offset:0\n"
    "ds_read_b64 v[16:17], v252 offset:55296\n"
    "ds_read_b64 v[18:19], v252 offset:55808\n"
    "ds_read_b64 v[20:21], v252 offset:56320\n"
    "ds_read_b64 v[22:23], v252 offset:56832\n"
    "v_exp_f32 v98, v98\n"
    "v_exp_f32 v99, v99\n"
    "s_setprio 1\n"
    "v_exp_f32 v100, v100\n"
    "v_mfma_f32_32x32x16_bf16 v[80:95], v[160:163], v[124:127], 0\n"
    "v_exp_f32 v101, v101\n"
    "s_xor_b32 s53, s89, 40960\n"
    "s_mov_b64 exec, s[46:47]\n"
    "buffer_load_dwordx4 v[144:147], v248, s[12:15], s53 offen offset:256 sc1\n"
    "s_mov_b64 exec, -1\n"
    "v_exp_f32 v102, v102\n"
    "s_waitcnt lgkmcnt(4)\n"
    "v_add_f32 v24, v24, v25\n"
    "v_add_f32 v26, v26, v27\n"
    "v_exp_f32 v103, v103\n"
    "s_waitcnt lgkmcnt(2)\n"
    "v_add_f32 v16, v16, v18\n"
    "v_add_f32 v24, v24, v26\n"
    "v_add_f32 v17, v17, v19\n"
    "v_rcp_f32 v25, v24\n"
    "v_exp_f32 v104, v104\n"
    "s_waitcnt lgkmcnt(0)\n"
    "v_add_f32 v20, v20, v22\n"
    "v_add_f32 v21, v21, v23\n"
    "v_exp_f32 v105, v105\n"
    "v_add_f32 v16, v16, v20\n"
    "v_add_f32 v17, v17, v21\n"
    "v_smfmac_f32_32x32x32_bf16 v[32:47], v[176:179], v[96:103], v112\n"
    "v_fma_f32 v24, v16, 0, v24\n"
    "v_mul_f32 v16, v16, v25\n"
    "v_fma_f32 v24, v17, 0, v24\n"
    "v_mul_f32 v17, v17, v25\n"
    "v_exp_f32 v106, v106\n"
    "v_subrev_u32 v24, 394264576, v24\n"
    "v_cvt_pk_bf16_f32 v20, v16, v17\n"
    "v_exp_f32 v107, v107\n"
    "v_min_u32 v20, -2, v20\n"
    "s_nop 1\n"
    "v_mov_b32_dpp v21, v20 quad_perm:[1,1,1,1] row_mask:0xf bank_mask:0xf\n"
    "v_mov_b32_dpp v22, v20 quad_perm:[2,2,2,2] row_mask:0xf bank_mask:0xf\n"
    "v_mov_b32_dpp v23, v20 quad_perm:[3,3,3,3] row_mask:0xf bank_mask:0xf\n"
    "v_exp_f32 v108, v108\n"
    "v_cmp_lt_u32 vcc, 1509949440, v24\n"
    "s_add_u32 s56, s68, 0\n"
    "s_cbranch_vccnz fb_23_%=\n"
    "buffer_store_dwordx4 v[20:23], v253, s[12:15], s56 offen sc1\n"
    "fr_24_%=:\n"
    "v_exp_f32 v109, v109\n"
    "v_and_b32 v123, 0x3c0, v2\n"
    "v_and_b32 v122, 4, v253\n"
    "v_and_b32 v121, 2, v1\n"
    "v_lshl_or_b32 v123, v121, 3, v123\n"
    "s_and_b32 s54, s3, 1\n"
    "s_lshl_b32 s54, s54, 10\n"
    "s_lshl_b32 s55, s39, 5\n"
    "s_add_u32 s54, s54, s55\n"
    "s_and_b32 s55, s3, 2\n"
    "s_lshl2_add_u32 s54, s55, s54\n"
    "s_add_u32 s54, s54, 71680\n"
    "v_add3_u32 v123, v123, v122, s54\n"
    "ds_read2st64_b32 v[120:121], v123 offset0:0 offset1:8\n"
    "ds_read2st64_b32 v[122:123], v123 offset0:16 offset1:24\n"
    "v_exp_f32 v110, v110\n"
    "v_exp_f32 v111, v111\n"
    "v_exp_f32 v80, v80\n"
    "v_exp_f32 v81, v81\n"
    "s_add_u32 m0, s57, 121856\n"
    "s_add_u32 s87, s67, 10240\n"
    "buffer_load_dwordx4 v119, s[12:15], s87 offen sc1 lds\n"
    "v_smfmac_f32_32x32x32_bf16 v[32:47], v[180:183], v[104:111], v112\n"
    "v_exp_f32 v82, v82\n"
    "v_exp_f32 v83, v83\n"
    "v_exp_f32 v84, v84\n"
    "v_mfma_f32_32x32x16_bf16 v[96:111], v[4:7], v[124:127], 0\n"
    "v_exp_f32 v85, v85\n"
    "s_add_u32 m0, s57, 122880\n"
    "s_add_u32 s87, s67, 10496\n"
    "buffer_load_dwordx4 v119, s[12:15], s87 offen sc1 lds\n"
    "v_exp_f32 v86, v86\n"
    "v_exp_f32 v87, v87\n"
    "v_exp_f32 v88, v88\n"
    "v_exp_f32 v89, v89\n"
    "v_smfmac_f32_32x32x32_bf16 v[32:47], v[164:167], v[80:87], v112\n"
    "v_exp_f32 v90, v90\n"
    "s_setprio 0\n"
    "v_exp_f32 v91, v91\n"
    "s_add_u32 m0, s57, 123904\n"
    "s_add_u32 s87, s67, 124928\n"
    "buffer_load_dwordx4 v119, s[12:15], s87 offen sc1 lds\n"
    "v_exp_f32 v92, v92\n"
    "v_exp_f32 v93, v93\n"
    "v_exp_f32 v94, v94\n"
    "v_exp_f32 v95, v95\n"
    "v_exp_f32 v96, v96\n"
    "v_exp_f32 v97, v97\n"
    "s_add_u32 m0, s57, 124928\n"
    "s_add_u32 s87, s67, 125184\n"
    "buffer_load_dwordx4 v119, s[12:15], s87 offen sc1 lds\n"
    "v_smfmac_f32_32x32x32_bf16 v[32:47], v[168:171], v[88:95], v112\n"
    "v_exp_f32 v98, v98\n"
    "v_exp_f32 v99, v99\n"
    "s_setprio 3\n"
    "v_exp_f32 v100, v100\n"
    "v_mfma_f32_32x32x16_bf16 v[80:95], v[148:151], v[124:127], 0\n"
    "v_exp_f32 v101, v101\n"
    "v_exp_f32 v102, v102\n"
    "v_exp_f32 v103, v103\n"
    "v_exp_f32 v104, v104\n"
    "v_exp_f32 v105, v105\n"
    "v_smfmac_f32_32x32x32_bf16 v[32:47], v[8:11], v[96:103], v112\n"
    "v_exp_f32 v106, v106\n"
    "v_exp_f32 v107, v107\n"
    "v_exp_f32 v108, v108\n"
    "v_exp_f32 v109, v109\n"
    "v_exp_f32 v110, v110\n"
    "v_exp_f32 v111, v111\n"
    "v_exp_f32 v80, v80\n"
    "v_exp_f32 v81, v81\n"
    "v_smfmac_f32_32x32x32_bf16 v[32:47], v[12:15], v[104:111], v112\n"
    "v_exp_f32 v82, v82\n"
    "v_exp_f32 v83, v83\n"
    "s_setprio 2\n"
    "v_exp_f32 v84, v84\n"
    "s_waitcnt vmcnt(11)\n"
    "v_mfma_f32_32x32x16_bf16 v[96:111], v[136:139], v[124:127], 0\n"
    "v_exp_f32 v85, v85\n"
    "v_exp_f32 v86, v86\n"
    "v_exp_f32 v87, v87\n"
    "v_exp_f32 v88, v88\n"
    "v_exp_f32 v89, v89\n"
    "v_smfmac_f32_32x32x32_bf16 v[32:47], v[152:155], v[80:87], v112\n"
    "v_exp_f32 v90, v90\n"
    "v_exp_f32 v91, v91\n"
    "v_exp_f32 v92, v92\n"
    "v_exp_f32 v93, v93\n"
    "v_exp_f32 v94, v94\n"
    "v_exp_f32 v95, v95\n"
    "v_exp_f32 v96, v96\n"
    "v_exp_f32 v97, v97\n"
    "v_smfmac_f32_32x32x32_bf16 v[32:47], v[156:159], v[88:95], v112\n"
    "v_exp_f32 v98, v98\n"
    "v_exp_f32 v99, v99\n"
    "s_setprio 1\n"
    "v_exp_f32 v100, v100\n"
    "v_mfma_f32_32x32x16_bf16 v[80:95], v[136:139], v[128:131], 0\n"
    "v_exp_f32 v101, v101\n"
    "s_xor_b32 s53, s89, 61440\n"
    "buffer_load_dwordx4 v[136:139], v2, s[12:15], s53 offen sc1\n"
    "v_exp_f32 v102, v102\n"
    "v_exp_f32 v103, v103\n"
    "v_exp_f32 v104, v104\n"
    "v_exp_f32 v105, v105\n"
    "s_waitcnt vmcnt(8)\n"
    "v_smfmac_f32_32x32x32_bf16 v[32:47], v[140:143], v[96:103], v112\n"
    "v_exp_f32 v106, v106\n"
    "v_exp_f32 v107, v107\n"
    "v_exp_f32 v108, v108\n"
    "v_exp_f32 v109, v109\n"
    "v_exp_f32 v110, v110\n"
    "v_exp_f32 v111, v111\n"
    "v_exp_f32 v80, v80\n"
    "v_exp_f32 v81, v81\n"
    "s_waitcnt vmcnt(6)\n"
    "v_smfmac_f32_32x32x32_bf16 v[32:47], v[144:147], v[104:111], v112\n"
    "v_exp_f32 v82, v82\n"
    "v_exp_f32 v83, v83\n"
    "v_exp_f32 v84, v84\n"
    "v_mfma_f32_32x32x16_bf16 v[96:111], v[148:151], v[128:131], 0\n"
    "v_exp_f32 v85, v85\n"
    "v_exp_f32 v86, v86\n"
    "v_exp_f32 v87, v87\n"
    "v_exp_f32 v88, v88\n"
    "s_nop 3\n"
    "ds_write_b128 v250, v[32:35] offset:2048\n"
    "v_exp_f32 v89, v89\n"
    "v_smfmac_f32_32x32x32_bf16 v[48:63], v[140:143], v[80:87], v112\n"
    "v_exp_f32 v90, v90\n"
    "ds_write_b128 v250, v[36:39] offset:2064\n"
    "s_setprio 0\n"
    "v_exp_f32 v91, v91\n"
    "s_xor_b32 s53, s89, 61440\n"
    "s_mov_b64 exec, s[46:47]\n"
    "buffer_load_dwordx4 v[140:143], v248, s[12:15], s53 offen offset:0 sc1\n"
    "s_mov_b64 exec, -1\n"
    "v_exp_f32 v92, v92\n"
    "v_exp_f32 v93, v93\n"
    "ds_write_b32 v251, v40 offset:1024\n"
    "v_exp_f32 v94, v94\n"
    "v_exp_f32 v95, v95\n"
    "v_exp_f32 v96, v96\n"
    "v_exp_f32 v97, v97\n"
    "v_smfmac_f32_32x32x32_bf16 v[48:63], v[144:147], v[88:95], v112\n"
    "s_waitcnt lgkmcnt(0)\n"
    "s_barrier\n"
    "ds_read_b128 v[40:43], v0 offset:1024\n"
    "ds_read_b64 v[32:33], v252 offset:57344\n"
    "ds_read_b64 v[34:35], v252 offset:57856\n"
    "ds_read_b64 v[36:37], v252 offset:58368\n"
    "ds_read_b64 v[38:39], v252 offset:58880\n"
    "v_exp_f32 v98, v98\n"
    "v_exp_f32 v99, v99\n"
    "s_setprio 1\n"
    "v_exp_f32 v100, v100\n"
    "v_mfma_f32_32x32x16_bf16 v[80:95], v[4:7], v[128:131], 0\n"
    "v_exp_f32 v101, v101\n"
    "s_xor_b32 s53, s89, 61440\n"
    "s_mov_b64 exec, s[46:47]\n"
    "buffer_load_dwordx4 v[144:147], v248, s[12:15], s53 offen offset:256 sc1\n"
    "s_mov_b64 exec, -1\n"
    "v_exp_f32 v102, v102\n"
    "s_waitcnt lgkmcnt(4)\n"
    "v_add_f32 v40, v40, v41\n"
    "v_add_f32 v42, v42, v43\n"
    "v_exp_f32 v103, v103\n"
    "s_waitcnt lgkmcnt(2)\n"
    "v_add_f32 v32, v32, v34\n"
    "v_add_f32 v40, v40, v42\n"
    "v_add_f32 v33, v33, v35\n"
    "v_rcp_f32 v41, v40\n"
    "v_exp_f32 v104, v104\n"
    "s_waitcnt lgkmcnt(0)\n"
    "v_add_f32 v36, v36, v38\n"
    "v_add_f32 v37, v37, v39\n"
    "v_exp_f32 v105, v105\n"
    "v_add_f32 v32, v32, v36\n"
    "v_add_f32 v33, v33, v37\n"
    "v_smfmac_f32_32x32x32_bf16 v[48:63], v[152:155], v[96:103], v112\n"
    "v_fma_f32 v40, v32, 0, v40\n"
    "v_mul_f32 v32, v32, v41\n"
    "v_fma_f32 v40, v33, 0, v40\n"
    "v_mul_f32 v33, v33, v41\n"
    "v_exp_f32 v106, v106\n"
    "v_subrev_u32 v40, 394264576, v40\n"
    "v_cvt_pk_bf16_f32 v36, v32, v33\n"
    "v_exp_f32 v107, v107\n"
    "v_min_u32 v36, -2, v36\n"
    "s_nop 1\n"
    "v_mov_b32_dpp v37, v36 quad_perm:[1,1,1,1] row_mask:0xf bank_mask:0xf\n"
    "v_mov_b32_dpp v38, v36 quad_perm:[2,2,2,2] row_mask:0xf bank_mask:0xf\n"
    "v_mov_b32_dpp v39, v36 quad_perm:[3,3,3,3] row_mask:0xf bank_mask:0xf\n"
    "v_exp_f32 v108, v108\n"
    "v_cmp_lt_u32 vcc, 1509949440, v40\n"
    "s_add_u32 s56, s68, 32768\n"
    "s_cbranch_vccnz fb_27_%=\n"
    "buffer_store_dwordx4 v[36:39], v253, s[12:15], s56 offen sc1\n"
    "fr_28_%=:\n"
    "v_exp_f32 v109, v109\n"
    "v_lshrrev_b32 v124, 2, v1\n"
    "v_exp_f32 v110, v110\n"
    "v_exp_f32 v111, v111\n"
    "v_exp_f32 v80, v80\n"
    "v_xor_b32 v124, v124, v1\n"
    "v_exp_f32 v81, v81\n"
    "v_smfmac_f32_32x32x32_bf16 v[48:63], v[156:159], v[104:111], v112\n"
    "v_bfe_u32 v124, v124, 2, 1\n"
    "v_exp_f32 v82, v82\n"
    "v_exp_f32 v83, v83\n"
    "v_add_u32 v124, -1, v124\n"
    "v_exp_f32 v84, v84\n"
    "v_mfma_f32_32x32x16_bf16 v[96:111], v[160:163], v[128:131], 0\n"
    "v_and_b32 v124, s62, v124\n"
    "v_exp_f32 v85, v85\n"
    "s_add_u32 s85, s73, 0\n"
    "buffer_load_dwordx4 v[32:35], v2, s[12:15], s85 offen offset:0 sc1\n"
    "v_exp_f32 v86, v86\n"
    "v_mov_b32 v125, v124\n"
    "v_exp_f32 v87, v87\n"
    "v_exp_f32 v88, v88\n"
    "v_mov_b32 v126, v124\n"
    "v_exp_f32 v89, v89\n"
    "v_smfmac_f32_32x32x32_bf16 v[48:63], v[8:11], v[80:87], v112\n"
    "v_mov_b32 v127, v124\n"
    "v_exp_f32 v90, v90\n"
    "s_setprio 0\n"
    "v_exp_f32 v91, v91\n"
    "s_add_u32 s85, s73, 0\n"
    "buffer_load_dwordx4 v[36:39], v2, s[12:15], s85 offen offset:1024 sc1\n"
    "v_exp_f32 v92, v92\n"
    "v_exp_f32 v93, v93\n"
    "v_exp_f32 v94, v94\n"
    "v_exp_f32 v95, v95\n"
    "v_exp_f32 v96, v96\n"
    "v_exp_f32 v97, v97\n"
    "s_add_u32 s85, s73, 0\n"
    "buffer_load_dwordx4 v[40:43], v2, s[12:15], s85 offen offset:2048 sc1\n"
    "v_smfmac_f32_32x32x32_bf16 v[48:63], v[12:15], v[88:95], v112\n"
    "v_exp_f32 v98, v98\n"
    "v_exp_f32 v99, v99\n"
    "s_setprio 3\n"
    "v_exp_f32 v100, v100\n"
    "v_mfma_f32_32x32x16_bf16 v[80:95], v[172:175], v[128:131], 0\n"
    "v_exp_f32 v101, v101\n"
    "s_add_u32 s85, s73, 0\n"
    "buffer_load_dwordx4 v[44:47], v2, s[12:15], s85 offen offset:3072 sc1\n"
    "v_exp_f32 v102, v102\n"
    "v_exp_f32 v103, v103\n"
    "v_exp_f32 v104, v104\n"
    "v_exp_f32 v105, v105\n"
    "v_smfmac_f32_32x32x32_bf16 v[48:63], v[164:167], v[96:103], v112\n"
    "v_exp_f32 v106, v106\n"
    "v_exp_f32 v107, v107\n"
    "v_exp_f32 v108, v108\n"
    "v_exp_f32 v109, v109\n"
    "v_exp_f32 v110, v110\n"
    "v_exp_f32 v111, v111\n"
    "v_exp_f32 v80, v80\n"
    "v_exp_f32 v81, v81\n"
    "v_smfmac_f32_32x32x32_bf16 v[48:63], v[168:171], v[104:111], v112\n"
    "v_exp_f32 v82, v82\n"
    "v_exp_f32 v83, v83\n"
    "s_setprio 2\n"
    "v_exp_f32 v84, v84\n"
    "s_waitcnt vmcnt(7)\n"
    "v_mfma_f32_32x32x16_bf16 v[96:111], v[136:139], v[128:131], 0\n"
    "v_exp_f32 v85, v85\n"
    "v_exp_f32 v86, v86\n"
    "v_exp_f32 v87, v87\n"
    "v_exp_f32 v88, v88\n"
    "v_exp_f32 v89, v89\n"
    "v_smfmac_f32_32x32x32_bf16 v[48:63], v[176:179], v[80:87], v112\n"
    "v_exp_f32 v90, v90\n"
    "v_exp_f32 v91, v91\n"
    "v_exp_f32 v92, v92\n"
    "s_waitcnt vmcnt(3)\n"
    "v_mfma_f32_32x32x16_bf16 v[16:31], v[232:235], v[32:35], 0\n"
    "v_exp_f32 v93, v93\n"
    "v_exp_f32 v94, v94\n"
    "v_exp_f32 v95, v95\n"
    "v_exp_f32 v96, v96\n"
    "v_exp_f32 v97, v97\n"
    "s_waitcnt vmcnt(2)\n"
    "v_mfma_f32_32x32x16_bf16 v[16:31], v[236:239], v[36:39], v[16:31]\n"
    "v_smfmac_f32_32x32x32_bf16 v[48:63], v[180:183], v[88:95], v112\n"
    "v_exp_f32 v98, v98\n"
    "v_exp_f32 v99, v99\n"
    "s_setprio 1\n"
    "v_exp_f32 v100, v100\n"
    "v_mfma_f32_32x32x16_bf16 v[80:95], v[136:139], v[132:135], 0\n"
    "s_waitcnt vmcnt(1)\n"
    "v_mfma_f32_32x32x16_bf16 v[16:31], v[240:243], v[40:43], v[16:31]\n"
    "v_exp_f32 v101, v101\n"
    "s_xor_b32 s53, s89, 40960\n"
    "buffer_load_dwordx4 v[136:139], v2, s[12:15], s53 offen sc1\n"
    "v_exp_f32 v102, v102\n"
    "v_exp_f32 v103, v103\n"
    "v_exp_f32 v104, v104\n"
    "v_exp_f32 v105, v105\n"
    "s_waitcnt vmcnt(1)\n"
    "v_mfma_f32_32x32x16_bf16 v[16:31], v[244:247], v[44:47], v[16:31]\n"
    "v_smfmac_f32_32x32x32_bf16 v[48:63], v[140:143], v[96:103], v112\n"
    "v_exp_f32 v106, v106\n"
    "v_exp_f32 v107, v107\n"
    "v_exp_f32 v108, v108\n"
    "v_exp_f32 v109, v109\n"
    "v_exp_f32 v110, v110\n"
    "s_nop 5\n"
    "v_cmp_u_f32 vcc, v16, v16\n"
    "s_cbranch_vccnz op_35_%=\n"
    "ob_36_%=:\n"
    "v_exp_f32 v111, v111\n"
    "ds_write_b128 v254, v[16:19] offset:0\n"
    "v_exp_f32 v80, v80\n"
    "v_exp_f32 v81, v81\n"
    "v_smfmac_f32_32x32x32_bf16 v[48:63], v[144:147], v[104:111], v112\n"
    "v_exp_f32 v82, v82\n"
    "v_exp_f32 v83, v83\n"
    "ds_write_b128 v254, v[20:23] offset:16\n"
    "v_exp_f32 v84, v84\n"
    "v_mfma_f32_32x32x16_bf16 v[96:111], v[172:175], v[132:135], 0\n"
    "v_exp_f32 v85, v85\n"
    "v_exp_f32 v86, v86\n"
    "v_exp_f32 v87, v87\n"
    "ds_write_b128 v254, v[24:27] offset:16384\n"
    "v_exp_f32 v88, v88\n"
    "s_nop 1\n"
    "ds_write_b128 v250, v[48:51] offset:0\n"
    "v_exp_f32 v89, v89\n"
    "v_smfmac_f32_32x32x32_bf16 v[64:79], v[140:143], v[80:87], v112\n"
    "v_exp_f32 v90, v90\n"
    "ds_write_b128 v250, v[52:55] offset:16\n"
    "s_setprio 0\n"
    "v_exp_f32 v91, v91\n"
    "ds_write_b128 v254, v[28:31] offset:16400\n"
    "s_xor_b32 s53, s89, 40960\n"
    "s_mov_b64 exec, s[46:47]\n"
    "buffer_load_dwordx4 v[140:143], v248, s[12:15], s53 offen offset:0 sc1\n"
    "s_mov_b64 exec, -1\n"
    "v_exp_f32 v92, v92\n"
    "v_exp_f32 v93, v93\n"
    "ds_write_b32 v251, v56 offset:0\n"
    "v_exp_f32 v94, v94\n"
    "v_exp_f32 v95, v95\n"
    "v_exp_f32 v96, v96\n"
    "v_exp_f32 v97, v97\n"
    "s_add_u32 s85, s73, 32768\n"
    "buffer_load_dwordx4 v[32:35], v2, s[12:15], s85 offen offset:0 sc1\n"
    "v_smfmac_f32_32x32x32_bf16 v[64:79], v[144:147], v[88:95], v112\n"
    "s_waitcnt lgkmcnt(0)\n"
    "s_barrier\n"
    "v_subrev_u32 v30, 32768, v252\n"
    "ds_read_b64 v[16:17], v30 offset:0\n"
    "ds_read_b64 v[18:19], v30 offset:512\n"
    "ds_read_b64 v[20:21], v30 offset:1024\n"
    "ds_read_b64 v[22:23], v30 offset:1536\n"
    "ds_read_b64 v[24:25], v30 offset:2048\n"
    "ds_read_b64 v[26:27], v30 offset:2560\n"
    "ds_read_b64 v[28:29], v30 offset:3072\n"
    "ds_read_b64 v[30:31], v30 offset:3584\n"
    "v_exp_f32 v98, v98\n"
    "v_exp_f32 v99, v99\n"
    "ds_read_b128 v[56:59], v0 offset:0\n"
    "v_exp_f32 v100, v100\n"
    "v_mfma_f32_32x32x16_bf16 v[80:95], v[160:163], v[132:135], 0\n"
    "v_exp_f32 v101, v101\n"
    "ds_read_b64 v[48:49], v252 offset:55296\n"
    "s_add_u32 s85, s73, 32768\n"
    "buffer_load_dwordx4 v[36:39], v2, s[12:15], s85 offen offset:1024 sc1\n"
    "v_exp_f32 v102, v102\n"
    "s_waitcnt lgkmcnt(8)\n"
    "v_add_f32 v16, v16, v18\n"
    "v_add_f32 v17, v17, v19\n"
    "v_exp_f32 v103, v103\n"
    "ds_read_b64 v[50:51], v252 offset:55808\n"
    "s_waitcnt lgkmcnt(7)\n"
    "v_add_f32 v20, v20, v22\n"
    "v_add_f32 v21, v21, v23\n"
    "s_waitcnt lgkmcnt(5)\n"
    "v_add_f32 v24, v24, v26\n"
    "v_add_f32 v25, v25, v27\n"
    "v_exp_f32 v104, v104\n"
    "s_waitcnt lgkmcnt(3)\n"
    "v_add_f32 v28, v28, v30\n"
    "v_add_f32 v29, v29, v31\n"
    "v_exp_f32 v105, v105\n"
    "ds_read_b64 v[52:53], v252 offset:56320\n"
    "v_add_f32 v16, v16, v20\n"
    "v_add_f32 v17, v17, v21\n"
    "v_smfmac_f32_32x32x32_bf16 v[64:79], v[176:179], v[96:103], v112\n"
    "v_add_f32 v24, v24, v28\n"
    "v_add_f32 v25, v25, v29\n"
    "v_add_f32 v16, v16, v24\n"
    "v_add_f32 v17, v17, v25\n"
    "v_exp_f32 v106, v106\n"
    "ds_read_b64 v[54:55], v252 offset:56832\n"
    "v_lshlrev_b32 v18, 16, v120\n"
    "v_and_b32 v19, s42, v120\n"
    "s_setprio 0\n"
    "v_exp_f32 v107, v107\n"
    "v_add_f32 v16, v16, v18\n"
    "v_add_f32 v17, v17, v19\n"
    "s_add_u32 s85, s73, 32768\n"
    "buffer_load_dwordx4 v[40:43], v2, s[12:15], s85 offen offset:2048 sc1\n"
    "v_cvt_pk_bf16_f32 v16, v16, v17\n"
    "v_min_u32 v16, -2, v16\n"
    "v_exp_f32 v108, v108\n"
    "s_nop 0\n"
    "v_mov_b32_dpp v17, v16 quad_perm:[1,1,1,1] row_mask:0xf bank_mask:0xf\n"
    "v_mov_b32_dpp v18, v16 quad_perm:[2,2,2,2] row_mask:0xf bank_mask:0xf\n"
    "v_exp_f32 v109, v109\n"
    "v_mov_b32_dpp v19, v16 quad_perm:[3,3,3,3] row_mask:0xf bank_mask:0xf\n"
    "s_add_u32 s56, s69, 0\n"
    "buffer_store_dwordx4 v[16:19], v253, s[12:15], s56 offen sc1\n"
    "s_waitcnt lgkmcnt(4)\n"
    "v_add_f32 v56, v56, v57\n"
    "v_add_f32 v58, v58, v59\n"
    "v_exp_f32 v110, v110\n"
    "s_waitcnt lgkmcnt(2)\n"
    "v_add_f32 v48, v48, v50\n"
    "v_add_f32 v56, v56, v58\n"
    "v_exp_f32 v111, v111\n"
    "v_add_f32 v49, v49, v51\n"
    "v_rcp_f32 v57, v56\n"
    "v_exp_f32 v80, v80\n"
    "s_waitcnt lgkmcnt(0)\n"
    "v_add_f32 v52, v52, v54\n"
    "v_add_f32 v53, v53, v55\n"
    "v_exp_f32 v81, v81\n"
    "v_add_f32 v48, v48, v52\n"
    "v_add_f32 v49, v49, v53\n"
    "v_fma_f32 v56, v48, 0, v56\n"
    "v_mul_f32 v48, v48, v57\n"
    "s_add_u32 s85, s73, 32768\n"
    "buffer_load_dwordx4 v[44:47], v2, s[12:15], s85 offen offset:3072 sc1\n"
    "v_smfmac_f32_32x32x32_bf16 v[64:79], v[180:183], v[104:111], v112\n"
    "v_fma_f32 v56, v49, 0, v56\n"
    "v_mul_f32 v49, v49, v57\n"
    "v_subrev_u32 v56, 394264576, v56\n"
    "v_cvt_pk_bf16_f32 v52, v48, v49\n"
    "v_exp_f32 v82, v82\n"
    "v_min_u32 v52, -2, v52\n"
    "s_nop 1\n"
    "v_mov_b32_dpp v53, v52 quad_perm:[1,1,1,1] row_mask:0xf bank_mask:0xf\n"
    "v_exp_f32 v83, v83\n"
    "v_mov_b32_dpp v54, v52 quad_perm:[2,2,2,2] row_mask:0xf bank_mask:0xf\n"
    "v_mov_b32_dpp v55, v52 quad_perm:[3,3,3,3] row_mask:0xf bank_mask:0xf\n"
    "s_setprio 3\n"
    "v_cmp_lt_u32 vcc, 1509949440, v56\n"
    "s_add_u32 s56, s68, 65536\n"
    "s_cbranch_vccnz fb_31_%=\n"
    "buffer_store_dwordx4 v[52:55], v253, s[12:15], s56 offen sc1\n"
    "fr_32_%=:\n"
    "v_exp_f32 v84, v84\n"
    "v_mfma_f32_32x32x16_bf16 v[96:111], v[4:7], v[132:135], 0\n"
    "v_mfma_f32_16x16x32_bf16 v[128:131], v[124:127], v[184:187], 0\n"
    "v_exp_f32 v85, v85\n"
    "s_xor_b32 s53, s89, 40960\n"
    "s_mov_b64 exec, s[46:47]\n"
    "buffer_load_dwordx4 v[144:147], v248, s[12:15], s53 offen offset:256 sc1\n"
    "s_mov_b64 exec, -1\n"
    "v_exp_f32 v86, v86\n"
    "v_exp_f32 v87, v87\n"
    "v_exp_f32 v88, v88\n"
    "v_exp_f32 v89, v89\n"
    "v_smfmac_f32_32x32x32_bf16 v[64:79], v[164:167], v[80:87], v112\n"
    "v_mfma_f32_16x16x32_bf16 v[128:131], v[124:127], v[188:191], v[128:131]\n"
    "v_exp_f32 v90, v90\n"
    "v_exp_f32 v91, v91\n"
    "s_add_u32 s84, s38, 3\n"
    "s_and_b32 s84, s84, 3\n"
    "s_lshl_b32 s84, s84, 12\n"
    "s_add_u32 s84, s84, s66\n"
    "buffer_load_dwordx4 v[48:51], v249, s[12:15], s84 offen offset:0 sc1\n"
    "v_exp_f32 v92, v92\n"
    "v_exp_f32 v93, v93\n"
    "v_exp_f32 v94, v94\n"
    "v_mfma_f32_16x16x32_bf16 v[128:131], v[124:127], v[192:195], v[128:131]\n"
    "v_exp_f32 v95, v95\n"
    "v_exp_f32 v96, v96\n"
    "v_exp_f32 v97, v97\n"
    "s_add_u32 s84, s38, 3\n"
    "s_and_b32 s84, s84, 3\n"
    "s_lshl_b32 s84, s84, 12\n"
    "s_add_u32 s84, s84, s66\n"
    "buffer_load_dwordx4 v[52:55], v249, s[12:15], s84 offen offset:1024 sc1\n"
    "v_smfmac_f32_32x32x32_bf16 v[64:79], v[168:171], v[88:95], v112\n"
    "v_exp_f32 v98, v98\n"
    "v_exp_f32 v99, v99\n"
    "v_mfma_f32_16x16x32_bf16 v[128:131], v[124:127], v[196:199], v[128:131]\n"
    "s_setprio 2\n"
    "v_exp_f32 v100, v100\n"
    "v_mfma_f32_32x32x16_bf16 v[80:95], v[148:151], v[132:135], 0\n"
    "v_exp_f32 v101, v101\n"
    "s_add_u32 s84, s38, 3\n"
    "s_and_b32 s84, s84, 3\n"
    "s_lshl_b32 s84, s84, 12\n"
    "s_add_u32 s84, s84, s66\n"
    "buffer_load_dwordx4 v[56:59], v249, s[12:15], s84 offen offset:2048 sc1\n"
    "v_exp_f32 v102, v102\n"
    "v_exp_f32 v103, v103\n"
    "v_mfma_f32_16x16x32_bf16 v[128:131], v[124:127], v[200:203], v[128:131]\n"
    "v_exp_f32 v104, v104\n"
    "v_exp_f32 v105, v105\n"
    "v_smfmac_f32_32x32x32_bf16 v[64:79], v[8:11], v[96:103], v112\n"
    "s_waitcnt vmcnt(9)\n"
    "v_mfma_f32_32x32x16_bf16 v[16:31], v[232:235], v[32:35], 0\n"
    "v_exp_f32 v106, v106\n"
    "v_exp_f32 v107, v107\n"
    "s_add_u32 s84, s38, 3\n"
    "s_and_b32 s84, s84, 3\n"
    "s_lshl_b32 s84, s84, 12\n"
    "s_add_u32 s84, s84, s66\n"
    "buffer_load_dwordx4 v[60:63], v249, s[12:15], s84 offen offset:3072 sc1\n"
    "v_exp_f32 v108, v108\n"
    "v_mfma_f32_16x16x32_bf16 v[128:131], v[124:127], v[204:207], v[128:131]\n"
    "v_exp_f32 v109, v109\n"
    "v_exp_f32 v110, v110\n"
    "s_waitcnt vmcnt(9)\n"
    "v_mfma_f32_32x32x16_bf16 v[16:31], v[236:239], v[36:39], v[16:31]\n"
    "v_exp_f32 v111, v111\n"
    "v_exp_f32 v80, v80\n"
    "v_exp_f32 v81, v81\n"
    "v_smfmac_f32_32x32x32_bf16 v[64:79], v[12:15], v[104:111], v112\n"
    "v_mfma_f32_16x16x32_bf16 v[128:131], v[124:127], v[208:211], v[128:131]\n"
    "v_exp_f32 v82, v82\n"
    "s_waitcnt vmcnt(8)\n"
    "v_mfma_f32_32x32x16_bf16 v[16:31], v[240:243], v[40:43], v[16:31]\n"
    "v_exp_f32 v83, v83\n"
    "s_setprio 1\n"
    "v_exp_f32 v84, v84\n"
    "v_mfma_f32_32x32x16_bf16 v[96:111], v[136:139], v[132:135], 0\n"
    "v_exp_f32 v85, v85\n"
    "v_mfma_f32_16x16x32_bf16 v[128:131], v[124:127], v[212:215], v[128:131]\n"
    "v_exp_f32 v86, v86\n"
    "s_waitcnt vmcnt(6)\n"
    "v_mfma_f32_32x32x16_bf16 v[16:31], v[244:247], v[44:47], v[16:31]\n"
    "v_exp_f32 v87, v87\n"
    "v_exp_f32 v88, v88\n"
    "v_exp_f32 v89, v89\n"
    "v_smfmac_f32_32x32x32_bf16 v[64:79], v[152:155], v[80:87], v112\n"
    "v_exp_f32 v90, v90\n"
    "v_mfma_f32_16x16x32_bf16 v[128:131], v[124:127], v[216:219], v[128:131]\n"
    "v_exp_f32 v91, v91\n"
    "s_nop 4\n"
    "v_cmp_u_f32 vcc, v16, v16\n"
    "s_cbranch_vccnz op_38_%=\n"
    "ob_39_%=:\n"
    "v_exp_f32 v92, v92\n"
    "ds_write_b128 v254, v[16:19] offset:32768\n"
    "v_exp_f32 v93, v93\n"
    "v_exp_f32 v94, v94\n"
    "v_exp_f32 v95, v95\n"
    "v_exp_f32 v96, v96\n"
    "v_mfma_f32_16x16x32_bf16 v[128:131], v[124:127], v[220:223], v[128:131]\n"
    "v_exp_f32 v97, v97\n"
    "ds_write_b128 v254, v[20:23] offset:32784\n"
    "v_smfmac_f32_32x32x32_bf16 v[64:79], v[156:159], v[88:95], v112\n"
    "v_exp_f32 v98, v98\n"
    "v_exp_f32 v99, v99\n"
    "v_exp_f32 v100, v100\n"
    "v_mfma_f32_16x16x32_bf16 v[128:131], v[124:127], v[224:227], v[128:131]\n"
    "ds_write_b128 v254, v[24:27] offset:49152\n"
    "v_exp_f32 v101, v101\n"
    "v_exp_f32 v102, v102\n"
    "v_exp_f32 v103, v103\n"
    "v_exp_f32 v104, v104\n"
    "v_exp_f32 v105, v105\n"
    "v_mfma_f32_16x16x32_bf16 v[128:131], v[124:127], v[228:231], v[128:131]\n"
    "ds_write_b128 v254, v[28:31] offset:49168\n"
    "v_smfmac_f32_32x32x32_bf16 v[64:79], v[140:143], v[96:103], v112\n"
    "v_exp_f32 v106, v106\n"
    "s_setprio 0\n"
    "v_exp_f32 v107, v107\n"
    "v_exp_f32 v108, v108\n"
    "v_exp_f32 v109, v109\n"
    "v_exp_f32 v110, v110\n"
    "v_exp_f32 v111, v111\n"
    "s_waitcnt vmcnt(4)\n"
    "s_nop 0\n"
    "v_smfmac_f32_32x32x32_bf16 v[64:79], v[144:147], v[104:111], v112\n"
    "Ltail_%=:\n"
    "s_setprio 0\n"
    "s_nop 10\n"
    "ds_write_b128 v250, v[64:67] offset:2048\n"
    "ds_write_b128 v250, v[68:71] offset:2064\n"
    "ds_write_b32 v251, v72 offset:1024\n"
    "v_add_u32 v3, s78, v2\n"
    "s_add_u32 m0, s78, 0\n"
    "s_add_u32 s87, s74, 0\n"
    "buffer_load_dwordx4 v2, s[12:15], s87 offen offset:0 sc1 lds\n"
    "s_add_u32 m0, s78, 0\n"
    "s_add_u32 s87, s74, 0\n"
    "buffer_load_dwordx4 v2, s[12:15], s87 offen offset:1024 sc1 lds\n"
    "s_add_u32 m0, s78, 0\n"
    "s_add_u32 s87, s74, 0\n"
    "buffer_load_dwordx4 v2, s[12:15], s87 offen offset:2048 sc1 lds\n"
    "s_add_u32 m0, s78, 0\n"
    "s_add_u32 s87, s74, 0\n"
    "buffer_load_dwordx4 v2, s[12:15], s87 offen offset:3072 sc1 lds\n"
    "s_waitcnt lgkmcnt(0)\n"
    "s_barrier\n"
    "ds_read_b128 v[72:75], v0 offset:1024\n"
    "ds_read_b64 v[64:65], v252 offset:57344\n"
    "ds_read_b64 v[66:67], v252 offset:57856\n"
    "ds_read_b64 v[68:69], v252 offset:58368\n"
    "ds_read_b64 v[70:71], v252 offset:58880\n"
    "ds_read_b64 v[16:17], v252 offset:0\n"
    "ds_read_b64 v[18:19], v252 offset:512\n"
    "ds_read_b64 v[20:21], v252 offset:1024\n"
    "ds_read_b64 v[22:23], v252 offset:1536\n"
    "ds_read_b64 v[24:25], v252 offset:2048\n"
    "ds_read_b64 v[26:27], v252 offset:2560\n"
    "ds_read_b64 v[28:29], v252 offset:3072\n"
    "ds_read_b64 v[30:31], v252 offset:3584\n"
    "s_add_u32 s85, s73, 65536\n"
    "buffer_load_dwordx4 v[32:35], v2, s[12:15], s85 offen offset:0 sc1\n"
    "s_waitcnt vmcnt(8)\n"
    "v_mfma_f32_16x16x32_bf16 v[128:131], v[124:127], v[48:51], v[128:131]\n"
    "v_add_u32 v4, s20, v2\n"
    "v_add_u32 v5, s21, v2\n"
    "buffer_load_dwordx4 v[36:39], v2, s[12:15], s85 offen offset:1024 sc1\n"
    "s_waitcnt vmcnt(8)\n"
    "v_mfma_f32_16x16x32_bf16 v[128:131], v[124:127], v[52:55], v[128:131]\n"
    "v_add_u32 v6, s22, v2\n"
    "v_add_u32 v10, 67584, v2\n"
    "s_waitcnt lgkmcnt(12)\n"
    "v_pk_add_f32 v[72:73], v[72:73], v[74:75]\n"
    "s_waitcnt lgkmcnt(10)\n"
    "v_pk_add_f32 v[64:65], v[64:65], v[66:67]\n"
    "v_add_f32 v72, v72, v73\n"
    "s_waitcnt lgkmcnt(8)\n"
    "v_pk_add_f32 v[68:69], v[68:69], v[70:71]\n"
    "v_rcp_f32 v73, v72\n"
    "v_pk_add_f32 v[64:65], v[64:65], v[68:69]\n"
    "v_fma_f32 v72, v64, 0, v72\n"
    "v_mul_f32 v64, v64, v73\n"
    "v_fma_f32 v72, v65, 0, v72\n"
    "v_mul_f32 v65, v65, v73\n"
    "v_subrev_u32 v72, 394264576, v72\n"
    "v_cvt_pk_bf16_f32 v68, v64, v65\n"
    "v_min_u32 v68, -2, v68\n"
    "s_nop 1\n"
    "v_mov_b32_dpp v69, v68 quad_perm:[1,1,1,1] row_mask:0xf bank_mask:0xf\n"
    "v_mov_b32_dpp v70, v68 quad_perm:[2,2,2,2] row_mask:0xf bank_mask:0xf\n"
    "v_mov_b32_dpp v71, v68 quad_perm:[3,3,3,3] row_mask:0xf bank_mask:0xf\n"
    "v_cmp_lt_u32 vcc, 1509949440, v72\n"
    "s_add_u32 s56, s68, 98304\n"
    "s_cbranch_vccnz fb_41_%=\n"
    "buffer_store_dwordx4 v[68:71], v253, s[12:15], s56 offen sc1\n"
    "fr_42_%=:\n"
    "buffer_load_dwordx4 v[40:43], v2, s[12:15], s85 offen offset:2048 sc1\n"
    "s_waitcnt vmcnt(9)\n"
    "v_mfma_f32_16x16x32_bf16 v[128:131], v[124:127], v[56:59], v[128:131]\n"
    "v_add_u32 v9, s23, v2\n"
    "v_add_u32 v7, s4, v2\n"
    "buffer_load_dwordx4 v[44:47], v2, s[12:15], s85 offen offset:3072 sc1\n"
    "s_waitcnt vmcnt(9)\n"
    "v_mfma_f32_16x16x32_bf16 v[128:131], v[124:127], v[60:63], v[128:131]\n"
    "v_add_u32 v8, s5, v2\n"
    "v_add_u32 v14, s6, v117\n"
    "s_waitcnt lgkmcnt(6)\n"
    "v_pk_add_f32 v[16:17], v[16:17], v[18:19]\n"
    "s_waitcnt lgkmcnt(4)\n"
    "v_pk_add_f32 v[20:21], v[20:21], v[22:23]\n"
    "s_waitcnt lgkmcnt(2)\n"
    "v_pk_add_f32 v[24:25], v[24:25], v[26:27]\n"
    "s_waitcnt lgkmcnt(0)\n"
    "v_pk_add_f32 v[28:29], v[28:29], v[30:31]\n"
    "v_pk_add_f32 v[16:17], v[16:17], v[20:21]\n"
    "v_pk_add_f32 v[24:25], v[24:25], v[28:29]\n"
    "v_pk_add_f32 v[16:17], v[16:17], v[24:25]\n"
    "v_lshlrev_b32 v18, 16, v121\n"
    "v_and_b32 v19, s42, v121\n"
    "v_add_f32 v16, v16, v18\n"
    "v_add_f32 v17, v17, v19\n"
    "v_cvt_pk_bf16_f32 v16, v16, v17\n"
    "v_min_u32 v16, -2, v16\n"
    "s_nop 1\n"
    "v_mov_b32_dpp v17, v16 quad_perm:[1,1,1,1] row_mask:0xf bank_mask:0xf\n"
    "v_mov_b32_dpp v18, v16 quad_perm:[2,2,2,2] row_mask:0xf bank_mask:0xf\n"
    "v_mov_b32_dpp v19, v16 quad_perm:[3,3,3,3] row_mask:0xf bank_mask:0xf\n"
    "s_add_u32 s56, s69, 32768\n"
    "buffer_store_dwordx4 v[16:19], v253, s[12:15], s56 offen sc1\n"
    "s_waitcnt lgkmcnt(0)\n"
    "s_barrier\n"
    "v_mov_b32 v248, v119\n"
    "buffer_load_dwordx2 v[132:133], v118, s[24:27], s7 offen\n"
    "buffer_load_dwordx2 v[134:135], v118, s[24:27], s7 offen offset:16\n"
    "v_cndmask_b32 v128, v128, 0, s[60:61]\n"
    "v_mov_b32 v172, 0\n"
    "v_cvt_pk_bf16_f32 v170, v128, v128\n"
    "v_mov_b32 v173, 0\n"
    "v_and_b32 v171, s42, v170\n"
    "v_sub_f32 v171, v128, v171\n"
    "v_cvt_pk_bf16_f32 v171, v171, 0\n"
    "s_waitcnt vmcnt(8)\n"
    "ds_read_b128 v[136:139], v3 offset:0\n"
    "ds_read_b128 v[140:143], v3 offset:1024\n"
    "ds_read_b128 v[144:147], v3 offset:2048\n"
    "ds_read_b128 v[148:151], v3 offset:3072\n"
    "s_waitcnt lgkmcnt(3)\n"
    "v_mfma_f32_4x4x4_16b_bf16 v[176:179], v[136:137], v[136:137], 0\n"
    "v_mfma_f32_16x16x32_bf16 v[180:183], v[124:127], v[136:139], 0\n"
    "s_nop 0\n"
    "v_mfma_f32_4x4x4_16b_bf16 v[176:179], v[138:139], v[138:139], v[176:179]\n"
    "s_waitcnt vmcnt(7)\n"
    "v_mfma_f32_32x32x16_bf16 v[16:31], v[232:235], v[32:35], 0\n"
    "s_waitcnt lgkmcnt(2)\n"
    "v_mfma_f32_4x4x4_16b_bf16 v[176:179], v[140:141], v[140:141], v[176:179]\n"
    "v_mfma_f32_16x16x32_bf16 v[180:183], v[124:127], v[140:143], v[180:183]\n"
    "s_nop 0\n"
    "v_mfma_f32_4x4x4_16b_bf16 v[176:179], v[142:143], v[142:143], v[176:179]\n"
    "s_waitcnt vmcnt(6)\n"
    "v_mfma_f32_32x32x16_bf16 v[16:31], v[236:239], v[36:39], v[16:31]\n"
    "s_waitcnt lgkmcnt(1)\n"
    "v_mfma_f32_4x4x4_16b_bf16 v[176:179], v[144:145], v[144:145], v[176:179]\n"
    "v_mfma_f32_16x16x32_bf16 v[180:183], v[124:127], v[144:147], v[180:183]\n"
    "s_nop 0\n"
    "v_mfma_f32_4x4x4_16b_bf16 v[176:179], v[146:147], v[146:147], v[176:179]\n"
    "s_waitcnt vmcnt(4)\n"
    "v_mfma_f32_32x32x16_bf16 v[16:31], v[240:243], v[40:43], v[16:31]\n"
    "s_waitcnt lgkmcnt(0)\n"
    "v_mfma_f32_4x4x4_16b_bf16 v[176:179], v[148:149], v[148:149], v[176:179]\n"
    "v_mfma_f32_16x16x32_bf16 v[180:183], v[124:127], v[148:151], v[180:183]\n"
    "s_nop 0\n"
    "v_mfma_f32_4x4x4_16b_bf16 v[176:179], v[150:151], v[150:151], v[176:179]\n"
    "s_waitcnt vmcnt(3)\n"
    "v_mfma_f32_32x32x16_bf16 v[16:31], v[244:247], v[44:47], v[16:31]\n"
    "s_nop 3\n"
    "v_cmp_u_f32 vcc, v180, v180\n"
    "s_cbranch_vccnz hp_45_%=\n"
    "hb_46_%=:\n"
    "v_cndmask_b32 v176, v176, v177, s[8:9]\n"
    "v_cndmask_b32 v178, v178, v179, s[8:9]\n"
    "v_cndmask_b32 v176, v176, v178, s[10:11]\n"
    "v_mov_b32 v177, v176\n"
    "s_nop 1\n"
    "v_permlane32_swap_b32 v176, v177\n"
    "v_add_f32 v176, v176, v177\n"
    "v_cndmask_b32 v176, v180, v176, s[60:61]\n"
    "ds_write_b32 v9, v176 offset:0\n"
    "v_cmp_u_f32 vcc, v16, v16\n"
    "s_cbranch_vccnz op_48_%=\n"
    "ob_49_%=:\n"
    "ds_write_b128 v254, v[16:19] offset:32768\n"
    "ds_write_b128 v254, v[20:23] offset:32784\n"
    "ds_write_b128 v254, v[24:27] offset:49152\n"
    "ds_write_b128 v254, v[28:31] offset:49168\n"
    "s_waitcnt lgkmcnt(0)\n"
    "s_barrier\n"
    "s_cmp_lg_u32 s39, 0\n"
    "s_cbranch_scc1 a1_51_%=\n"
    "s_setprio 1\n"
    "ds_read_b128 v[96:99], v4 offset:0\n"
    "ds_read_b128 v[100:103], v4 offset:1024\n"
    "ds_read_b64 v[16:17], v252 offset:0\n"
    "s_waitcnt lgkmcnt(0)\n"
    "s_add_u32 m0, s78, 32768\n"
    "s_add_u32 s87, s74, 32768\n"
    "buffer_load_dwordx4 v2, s[12:15], s87 offen offset:0 sc1 lds\n"
    "v_mfma_f32_32x32x16_bf16 v[80:95], v[184:187], v[136:139], 0\n"
    "ds_read_b64 v[18:19], v252 offset:512\n"
    "ds_read_b64 v[20:21], v252 offset:1024\n"
    "ds_read_b64 v[22:23], v252 offset:1536\n"
    "ds_read_b64 v[24:25], v252 offset:2048\n"
    "ds_read_b64 v[26:27], v252 offset:2560\n"
    "ds_read_b64 v[28:29], v252 offset:3072\n"
    "ds_read_b64 v[30:31], v252 offset:3584\n"
    "ds_read_b128 v[104:107], v4 offset:2048\n"
    "v_mfma_f32_32x32x16_bf16 v[80:95], v[188:191], v[140:143], v[80:95]\n"
    "buffer_load_dwordx4 v2, s[12:15], s87 offen offset:1024 sc1 lds\n"
    "ds_read_b128 v[108:111], v4 offset:3072\n"
    "v_mfma_f32_32x32x16_bf16 v[80:95], v[192:195], v[144:147], v[80:95]\n"
    "buffer_load_dwordx4 v2, s[12:15], s87 offen offset:2048 sc1 lds\n"
    "s_waitcnt lgkmcnt(8)\n"
    "v_add_f32 v16, v16, v18\n"
    "v_mfma_f32_32x32x16_bf16 v[80:95], v[196:199], v[148:151], v[80:95]\n"
    "v_add_f32 v17, v17, v19\n"
    "s_waitcnt lgkmcnt(6)\n"
    "v_add_f32 v20, v20, v22\n"
    "v_add_f32 v21, v21, v23\n"
    "s_waitcnt lgkmcnt(4)\n"
    "v_add_f32 v24, v24, v26\n"
    "v_add_f32 v25, v25, v27\n"
    "v_mfma_f32_32x32x16_bf16 v[80:95], v[200:203], v[96:99], v[80:95]\n"
    "ds_read_b128 v[96:99], v5 offset:0\n"
    "s_waitcnt lgkmcnt(3)\n"
    "v_add_f32 v28, v28, v30\n"
    "v_add_f32 v29, v29, v31\n"
    "v_add_f32 v16, v16, v20\n"
    "buffer_load_dwordx4 v2, s[12:15], s87 offen offset:3072 sc1 lds\n"
    "v_add_f32 v17, v17, v21\n"
    "v_add_f32 v24, v24, v28\n"
    "v_add_f32 v25, v25, v29\n"
    "v_mfma_f32_32x32x16_bf16 v[80:95], v[204:207], v[100:103], v[80:95]\n"
    "ds_read_b128 v[100:103], v5 offset:1024\n"
    "v_add_f32 v16, v16, v24\n"
    "v_add_f32 v17, v17, v25\n"
    "v_lshlrev_b32 v18, 16, v122\n"
    "v_and_b32 v19, s42, v122\n"
    "v_add_f32 v16, v16, v18\n"
    "v_add_f32 v17, v17, v19\n"
    "s_waitcnt lgkmcnt(3)\n"
    "v_mfma_f32_32x32x16_bf16 v[80:95], v[208:211], v[104:107], v[80:95]\n"
    "ds_read_b128 v[104:107], v5 offset:2048\n"
    "v_cvt_pk_bf16_f32 v16, v16, v17\n"
    "v_min_u32 v16, -2, v16\n"
    "s_nop 1\n"
    "v_mov_b32_dpp v17, v16 quad_perm:[1,1,1,1] row_mask:0xf bank_mask:0xf\n"
    "v_mov_b32_dpp v18, v16 quad_perm:[2,2,2,2] row_mask:0xf bank_mask:0xf\n"
    "v_mov_b32_dpp v19, v16 quad_perm:[3,3,3,3] row_mask:0xf bank_mask:0xf\n"
    "s_add_u32 s56, s69, 65536\n"
    "buffer_store_dwordx4 v[16:19], v253, s[12:15], s56 offen sc1\n"
    "ds_read_b128 v[176:179], v10 offset:0\n"
    "ds_read_b128 v[180:183], v10 offset:1024\n"
    "s_waitcnt lgkmcnt(5)\n"
    "v_mfma_f32_32x32x16_bf16 v[80:95], v[212:215], v[108:111], v[80:95]\n"
    "ds_read_b128 v[108:111], v5 offset:3072\n"
    "s_add_u32 s85, s73, 98304\n"
    "buffer_load_dwordx4 v[32:35], v2, s[12:15], s85 offen offset:0 sc1\n"
    "s_waitcnt lgkmcnt(5)\n"
    "v_mfma_f32_32x32x16_bf16 v[80:95], v[216:219], v[96:99], v[80:95]\n"
    "ds_read_b128 v[96:99], v6 offset:0\n"
    "s_waitcnt lgkmcnt(5)\n"
    "v_mfma_f32_32x32x16_bf16 v[80:95], v[220:223], v[100:103], v[80:95]\n"
    "ds_read_b128 v[100:103], v6 offset:1024\n"
    "s_add_u32 s85, s73, 98304\n"
    "buffer_load_dwordx4 v[36:39], v2, s[12:15], s85 offen offset:1024 sc1\n"
    "s_waitcnt lgkmcnt(3)\n"
    "v_add_f32 v176, v176, v180\n"
    "v_mfma_f32_32x32x16_bf16 v[80:95], v[224:227], v[104:107], v[80:95]\n"
    "ds_read_b128 v[104:107], v6 offset:2048\n"
    "v_add_f32 v177, v177, v181\n"
    "v_add_f32 v178, v178, v182\n"
    "v_add_f32 v179, v179, v183\n"
    "s_add_u32 s85, s73, 98304\n"
    "buffer_load_dwordx4 v[40:43], v2, s[12:15], s85 offen offset:2048 sc1\n"
    "s_waitcnt lgkmcnt(3)\n"
    "v_mfma_f32_32x32x16_bf16 v[80:95], v[228:231], v[108:111], v[80:95]\n"
    "ds_read_b128 v[108:111], v6 offset:3072\n"
    "v_add_f32 v176, v176, v177\n"
    "v_add_f32 v178, v178, v179\n"
    "v_add_f32 v176, v176, v178\n"
    "v_mov_b32 v177, v176\n"
    "s_waitcnt lgkmcnt(3)\n"
    "v_mfma_f32_32x32x16_bf16 v[80:95], v[48:51], v[96:99], v[80:95]\n"
    "v_permlane32_swap_b32 v176, v177\n"
    "v_mul_f32 v176, 0xbb000000, v176\n"
    "s_add_u32 s85, s73, 98304\n"
    "buffer_load_dwordx4 v[44:47], v2, s[12:15], s85 offen offset:3072 sc1\n"
    "v_mul_f32 v177, 0x3b000000, v177\n"
    "v_cvt_pk_bf16_f32 v169, v176, v176\n"
    "ds_read_b32 v120, v14 offset:0\n"
    "s_waitcnt lgkmcnt(3)\n"
    "v_mfma_f32_32x32x16_bf16 v[80:95], v[52:55], v[100:103], v[80:95]\n"
    "v_fma_f32 v177, -v176, v176, v177\n"
    "v_and_b32 v178, s42, v169\n"
    "v_max_f32 v177, 0, v177\n"
    "v_sub_f32 v178, v176, v178\n"
    "s_waitcnt lgkmcnt(2)\n"
    "v_mfma_f32_32x32x16_bf16 v[80:95], v[56:59], v[104:107], v[80:95]\n"
    "v_add_f32 v177, 0x3727c5ac, v177\n"
    "v_cvt_pk_bf16_f32 v168, v176, v178\n"
    "v_rsq_f32 v177, v177\n"
    "s_nop 0\n"
    "v_mul_f32 v11, s88, v177\n"
    "s_waitcnt lgkmcnt(1)\n"
    "v_mfma_f32_32x32x16_bf16 v[80:95], v[60:63], v[108:111], v[80:95]\n"
    "v_mfma_f32_32x32x16_bf16 v[80:95], v[170:173], v[168:171], v[80:95]\n"
    "s_setprio 0\n"
    "s_waitcnt vmcnt(10)\n"
    "v_lshlrev_b32 v128, 16, v132\n"
    "s_add_u32 s84, s67, 0\n"
    "buffer_load_dwordx4 v[72:75], v248, s[12:15], s84 offen sc1\n"
    "v_and_b32 v129, s42, v132\n"
    "v_lshlrev_b32 v130, 16, v133\n"
    "v_and_b32 v131, s42, v133\n"
    "s_waitcnt vmcnt(10)\n"
    "v_lshlrev_b32 v132, 16, v134\n"
    "v_and_b32 v133, s42, v134\n"
    "s_add_u32 s84, s67, 256\n"
    "buffer_load_dwordx4 v[76:79], v248, s[12:15], s84 offen sc1\n"
    "v_lshlrev_b32 v134, 16, v135\n"
    "v_and_b32 v135, s42, v135\n"
    "v_mul_f32 v128, s88, v128\n"
    "ds_write_b128 v7, v[88:91]\n"
    "v_mul_f32 v129, s88, v129\n"
    "v_mul_f32 v130, s88, v130\n"
    "s_add_u32 s84, s67, 114688\n"
    "buffer_load_dwordx4 v[112:115], v248, s[12:15], s84 offen sc1\n"
    "v_mul_f32 v131, s88, v131\n"
    "v_mul_f32 v132, s88, v132\n"
    "v_mul_f32 v133, s88, v133\n"
    "v_mul_f32 v134, s88, v134\n"
    "ds_write_b128 v7, v[92:95] offset:1024\n"
    "v_mul_f32 v135, s88, v135\n"
    "s_add_u32 s84, s67, 114944\n"
    "buffer_load_dwordx4 v[116:119], v248, s[12:15], s84 offen sc1\n"
    "s_waitcnt vmcnt(9)\n"
    "ds_read_b128 v[136:139], v3 offset:32768\n"
    "ds_read_b128 v[140:143], v3 offset:33792\n"
    "ds_read_b128 v[144:147], v3 offset:34816\n"
    "ds_read_b128 v[148:151], v3 offset:35840\n"
    "s_waitcnt vmcnt(7)\n"
    "v_mfma_f32_32x32x16_bf16 v[16:31], v[232:235], v[32:35], 0\n"
    "s_waitcnt lgkmcnt(3)\n"
    "v_mfma_f32_4x4x4_16b_bf16 v[176:179], v[136:137], v[136:137], 0\n"
    "v_mfma_f32_16x16x32_bf16 v[180:183], v[124:127], v[136:139], 0\n"
    "s_nop 0\n"
    "v_mfma_f32_4x4x4_16b_bf16 v[176:179], v[138:139], v[138:139], v[176:179]\n"
    "s_waitcnt vmcnt(6)\n"
    "v_mfma_f32_32x32x16_bf16 v[16:31], v[236:239], v[36:39], v[16:31]\n"
    "s_waitcnt lgkmcnt(2)\n"
    "v_mfma_f32_4x4x4_16b_bf16 v[176:179], v[140:141], v[140:141], v[176:179]\n"
    "v_mfma_f32_16x16x32_bf16 v[180:183], v[124:127], v[140:143], v[180:183]\n"
    "s_nop 0\n"
    "v_mfma_f32_4x4x4_16b_bf16 v[176:179], v[142:143], v[142:143], v[176:179]\n"
    "s_waitcnt vmcnt(5)\n"
    "v_mfma_f32_32x32x16_bf16 v[16:31], v[240:243], v[40:43], v[16:31]\n"
    "s_waitcnt lgkmcnt(1)\n"
    "v_mfma_f32_4x4x4_16b_bf16 v[176:179], v[144:145], v[144:145], v[176:179]\n"
    "v_mfma_f32_16x16x32_bf16 v[180:183], v[124:127], v[144:147], v[180:183]\n"
    "s_nop 0\n"
    "v_mfma_f32_4x4x4_16b_bf16 v[176:179], v[146:147], v[146:147], v[176:179]\n"
    "s_waitcnt vmcnt(4)\n"
    "v_mfma_f32_32x32x16_bf16 v[16:31], v[244:247], v[44:47], v[16:31]\n"
    "s_waitcnt lgkmcnt(0)\n"
    "v_mfma_f32_4x4x4_16b_bf16 v[176:179], v[148:149], v[148:149], v[176:179]\n"
    "v_mfma_f32_16x16x32_bf16 v[180:183], v[124:127], v[148:151], v[180:183]\n"
    "s_nop 0\n"
    "v_mfma_f32_4x4x4_16b_bf16 v[176:179], v[150:151], v[150:151], v[176:179]\n"
    "s_nop 6\n"
    "v_cmp_u_f32 vcc, v16, v16\n"
    "s_cbranch_vccnz op_53_%=\n"
    "ob_54_%=:\n"
    "v_add_u32 v254, 88064, v254\n"
    "ds_write_b128 v254, v[16:19] offset:0\n"
    "ds_write_b128 v254, v[20:23] offset:16\n"
    "ds_write_b128 v254, v[24:27] offset:16384\n"
    "ds_write_b128 v254, v[28:31] offset:16400\n"
    "v_mfma_f32_32x32x16_bf16 v[152:167], v[184:187], v[136:139], 0\n"
    "v_cmp_u_f32 vcc, v180, v180\n"
    "s_cbranch_vccnz hp_56_%=\n"
    "hb_57_%=:\n"
    "v_cndmask_b32 v176, v176, v177, s[8:9]\n"
    "v_cndmask_b32 v178, v178, v179, s[8:9]\n"
    "v_cndmask_b32 v176, v176, v178, s[10:11]\n"
    "v_mov_b32 v177, v176\n"
    "s_nop 1\n"
    "v_permlane32_swap_b32 v176, v177\n"
    "v_add_f32 v176, v176, v177\n"
    "v_cndmask_b32 v176, v180, v176, s[60:61]\n"
    "ds_write_b32 v9, v176 offset:2048\n"
    "v_mfma_f32_32x32x16_bf16 v[152:167], v[188:191], v[140:143], v[152:167]\n"
    "s_waitcnt lgkmcnt(0)\n"
    "s_barrier\n"
    "s_setprio 1\n"
    "ds_read_b128 v[96:99], v4 offset:32768\n"
    "ds_read_b128 v[100:103], v4 offset:33792\n"
    "ds_read_b64 v[16:17], v252 offset:55296\n"
    "s_add_u32 m0, s78, 0\n"
    "s_add_u32 s87, s74, 65536\n"
    "buffer_load_dwordx4 v2, s[12:15], s87 offen offset:0 sc1 lds\n"
    "v_mfma_f32_32x32x16_bf16 v[152:167], v[192:195], v[144:147], v[152:167]\n"
    "ds_read_b64 v[18:19], v252 offset:55808\n"
    "ds_read_b64 v[20:21], v252 offset:56320\n"
    "ds_read_b64 v[22:23], v252 offset:56832\n"
    "ds_read_b64 v[24:25], v252 offset:57344\n"
    "ds_read_b64 v[26:27], v252 offset:57856\n"
    "ds_read_b64 v[28:29], v252 offset:58368\n"
    "ds_read_b64 v[30:31], v252 offset:58880\n"
    "ds_read_b128 v[104:107], v4 offset:34816\n"
    "v_mfma_f32_32x32x16_bf16 v[152:167], v[196:199], v[148:151], v[152:167]\n"
    "buffer_load_dwordx4 v2, s[12:15], s87 offen offset:1024 sc1 lds\n"
    "ds_read_b128 v[108:111], v4 offset:35840\n"
    "s_waitcnt lgkmcnt(11)\n"
    "v_mfma_f32_32x32x16_bf16 v[152:167], v[200:203], v[96:99], v[152:167]\n"
    "ds_read_b128 v[96:99], v5 offset:32768\n"
    "buffer_load_dwordx4 v2, s[12:15], s87 offen offset:2048 sc1 lds\n"
    "s_waitcnt lgkmcnt(9)\n"
    "v_add_f32 v16, v16, v18\n"
    "v_mfma_f32_32x32x16_bf16 v[152:167], v[204:207], v[100:103], v[152:167]\n"
    "ds_read_b128 v[100:103], v5 offset:33792\n"
    "v_add_f32 v17, v17, v19\n"
    "s_waitcnt lgkmcnt(8)\n"
    "v_add_f32 v20, v20, v22\n"
    "v_add_f32 v21, v21, v23\n"
    "s_waitcnt lgkmcnt(6)\n"
    "v_add_f32 v24, v24, v26\n"
    "v_add_f32 v25, v25, v27\n"
    "s_waitcnt lgkmcnt(3)\n"
    "v_mfma_f32_32x32x16_bf16 v[152:167], v[208:211], v[104:107], v[152:167]\n"
    "ds_read_b128 v[104:107], v5 offset:34816\n"
    "v_add_f32 v28, v28, v30\n"
    "v_add_f32 v29, v29, v31\n"
    "v_add_f32 v16, v16, v20\n"
    "v_add_f32 v17, v17, v21\n"
    "v_add_f32 v24, v24, v28\n"
    "v_add_f32 v25, v25, v29\n"
    "ds_read_b128 v[176:179], v10 offset:2048\n"
    "ds_read_b128 v[180:183], v10 offset:3072\n"
    "s_waitcnt lgkmcnt(5)\n"
    "v_mfma_f32_32x32x16_bf16 v[152:167], v[212:215], v[108:111], v[152:167]\n"
    "ds_read_b128 v[108:111], v5 offset:35840\n"
    "v_add_f32 v16, v16, v24\n"
    "v_add_f32 v17, v17, v25\n"
    "v_lshlrev_b32 v18, 16, v123\n"
    "v_and_b32 v19, s42, v123\n"
    "v_add_f32 v16, v16, v18\n"
    "v_add_f32 v17, v17, v19\n"
    "s_waitcnt lgkmcnt(5)\n"
    "v_mfma_f32_32x32x16_bf16 v[152:167], v[216:219], v[96:99], v[152:167]\n"
    "ds_read_b128 v[96:99], v6 offset:32768\n"
    "v_cvt_pk_bf16_f32 v16, v16, v17\n"
    "v_min_u32 v16, -2, v16\n"
    "s_nop 1\n"
    "v_mov_b32_dpp v17, v16 quad_perm:[1,1,1,1] row_mask:0xf bank_mask:0xf\n"
    "v_mov_b32_dpp v18, v16 quad_perm:[2,2,2,2] row_mask:0xf bank_mask:0xf\n"
    "v_mov_b32_dpp v19, v16 quad_perm:[3,3,3,3] row_mask:0xf bank_mask:0xf\n"
    "s_add_u32 s56, s69, 98304\n"
    "buffer_store_dwordx4 v[16:19], v253, s[12:15], s56 offen sc1\n"
    "s_waitcnt lgkmcnt(5)\n"
    "v_mfma_f32_32x32x16_bf16 v[152:167], v[220:223], v[100:103], v[152:167]\n"
    "ds_read_b128 v[100:103], v6 offset:33792\n"
    "buffer_load_dwordx4 v2, s[12:15], s87 offen offset:3072 sc1 lds\n"
    "s_waitcnt lgkmcnt(3)\n"
    "v_add_f32 v176, v176, v180\n"
    "v_mfma_f32_32x32x16_bf16 v[152:167], v[224:227], v[104:107], v[152:167]\n"
    "ds_read_b128 v[104:107], v6 offset:34816\n"
    "v_add_f32 v177, v177, v181\n"
    "v_add_f32 v178, v178, v182\n"
    "v_add_f32 v179, v179, v183\n"
    "s_waitcnt lgkmcnt(3)\n"
    "v_mfma_f32_32x32x16_bf16 v[152:167], v[228:231], v[108:111], v[152:167]\n"
    "ds_read_b128 v[108:111], v6 offset:35840\n"
    "v_add_f32 v176, v176, v177\n"
    "v_add_f32 v178, v178, v179\n"
    "v_add_f32 v176, v176, v178\n"
    "v_mov_b32 v177, v176\n"
    "s_waitcnt lgkmcnt(3)\n"
    "v_mfma_f32_32x32x16_bf16 v[152:167], v[48:51], v[96:99], v[152:167]\n"
    "v_permlane32_swap_b32 v176, v177\n"
    "v_mul_f32 v176, 0xbb000000, v176\n"
    "v_mul_f32 v177, 0x3b000000, v177\n"
    "v_cvt_pk_bf16_f32 v169, v176, v176\n"
    "ds_read_b128 v[64:67], v8\n"
    "s_waitcnt lgkmcnt(3)\n"
    "v_mfma_f32_32x32x16_bf16 v[152:167], v[52:55], v[100:103], v[152:167]\n"
    "v_fma_f32 v177, -v176, v176, v177\n"
    "v_and_b32 v178, s42, v169\n"
    "v_max_f32 v177, 0, v177\n"
    "v_sub_f32 v178, v176, v178\n"
    "ds_read_b128 v[68:71], v8 offset:1024\n"
    "s_waitcnt lgkmcnt(3)\n"
    "v_mfma_f32_32x32x16_bf16 v[152:167], v[56:59], v[104:107], v[152:167]\n"
    "v_add_f32 v177, 0x3727c5ac, v177\n"
    "v_cvt_pk_bf16_f32 v168, v176, v178\n"
    "v_rsq_f32 v177, v177\n"
    "s_nop 0\n"
    "v_mul_f32 v12, s88, v177\n"
    "ds_read_b32 v121, v14 offset:32768\n"
    "s_waitcnt lgkmcnt(3)\n"
    "v_mfma_f32_32x32x16_bf16 v[152:167], v[60:63], v[108:111], v[152:167]\n"
    "v_mfma_f32_32x32x16_bf16 v[152:167], v[170:173], v[168:171], v[152:167]\n"
    "s_setprio 0\n"
    "s_waitcnt lgkmcnt(2)\n"
    "v_add_f32 v80, v80, v64\n"
    "v_add_f32 v81, v81, v65\n"
    "v_add_f32 v82, v82, v66\n"
    "v_add_f32 v83, v83, v67\n"
    "v_fma_f32 v80, v80, v11, v128\n"
    "v_fma_f32 v81, v81, v11, v129\n"
    "v_fma_f32 v82, v82, v11, v130\n"
    "v_fma_f32 v83, v83, v11, v131\n"
    "v_fmaak_f32 v64, v80, v80, 0x409d30fc\n"
    "s_add_u32 s84, s67, 2048\n"
    "buffer_load_dwordx4 v[32:35], v248, s[12:15], s84 offen sc1\n"
    "ds_write_b128 v8, v[160:163]\n"
    "v_fmaak_f32 v65, v81, v81, 0x409d30fc\n"
    "v_fmaak_f32 v66, v82, v82, 0x409d30fc\n"
    "v_fmaak_f32 v67, v83, v83, 0x409d30fc\n"
    "v_mul_f32 v64, v80, v64\n"
    "v_mul_f32 v65, v81, v65\n"
    "v_mul_f32 v66, v82, v66\n"
    "ds_write_b128 v8, v[164:167] offset:1024\n"
    "v_mul_f32 v67, v83, v67\n"
    "v_exp_f32_e64 v64, -v64\n"
    "v_exp_f32_e64 v65, -v65\n"
    "s_add_u32 s84, s67, 2304\n"
    "buffer_load_dwordx4 v[36:39], v248, s[12:15], s84 offen sc1\n"
    "v_exp_f32_e64 v66, -v66\n"
    "v_exp_f32_e64 v67, -v67\n"
    "v_fma_f32 v64, v64, s88, s88\n"
    "v_fma_f32 v65, v65, s88, s88\n"
    "v_fma_f32 v66, v66, s88, s88\n"
    "v_fma_f32 v67, v67, s88, s88\n"
    "v_rcp_f32 v64, v64\n"
    "v_rcp_f32 v65, v65\n"
    "s_add_u32 s84, s67, 116736\n"
    "buffer_load_dwordx4 v[40:43], v248, s[12:15], s84 offen sc1\n"
    "v_rcp_f32 v66, v66\n"
    "v_rcp_f32 v67, v67\n"
    "s_waitcnt vmcnt(3)\n"
    "ds_read_b128 v[136:139], v3 offset:0\n"
    "v_mul_f32 v80, v80, v64\n"
    "v_mul_f32 v81, v81, v65\n"
    "v_mul_f32 v82, v82, v66\n"
    "ds_read_b128 v[140:143], v3 offset:1024\n"
    "v_mul_f32 v83, v83, v67\n"
    "v_cvt_pk_bf16_f32 v88, v80, v81\n"
    "v_cvt_pk_bf16_f32 v89, v82, v83\n"
    "ds_read_b128 v[144:147], v3 offset:2048\n"
    "v_min_u32 v88, -2, v88\n"
    "v_min_u32 v89, -2, v89\n"
    "s_add_u32 s84, s67, 116992\n"
    "buffer_load_dwordx4 v[44:47], v248, s[12:15], s84 offen sc1\n"
    "s_waitcnt lgkmcnt(6)\n"
    "v_add_f32 v84, v84, v68\n"
    "ds_read_b128 v[148:151], v3 offset:3072\n"
    "v_add_f32 v85, v85, v69\n"
    "v_add_f32 v86, v86, v70\n"
    "v_add_f32 v87, v87, v71\n"
    "v_fma_f32 v84, v84, v11, v132\n"
    "v_fma_f32 v85, v85, v11, v133\n"
    "v_fma_f32 v86, v86, v11, v134\n"
    "v_fma_f32 v87, v87, v11, v135\n"
    "v_fmaak_f32 v68, v84, v84, 0x409d30fc\n"
    "v_fmaak_f32 v69, v85, v85, 0x409d30fc\n"
    "s_waitcnt lgkmcnt(3)\n"
    "v_mfma_f32_4x4x4_16b_bf16 v[176:179], v[136:137], v[136:137], 0\n"
    "v_fmaak_f32 v70, v86, v86, 0x409d30fc\n"
    "s_add_u32 s84, s67, 4096\n"
    "buffer_load_dwordx4 v[232:235], v248, s[12:15], s84 offen sc1\n"
    "v_fmaak_f32 v71, v87, v87, 0x409d30fc\n"
    "v_mul_f32 v68, v84, v68\n"
    "v_mfma_f32_16x16x32_bf16 v[180:183], v[124:127], v[136:139], 0\n"
    "v_mul_f32 v69, v85, v69\n"
    "v_mul_f32 v70, v86, v70\n"
    "v_mfma_f32_4x4x4_16b_bf16 v[176:179], v[138:139], v[138:139], v[176:179]\n"
    "v_mul_f32 v71, v87, v71\n"
    "v_exp_f32_e64 v68, -v68\n"
    "s_waitcnt lgkmcnt(2)\n"
    "v_mfma_f32_4x4x4_16b_bf16 v[176:179], v[140:141], v[140:141], v[176:179]\n"
    "v_exp_f32_e64 v69, -v69\n"
    "v_mfma_f32_16x16x32_bf16 v[180:183], v[124:127], v[140:143], v[180:183]\n"
    "v_exp_f32_e64 v70, -v70\n"
    "v_exp_f32_e64 v71, -v71\n"
    "v_mfma_f32_4x4x4_16b_bf16 v[176:179], v[142:143], v[142:143], v[176:179]\n"
    "s_add_u32 s84, s67, 4352\n"
    "buffer_load_dwordx4 v[236:239], v248, s[12:15], s84 offen sc1\n"
    "v_fma_f32 v68, v68, s88, s88\n"
    "s_waitcnt lgkmcnt(1)\n"
    "v_mfma_f32_4x4x4_16b_bf16 v[176:179], v[144:145], v[144:145], v[176:179]\n"
    "v_fma_f32 v69, v69, s88, s88\n"
    "v_fma_f32 v70, v70, s88, s88\n"
    "v_mfma_f32_16x16x32_bf16 v[180:183], v[124:127], v[144:147], v[180:183]\n"
    "v_fma_f32 v71, v71, s88, s88\n"
    "v_rcp_f32 v68, v68\n"
    "v_mfma_f32_4x4x4_16b_bf16 v[176:179], v[146:147], v[146:147], v[176:179]\n"
    "v_rcp_f32 v69, v69\n"
    "s_waitcnt lgkmcnt(0)\n"
    "v_mfma_f32_4x4x4_16b_bf16 v[176:179], v[148:149], v[148:149], v[176:179]\n"
    "v_rcp_f32 v70, v70\n"
    "v_rcp_f32 v71, v71\n"
    "s_add_u32 s84, s67, 118784\n"
    "buffer_load_dwordx4 v[240:243], v248, s[12:15], s84 offen sc1\n"
    "v_mfma_f32_16x16x32_bf16 v[180:183], v[124:127], v[148:151], v[180:183]\n"
    "v_mul_f32 v84, v84, v68\n"
    "v_mfma_f32_4x4x4_16b_bf16 v[176:179], v[150:151], v[150:151], v[176:179]\n"
    "v_mul_f32 v85, v85, v69\n"
    "v_mul_f32 v86, v86, v70\n"
    "v_mul_f32 v87, v87, v71\n"
    "v_cvt_pk_bf16_f32 v90, v84, v85\n"
    "v_cvt_pk_bf16_f32 v91, v86, v87\n"
    "v_min_u32 v90, -2, v90\n"
    "v_min_u32 v91, -2, v91\n"
    "s_add_u32 s54, s76, 0\n"
    "buffer_store_dwordx4 v[88:91], v2, s[12:15], s54 offen sc1\n"
    "s_add_u32 s84, s67, 119040\n"
    "buffer_load_dwordx4 v[244:247], v248, s[12:15], s84 offen sc1\n"
    "v_cmp_u_f32 vcc, v180, v180\n"
    "s_cbranch_vccnz hp_59_%=\n"
    "hb_60_%=:\n"
    "v_cndmask_b32 v176, v176, v177, s[8:9]\n"
    "v_cndmask_b32 v178, v178, v179, s[8:9]\n"
    "v_cndmask_b32 v176, v176, v178, s[10:11]\n"
    "v_mov_b32 v177, v176\n"
    "s_nop 1\n"
    "v_permlane32_swap_b32 v176, v177\n"
    "v_add_f32 v176, v176, v177\n"
    "v_cndmask_b32 v176, v180, v176, s[60:61]\n"
    "ds_write_b32 v9, v176 offset:0\n"
    "v_mfma_f32_32x32x16_bf16 v[80:95], v[184:187], v[136:139], 0\n"
    "v_mfma_f32_32x32x16_bf16 v[80:95], v[188:191], v[140:143], v[80:95]\n"
    "s_waitcnt lgkmcnt(0)\n"
    "s_barrier\n"
    "s_setprio 1\n"
    "ds_read_b128 v[96:99], v4 offset:0\n"
    "ds_read_b128 v[100:103], v4 offset:1024\n"
    "s_add_u32 m0, s78, 32768\n"
    "s_add_u32 s87, s74, 98304\n"
    "buffer_load_dwordx4 v2, s[12:15], s87 offen offset:0 sc1 lds\n"
    "v_mfma_f32_32x32x16_bf16 v[80:95], v[192:195], v[144:147], v[80:95]\n"
    "ds_read_b128 v[104:107], v4 offset:2048\n"
    "v_mfma_f32_32x32x16_bf16 v[80:95], v[196:199], v[148:151], v[80:95]\n"
    "buffer_load_dwordx4 v2, s[12:15], s87 offen offset:1024 sc1 lds\n"
    "ds_read_b128 v[108:111], v4 offset:3072\n"
    "s_waitcnt lgkmcnt(3)\n"
    "v_mfma_f32_32x32x16_bf16 v[80:95], v[200:203], v[96:99], v[80:95]\n"
    "ds_read_b128 v[96:99], v5 offset:0\n"
    "buffer_load_dwordx4 v2, s[12:15], s87 offen offset:2048 sc1 lds\n"
    "s_waitcnt lgkmcnt(3)\n"
    "v_mfma_f32_32x32x16_bf16 v[80:95], v[204:207], v[100:103], v[80:95]\n"
    "ds_read_b128 v[100:103], v5 offset:1024\n"
    "s_waitcnt lgkmcnt(3)\n"
    "v_mfma_f32_32x32x16_bf16 v[80:95], v[208:211], v[104:107], v[80:95]\n"
    "ds_read_b128 v[104:107], v5 offset:2048\n"
    "buffer_load_dwordx4 v2, s[12:15], s87 offen offset:3072 sc1 lds\n"
    "ds_read_b128 v[176:179], v10 offset:0\n"
    "ds_read_b128 v[180:183], v10 offset:1024\n"
    "s_waitcnt lgkmcnt(5)\n"
    "v_mfma_f32_32x32x16_bf16 v[80:95], v[212:215], v[108:111], v[80:95]\n"
    "ds_read_b128 v[108:111], v5 offset:3072\n"
    "s_waitcnt lgkmcnt(5)\n"
    "v_mfma_f32_32x32x16_bf16 v[80:95], v[216:219], v[96:99], v[80:95]\n"
    "ds_read_b128 v[96:99], v6 offset:0\n"
    "s_waitcnt lgkmcnt(5)\n"
    "v_mfma_f32_32x32x16_bf16 v[80:95], v[220:223], v[100:103], v[80:95]\n"
    "ds_read_b128 v[100:103], v6 offset:1024\n"
    "s_waitcnt lgkmcnt(3)\n"
    "v_add_f32 v176, v176, v180\n"
    "v_mfma_f32_32x32x16_bf16 v[80:95], v[224:227], v[104:107], v[80:95]\n"
    "ds_read_b128 v[104:107], v6 offset:2048\n"
    "v_add_f32 v177, v177, v181\n"
    "v_add_f32 v178, v178, v182\n"
    "v_add_f32 v179, v179, v183\n"
    "s_waitcnt lgkmcnt(3)\n"
    "v_mfma_f32_32x32x16_bf16 v[80:95], v[228:231], v[108:111], v[80:95]\n"
    "ds_read_b128 v[108:111], v6 offset:3072\n"
    "v_add_f32 v176, v176, v177\n"
    "v_add_f32 v178, v178, v179\n"
    "v_add_f32 v176, v176, v178\n"
    "v_mov_b32 v177, v176\n"
    "s_waitcnt lgkmcnt(3)\n"
    "v_mfma_f32_32x32x16_bf16 v[80:95], v[48:51], v[96:99], v[80:95]\n"
    "v_permlane32_swap_b32 v176, v177\n"
    "v_mul_f32 v176, 0xbb000000, v176\n"
    "v_mul_f32 v177, 0x3b000000, v177\n"
    "v_cvt_pk_bf16_f32 v169, v176, v176\n"
    "ds_read_b128 v[64:67], v7\n"
    "s_waitcnt lgkmcnt(3)\n"
    "v_mfma_f32_32x32x16_bf16 v[80:95], v[52:55], v[100:103], v[80:95]\n"
    "v_fma_f32 v177, -v176, v176, v177\n"
    "v_and_b32 v178, s42, v169\n"
    "v_max_f32 v177, 0, v177\n"
    "v_sub_f32 v178, v176, v178\n"
    "ds_read_b128 v[68:71], v7 offset:1024\n"
    "s_waitcnt lgkmcnt(3)\n"
    "v_mfma_f32_32x32x16_bf16 v[80:95], v[56:59], v[104:107], v[80:95]\n"
    "v_add_f32 v177, 0x3727c5ac, v177\n"
    "v_cvt_pk_bf16_f32 v168, v176, v178\n"
    "v_rsq_f32 v177, v177\n"
    "s_nop 0\n"
    "v_mul_f32 v11, s88, v177\n"
    "ds_read_b32 v122, v14 offset:0\n"
    "s_waitcnt lgkmcnt(3)\n"
    "v_mfma_f32_32x32x16_bf16 v[80:95], v[60:63], v[108:111], v[80:95]\n"
    "v_mfma_f32_32x32x16_bf16 v[80:95], v[170:173], v[168:171], v[80:95]\n"
    "s_setprio 0\n"
    "s_waitcnt lgkmcnt(2)\n"
    "v_add_f32 v152, v152, v64\n"
    "v_add_f32 v153, v153, v65\n"
    "v_add_f32 v154, v154, v66\n"
    "v_add_f32 v155, v155, v67\n"
    "v_fma_f32 v152, v152, v12, v128\n"
    "v_fma_f32 v153, v153, v12, v129\n"
    "v_fma_f32 v154, v154, v12, v130\n"
    "v_fma_f32 v155, v155, v12, v131\n"
    "v_fmaak_f32 v64, v152, v152, 0x409d30fc\n"
    "s_add_u32 s84, s67, 6144\n"
    "buffer_load_dwordx4 v[16:19], v248, s[12:15], s84 offen sc1\n"
    "ds_write_b128 v7, v[88:91]\n"
    "v_fmaak_f32 v65, v153, v153, 0x409d30fc\n"
    "v_fmaak_f32 v66, v154, v154, 0x409d30fc\n"
    "v_fmaak_f32 v67, v155, v155, 0x409d30fc\n"
    "v_mul_f32 v64, v152, v64\n"
    "v_mul_f32 v65, v153, v65\n"
    "v_mul_f32 v66, v154, v66\n"
    "ds_write_b128 v7, v[92:95] offset:1024\n"
    "v_mul_f32 v67, v155, v67\n"
    "v_exp_f32_e64 v64, -v64\n"
    "v_exp_f32_e64 v65, -v65\n"
    "s_add_u32 s84, s67, 6400\n"
    "buffer_load_dwordx4 v[20:23], v248, s[12:15], s84 offen sc1\n"
    "v_exp_f32_e64 v66, -v66\n"
    "v_exp_f32_e64 v67, -v67\n"
    "v_fma_f32 v64, v64, s88, s88\n"
    "v_fma_f32 v65, v65, s88, s88\n"
    "v_fma_f32 v66, v66, s88, s88\n"
    "v_fma_f32 v67, v67, s88, s88\n"
    "v_rcp_f32 v64, v64\n"
    "v_rcp_f32 v65, v65\n"
    "s_add_u32 s84, s67, 120832\n"
    "buffer_load_dwordx4 v[24:27], v248, s[12:15], s84 offen sc1\n"
    "v_rcp_f32 v66, v66\n"
    "v_rcp_f32 v67, v67\n"
    "s_waitcnt vmcnt(3)\n"
    "ds_read_b128 v[136:139], v3 offset:32768\n"
    "v_mul_f32 v152, v152, v64\n"
    "v_mul_f32 v153, v153, v65\n"
    "v_mul_f32 v154, v154, v66\n"
    "ds_read_b128 v[140:143], v3 offset:33792\n"
    "v_mul_f32 v155, v155, v67\n"
    "v_cvt_pk_bf16_f32 v160, v152, v153\n"
    "v_cvt_pk_bf16_f32 v161, v154, v155\n"
    "ds_read_b128 v[144:147], v3 offset:34816\n"
    "v_min_u32 v160, -2, v160\n"
    "v_min_u32 v161, -2, v161\n"
    "s_add_u32 s84, s67, 121088\n"
    "buffer_load_dwordx4 v[28:31], v248, s[12:15], s84 offen sc1\n"
    "s_waitcnt lgkmcnt(6)\n"
    "v_add_f32 v156, v156, v68\n"
    "ds_read_b128 v[148:151], v3 offset:35840\n"
    "v_add_f32 v157, v157, v69\n"
    "v_add_f32 v158, v158, v70\n"
    "v_add_f32 v159, v159, v71\n"
    "v_fma_f32 v156, v156, v12, v132\n"
    "v_fma_f32 v157, v157, v12, v133\n"
    "v_fma_f32 v158, v158, v12, v134\n"
    "v_fma_f32 v159, v159, v12, v135\n"
    "v_fmaak_f32 v68, v156, v156, 0x409d30fc\n"
    "v_fmaak_f32 v69, v157, v157, 0x409d30fc\n"
    "s_waitcnt lgkmcnt(3)\n"
    "v_mfma_f32_4x4x4_16b_bf16 v[176:179], v[136:137], v[136:137], 0\n"
    "v_fmaak_f32 v70, v158, v158, 0x409d30fc\n"
    "s_add_u32 m0, s78, 88064\n"
    "s_add_u32 s87, s67, 12288\n"
    "buffer_load_dwordx4 v248, s[12:15], s87 offen sc1 lds\n"
    "v_fmaak_f32 v71, v159, v159, 0x409d30fc\n"
    "v_mul_f32 v68, v156, v68\n"
    "v_mfma_f32_16x16x32_bf16 v[180:183], v[124:127], v[136:139], 0\n"
    "v_mul_f32 v69, v157, v69\n"
    "v_mul_f32 v70, v158, v70\n"
    "v_mfma_f32_4x4x4_16b_bf16 v[176:179], v[138:139], v[138:139], v[176:179]\n"
    "v_mul_f32 v71, v159, v71\n"
    "v_exp_f32_e64 v68, -v68\n"
    "s_waitcnt lgkmcnt(2)\n"
    "v_mfma_f32_4x4x4_16b_bf16 v[176:179], v[140:141], v[140:141], v[176:179]\n"
    "v_exp_f32_e64 v69, -v69\n"
    "v_mfma_f32_16x16x32_bf16 v[180:183], v[124:127], v[140:143], v[180:183]\n"
    "v_exp_f32_e64 v70, -v70\n"
    "v_exp_f32_e64 v71, -v71\n"
    "v_mfma_f32_4x4x4_16b_bf16 v[176:179], v[142:143], v[142:143], v[176:179]\n"
    "s_add_u32 m0, s78, 89088\n"
    "s_add_u32 s87, s67, 12544\n"
    "buffer_load_dwordx4 v248, s[12:15], s87 offen sc1 lds\n"
    "v_fma_f32 v68, v68, s88, s88\n"
    "s_waitcnt lgkmcnt(1)\n"
    "v_mfma_f32_4x4x4_16b_bf16 v[176:179], v[144:145], v[144:145], v[176:179]\n"
    "v_fma_f32 v69, v69, s88, s88\n"
    "v_fma_f32 v70, v70, s88, s88\n"
    "v_mfma_f32_16x16x32_bf16 v[180:183], v[124:127], v[144:147], v[180:183]\n"
    "v_fma_f32 v71, v71, s88, s88\n"
    "v_rcp_f32 v68, v68\n"
    "v_mfma_f32_4x4x4_16b_bf16 v[176:179], v[146:147], v[146:147], v[176:179]\n"
    "v_rcp_f32 v69, v69\n"
    "s_waitcnt lgkmcnt(0)\n"
    "v_mfma_f32_4x4x4_16b_bf16 v[176:179], v[148:149], v[148:149], v[176:179]\n"
    "v_rcp_f32 v70, v70\n"
    "v_rcp_f32 v71, v71\n"
    "s_add_u32 m0, s78, 90112\n"
    "s_add_u32 s87, s67, 126976\n"
    "buffer_load_dwordx4 v248, s[12:15], s87 offen sc1 lds\n"
    "v_mfma_f32_16x16x32_bf16 v[180:183], v[124:127], v[148:151], v[180:183]\n"
    "v_mul_f32 v156, v156, v68\n"
    "v_mfma_f32_4x4x4_16b_bf16 v[176:179], v[150:151], v[150:151], v[176:179]\n"
    "v_mul_f32 v157, v157, v69\n"
    "v_mul_f32 v158, v158, v70\n"
    "v_mul_f32 v159, v159, v71\n"
    "v_cvt_pk_bf16_f32 v162, v156, v157\n"
    "v_cvt_pk_bf16_f32 v163, v158, v159\n"
    "v_min_u32 v162, -2, v162\n"
    "v_min_u32 v163, -2, v163\n"
    "s_add_u32 s54, s76, 114688\n"
    "buffer_store_dwordx4 v[160:163], v2, s[12:15], s54 offen sc1\n"
    "s_add_u32 m0, s78, 91136\n"
    "s_add_u32 s87, s67, 127232\n"
    "buffer_load_dwordx4 v248, s[12:15], s87 offen sc1 lds\n"
    "v_cmp_u_f32 vcc, v180, v180\n"
    "s_cbranch_vccnz hp_62_%=\n"
    "hb_63_%=:\n"
    "v_cndmask_b32 v176, v176, v177, s[8:9]\n"
    "v_cndmask_b32 v178, v178, v179, s[8:9]\n"
    "v_cndmask_b32 v176, v176, v178, s[10:11]\n"
    "v_mov_b32 v177, v176\n"
    "s_nop 1\n"
    "v_permlane32_swap_b32 v176, v177\n"
    "v_add_f32 v176, v176, v177\n"
    "v_cndmask_b32 v176, v180, v176, s[60:61]\n"
    "ds_write_b32 v9, v176 offset:2048\n"
    "v_mfma_f32_32x32x16_bf16 v[152:167], v[184:187], v[136:139], 0\n"
    "v_mfma_f32_32x32x16_bf16 v[152:167], v[188:191], v[140:143], v[152:167]\n"
    "s_waitcnt lgkmcnt(0)\n"
    "s_barrier\n"
    "s_setprio 1\n"
    "ds_read_b128 v[96:99], v4 offset:32768\n"
    "ds_read_b128 v[100:103], v4 offset:33792\n"
    "s_add_u32 s86, s75, 0\n"
    "buffer_load_dwordx4 v[184:187], v248, s[12:15], s86 offen sc1\n"
    "v_mfma_f32_32x32x16_bf16 v[152:167], v[192:195], v[144:147], v[152:167]\n"
    "ds_read_b128 v[104:107], v4 offset:34816\n"
    "v_mfma_f32_32x32x16_bf16 v[152:167], v[196:199], v[148:151], v[152:167]\n"
    "s_add_u32 s86, s75, 2048\n"
    "buffer_load_dwordx4 v[188:191], v248, s[12:15], s86 offen sc1\n"
    "ds_read_b128 v[108:111], v4 offset:35840\n"
    "s_add_u32 s54, s57, 120832\n"
    "v_add_u32 v249, s54, v2\n"
    "s_waitcnt lgkmcnt(3)\n"
    "v_mfma_f32_32x32x16_bf16 v[152:167], v[200:203], v[96:99], v[152:167]\n"
    "ds_read_b128 v[96:99], v5 offset:32768\n"
    "v_add_u32 v15, 88064, v3\n"
    "v_and_b32 v0, 31, v1\n"
    "v_lshlrev_b32 v0, 2, v0\n"
    "s_lshr_b32 s55, s65, 1\n"
    "s_lshl_b32 s55, s55, 7\n"
    "buffer_load_dword v252, v0, s[28:31], s55 offen\n"
    "s_and_b32 s54, s65, 1\n"
    "s_lshl3_add_u32 s54, s64, s54\n"
    "s_lshl3_add_u32 s54, s54, s3\n"
    "s_lshl_b32 s54, s54, 11\n"
    "s_add_u32 s77, s54, s55\n"
    "s_add_u32 s86, s75, 4096\n"
    "buffer_load_dwordx4 v[192:195], v248, s[12:15], s86 offen sc1\n"
    "s_waitcnt lgkmcnt(3)\n"
    "v_mfma_f32_32x32x16_bf16 v[152:167], v[204:207], v[100:103], v[152:167]\n"
    "ds_read_b128 v[100:103], v5 offset:33792\n"
    "v_and_b32 v250, 48, v1\n"
    "s_mul_i32 s54, s3, 4608\n"
    "s_add_u32 s54, s54, 32768\n"
    "v_add3_u32 v250, v250, v2, s54\n"
    "v_lshrrev_b32 v254, 5, v1\n"
    "s_waitcnt lgkmcnt(3)\n"
    "v_mfma_f32_32x32x16_bf16 v[152:167], v[208:211], v[104:107], v[152:167]\n"
    "ds_read_b128 v[104:107], v5 offset:34816\n"
    "v_bfe_u32 v251, v1, 1, 4\n"
    "s_add_u32 s86, s75, 6144\n"
    "buffer_load_dwordx4 v[196:199], v248, s[12:15], s86 offen sc1\n"
    "v_lshl_or_b32 v0, v254, 10, v0\n"
    "ds_read_b128 v[176:179], v10 offset:2048\n"
    "ds_read_b128 v[180:183], v10 offset:3072\n"
    "s_waitcnt lgkmcnt(5)\n"
    "v_mfma_f32_32x32x16_bf16 v[152:167], v[212:215], v[108:111], v[152:167]\n"
    "ds_read_b128 v[108:111], v5 offset:35840\n"
    "v_mul_u32_u24 v251, 272, v251\n"
    "v_lshl_add_u32 v251, v254, 4, v251\n"
    "s_add_u32 s84, s67, 8192\n"
    "buffer_load_dwordx4 v[136:139], v248, s[12:15], s84 offen sc1\n"
    "s_waitcnt lgkmcnt(5)\n"
    "v_mfma_f32_32x32x16_bf16 v[152:167], v[216:219], v[96:99], v[152:167]\n"
    "ds_read_b128 v[96:99], v6 offset:32768\n"
    "v_and_b32 v254, 1, v1\n"
    "v_lshl_add_u32 v251, v254, 3, v251\n"
    "s_lshl_b32 s54, s3, 5\n"
    "s_add_u32 s54, s54, 32768\n"
    "v_add_u32 v251, s54, v251\n"
    "s_waitcnt lgkmcnt(5)\n"
    "v_mfma_f32_32x32x16_bf16 v[152:167], v[220:223], v[100:103], v[152:167]\n"
    "ds_read_b128 v[100:103], v6 offset:33792\n"
    "s_add_u32 s84, s67, 8448\n"
    "buffer_load_dwordx4 v[140:143], v248, s[12:15], s84 offen sc1\n"
    "s_waitcnt lgkmcnt(3)\n"
    "v_add_f32 v176, v176, v180\n"
    "ds_read_b128 v[148:151], v249 offset:0\n"
    "v_mfma_f32_32x32x16_bf16 v[152:167], v[224:227], v[104:107], v[152:167]\n"
    "ds_read_b128 v[104:107], v6 offset:34816\n"
    "v_add_f32 v177, v177, v181\n"
    "v_add_f32 v178, v178, v182\n"
    "v_add_f32 v179, v179, v183\n"
    "s_add_u32 s84, s67, 122880\n"
    "buffer_load_dwordx4 v[144:147], v248, s[12:15], s84 offen sc1\n"
    "s_waitcnt lgkmcnt(4)\n"
    "v_mfma_f32_32x32x16_bf16 v[152:167], v[228:231], v[108:111], v[152:167]\n"
    "ds_read_b128 v[108:111], v6 offset:35840\n"
    "v_add_f32 v176, v176, v177\n"
    "v_add_f32 v178, v178, v179\n"
    "v_add_f32 v176, v176, v178\n"
    "v_mov_b32 v177, v176\n"
    "s_waitcnt lgkmcnt(4)\n"
    "v_mfma_f32_32x32x16_bf16 v[152:167], v[48:51], v[96:99], v[152:167]\n"
    "s_waitcnt vmcnt(8)\n"
    "ds_read_b128 v[96:99], v15 offset:0\n"
    "v_permlane32_swap_b32 v176, v177\n"
    "v_mul_f32 v176, 0xbb000000, v176\n"
    "s_add_u32 s86, s75, 8192\n"
    "buffer_load_dwordx4 v[200:203], v248, s[12:15], s86 offen sc1\n"
    "v_mul_f32 v177, 0x3b000000, v177\n"
    "v_cvt_pk_bf16_f32 v169, v176, v176\n"
    "ds_read_b128 v[64:67], v8\n"
    "s_waitcnt lgkmcnt(5)\n"
    "v_mfma_f32_32x32x16_bf16 v[152:167], v[52:55], v[100:103], v[152:167]\n"
    "ds_read_b128 v[100:103], v15 offset:1024\n"
    "v_fma_f32 v177, -v176, v176, v177\n"
    "v_and_b32 v178, s42, v169\n"
    "v_max_f32 v177, 0, v177\n"
    "v_sub_f32 v178, v176, v178\n"
    "ds_read_b128 v[68:71], v8 offset:1024\n"
    "s_add_u32 s86, s75, 10240\n"
    "buffer_load_dwordx4 v[204:207], v248, s[12:15], s86 offen sc1\n"
    "s_waitcnt lgkmcnt(5)\n"
    "v_mfma_f32_32x32x16_bf16 v[152:167], v[56:59], v[104:107], v[152:167]\n"
    "ds_read_b128 v[104:107], v15 offset:2048\n"
    "v_add_f32 v177, 0x3727c5ac, v177\n"
    "v_cvt_pk_bf16_f32 v168, v176, v178\n"
    "v_rsq_f32 v177, v177\n"
    "s_nop 0\n"
    "v_mul_f32 v12, s88, v177\n"
    "ds_read_b32 v123, v14 offset:32768\n"
    "s_waitcnt lgkmcnt(6)\n"
    "v_mfma_f32_32x32x16_bf16 v[152:167], v[60:63], v[108:111], v[152:167]\n"
    "ds_read_b128 v[108:111], v15 offset:3072\n"
    "s_add_u32 s86, s75, 12288\n"
    "buffer_load_dwordx4 v[208:211], v248, s[12:15], s86 offen sc1\n"
    "v_mfma_f32_32x32x16_bf16 v[152:167], v[170:173], v[168:171], v[152:167]\n"
    "s_setprio 0\n"
    "s_waitcnt lgkmcnt(5)\n"
    "v_add_f32 v80, v80, v64\n"
    "v_add_f32 v81, v81, v65\n"
    "v_add_f32 v82, v82, v66\n"
    "v_add_f32 v83, v83, v67\n"
    "v_fma_f32 v80, v80, v11, v128\n"
    "v_fma_f32 v81, v81, v11, v129\n"
    "v_fma_f32 v82, v82, v11, v130\n"
    "v_fma_f32 v83, v83, v11, v131\n"
    "v_fmaak_f32 v64, v80, v80, 0x409d30fc\n"
    "s_nop 0\n"
    "ds_write_b128 v8, v[160:163]\n"
    "v_fmaak_f32 v65, v81, v81, 0x409d30fc\n"
    "v_fmaak_f32 v66, v82, v82, 0x409d30fc\n"
    "v_fmaak_f32 v67, v83, v83, 0x409d30fc\n"
    "v_mul_f32 v64, v80, v64\n"
    "v_mul_f32 v65, v81, v65\n"
    "v_mul_f32 v66, v82, v66\n"
    "ds_write_b128 v8, v[164:167] offset:1024\n"
    "v_mul_f32 v67, v83, v67\n"
    "v_exp_f32_e64 v64, -v64\n"
    "v_exp_f32_e64 v65, -v65\n"
    "v_exp_f32_e64 v66, -v66\n"
    "v_exp_f32_e64 v67, -v67\n"
    "v_fma_f32 v64, v64, s88, s88\n"
    "v_fma_f32 v65, v65, s88, s88\n"
    "v_fma_f32 v66, v66, s88, s88\n"
    "v_fma_f32 v67, v67, s88, s88\n"
    "v_rcp_f32 v64, v64\n"
    "v_rcp_f32 v65, v65\n"
    "v_rcp_f32 v66, v66\n"
    "v_rcp_f32 v67, v67\n"
    "v_mul_f32 v80, v80, v64\n"
    "v_mul_f32 v81, v81, v65\n"
    "v_mul_f32 v82, v82, v66\n"
    "v_mul_f32 v83, v83, v67\n"
    "v_cvt_pk_bf16_f32 v88, v80, v81\n"
    "v_cvt_pk_bf16_f32 v89, v82, v83\n"
    "v_min_u32 v88, -2, v88\n"
    "v_min_u32 v89, -2, v89\n"
    "s_waitcnt lgkmcnt(5)\n"
    "v_add_f32 v84, v84, v68\n"
    "v_add_f32 v85, v85, v69\n"
    "v_add_f32 v86, v86, v70\n"
    "v_add_f32 v87, v87, v71\n"
    "v_fma_f32 v84, v84, v11, v132\n"
    "v_fma_f32 v85, v85, v11, v133\n"
    "v_fma_f32 v86, v86, v11, v134\n"
    "v_fma_f32 v87, v87, v11, v135\n"
    "v_fmaak_f32 v68, v84, v84, 0x409d30fc\n"
    "v_fmaak_f32 v69, v85, v85, 0x409d30fc\n"
    "v_fmaak_f32 v70, v86, v86, 0x409d30fc\n"
    "v_fmaak_f32 v71, v87, v87, 0x409d30fc\n"
    "v_mul_f32 v68, v84, v68\n"
    "v_mul_f32 v69, v85, v69\n"
    "v_mul_f32 v70, v86, v70\n"
    "v_mul_f32 v71, v87, v71\n"
    "v_exp_f32_e64 v68, -v68\n"
    "v_exp_f32_e64 v69, -v69\n"
    "v_exp_f32_e64 v70, -v70\n"
    "v_exp_f32_e64 v71, -v71\n"
    "v_fma_f32 v68, v68, s88, s88\n"
    "v_fma_f32 v69, v69, s88, s88\n"
    "v_fma_f32 v70, v70, s88, s88\n"
    "v_fma_f32 v71, v71, s88, s88\n"
    "v_rcp_f32 v68, v68\n"
    "v_rcp_f32 v69, v69\n"
    "v_rcp_f32 v70, v70\n"
    "v_rcp_f32 v71, v71\n"
    "v_mul_f32 v84, v84, v68\n"
    "v_mul_f32 v85, v85, v69\n"
    "v_mul_f32 v86, v86, v70\n"
    "v_mul_f32 v87, v87, v71\n"
    "v_cvt_pk_bf16_f32 v90, v84, v85\n"
    "v_cvt_pk_bf16_f32 v91, v86, v87\n"
    "v_min_u32 v90, -2, v90\n"
    "v_min_u32 v91, -2, v91\n"
    "s_add_u32 s54, s76, 229376\n"
    "buffer_store_dwordx4 v[88:91], v2, s[12:15], s54 offen sc1\n"
    "ds_read_b128 v[80:83], v249 offset:1024\n"
    "ds_read_b128 v[84:87], v249 offset:2048\n"
    "ds_read_b128 v[88:91], v249 offset:3072\n"
    "ds_read_b128 v[92:95], v249 offset:4096\n"
    "s_add_u32 s86, s75, 114688\n"
    "buffer_load_dwordx4 v[212:215], v248, s[12:15], s86 offen sc1\n"
    "s_setprio 1\n"
    "s_waitcnt vmcnt(12)\n"
    "v_mfma_f32_16x16x32_bf16 v[168:171], v[72:75], v[184:187], 0\n"
    "v_mfma_f32_16x16x32_bf16 v[172:175], v[76:79], v[184:187], 0\n"
    "v_mfma_f32_16x16x32_bf16 v[176:179], v[112:115], v[184:187], 0\n"
    "v_mfma_f32_16x16x32_bf16 v[180:183], v[116:119], v[184:187], 0\n"
    "s_add_u32 s86, s75, 116736\n"
    "buffer_load_dwordx4 v[216:219], v248, s[12:15], s86 offen sc1\n"
    "s_waitcnt vmcnt(12)\n"
    "v_mfma_f32_16x16x32_bf16 v[168:171], v[32:35], v[188:191], v[168:171]\n"
    "v_mfma_f32_16x16x32_bf16 v[172:175], v[36:39], v[188:191], v[172:175]\n"
    "v_mfma_f32_16x16x32_bf16 v[176:179], v[40:43], v[188:191], v[176:179]\n"
    "v_mfma_f32_16x16x32_bf16 v[180:183], v[44:47], v[188:191], v[180:183]\n"
    "s_add_u32 s86, s75, 118784\n"
    "buffer_load_dwordx4 v[220:223], v248, s[12:15], s86 offen sc1\n"
    "s_setprio 2\n"
    "s_waitcnt vmcnt(11)\n"
    "v_mfma_f32_16x16x32_bf16 v[168:171], v[232:235], v[192:195], v[168:171]\n"
    "v_mfma_f32_16x16x32_bf16 v[172:175], v[236:239], v[192:195], v[172:175]\n"
    "v_mfma_f32_16x16x32_bf16 v[176:179], v[240:243], v[192:195], v[176:179]\n"
    "v_mfma_f32_16x16x32_bf16 v[180:183], v[244:247], v[192:195], v[180:183]\n"
    "s_add_u32 s86, s75, 120832\n"
    "buffer_load_dwordx4 v[224:227], v248, s[12:15], s86 offen sc1\n"
    "s_waitcnt lgkmcnt(0)\n"
    "s_barrier\n"
    "ds_read_b128 v[64:67], v7\n"
    "ds_read_b128 v[68:71], v7 offset:1024\n"
    "s_waitcnt vmcnt(11)\n"
    "v_mfma_f32_16x16x32_bf16 v[168:171], v[16:19], v[196:199], v[168:171]\n"
    "v_mfma_f32_16x16x32_bf16 v[172:175], v[20:23], v[196:199], v[172:175]\n"
    "v_mfma_f32_16x16x32_bf16 v[176:179], v[24:27], v[196:199], v[176:179]\n"
    "s_add_u32 s86, s75, 122880\n"
    "buffer_load_dwordx4 v[228:231], v248, s[12:15], s86 offen sc1\n"
    "v_mfma_f32_16x16x32_bf16 v[180:183], v[28:31], v[196:199], v[180:183]\n"
    "s_waitcnt vmcnt(8)\n"
    "v_mfma_f32_16x16x32_bf16 v[168:171], v[136:139], v[200:203], v[168:171]\n"
    "v_mfma_f32_16x16x32_bf16 v[172:175], v[140:143], v[200:203], v[172:175]\n"
    "v_mfma_f32_16x16x32_bf16 v[176:179], v[144:147], v[200:203], v[176:179]\n"
    "s_add_u32 s86, s75, 124928\n"
    "buffer_load_dwordx4 v[48:51], v248, s[12:15], s86 offen sc1\n"
    "v_mfma_f32_16x16x32_bf16 v[180:183], v[148:151], v[200:203], v[180:183]\n"
    "s_waitcnt vmcnt(8)\n"
    "v_mfma_f32_16x16x32_bf16 v[168:171], v[80:83], v[204:207], v[168:171]\n"
    "v_mfma_f32_16x16x32_bf16 v[172:175], v[84:87], v[204:207], v[172:175]\n"
    "s_add_u32 s86, s75, 126976\n"
    "buffer_load_dwordx4 v[52:55], v248, s[12:15], s86 offen sc1\n"
    "v_mfma_f32_16x16x32_bf16 v[176:179], v[88:91], v[204:207], v[176:179]\n"
    "v_mfma_f32_16x16x32_bf16 v[180:183], v[92:95], v[204:207], v[180:183]\n"
    "s_waitcnt vmcnt(8)\n"
    "v_mfma_f32_16x16x32_bf16 v[168:171], v[96:99], v[208:211], v[168:171]\n"
    "v_mfma_f32_16x16x32_bf16 v[172:175], v[100:103], v[208:211], v[172:175]\n"
    "v_mfma_f32_16x16x32_bf16 v[176:179], v[104:107], v[208:211], v[176:179]\n"
    "v_mfma_f32_16x16x32_bf16 v[180:183], v[108:111], v[208:211], v[180:183]\n"
    "s_nop 4\n"
    "v_cmp_u_f32 vcc, v168, v168\n"
    "s_cbranch_vccnz gp_65_%=\n"
    "gb_66_%=:\n"
    "s_setprio 0\n"
    "s_waitcnt lgkmcnt(1)\n"
    "v_add_f32 v152, v152, v64\n"
    "v_add_f32 v153, v153, v65\n"
    "v_add_f32 v154, v154, v66\n"
    "v_add_f32 v155, v155, v67\n"
    "v_fma_f32 v152, v152, v12, v128\n"
    "v_fma_f32 v153, v153, v12, v129\n"
    "v_fma_f32 v154, v154, v12, v130\n"
    "v_fma_f32 v155, v155, v12, v131\n"
    "ds_write_b128 v250, v[168:171] offset:0\n"
    "v_fmaak_f32 v64, v152, v152, 0x409d30fc\n"
    "v_fmaak_f32 v65, v153, v153, 0x409d30fc\n"
    "v_fmaak_f32 v66, v154, v154, 0x409d30fc\n"
    "v_fmaak_f32 v67, v155, v155, 0x409d30fc\n"
    "v_mul_f32 v64, v152, v64\n"
    "v_mul_f32 v65, v153, v65\n"
    "v_mul_f32 v66, v154, v66\n"
    "v_mul_f32 v67, v155, v67\n"
    "v_exp_f32_e64 v64, -v64\n"
    "v_exp_f32_e64 v65, -v65\n"
    "v_exp_f32_e64 v66, -v66\n"
    "ds_write_b128 v250, v[172:175] offset:1088\n"
    "v_exp_f32_e64 v67, -v67\n"
    "v_fma_f32 v64, v64, s88, s88\n"
    "v_fma_f32 v65, v65, s88, s88\n"
    "v_fma_f32 v66, v66, s88, s88\n"
    "v_fma_f32 v67, v67, s88, s88\n"
    "v_rcp_f32 v64, v64\n"
    "v_rcp_f32 v65, v65\n"
    "v_rcp_f32 v66, v66\n"
    "v_rcp_f32 v67, v67\n"
    "ds_write_b128 v250, v[176:179] offset:2176\n"
    "v_mul_f32 v152, v152, v64\n"
    "v_mul_f32 v153, v153, v65\n"
    "v_mul_f32 v154, v154, v66\n"
    "v_mul_f32 v155, v155, v67\n"
    "v_cvt_pk_bf16_f32 v160, v152, v153\n"
    "v_cvt_pk_bf16_f32 v161, v154, v155\n"
    "v_min_u32 v160, -2, v160\n"
    "v_min_u32 v161, -2, v161\n"
    "s_waitcnt lgkmcnt(3)\n"
    "v_add_f32 v156, v156, v68\n"
    "v_add_f32 v157, v157, v69\n"
    "v_add_f32 v158, v158, v70\n"
    "v_add_f32 v159, v159, v71\n"
    "ds_write_b128 v250, v[180:183] offset:3264\n"
    "v_fma_f32 v156, v156, v12, v132\n"
    "v_fma_f32 v157, v157, v12, v133\n"
    "v_fma_f32 v158, v158, v12, v134\n"
    "v_fma_f32 v159, v159, v12, v135\n"
    "v_fmaak_f32 v68, v156, v156, 0x409d30fc\n"
    "v_fmaak_f32 v69, v157, v157, 0x409d30fc\n"
    "v_fmaak_f32 v70, v158, v158, 0x409d30fc\n"
    "v_fmaak_f32 v71, v159, v159, 0x409d30fc\n"
    "v_mul_f32 v68, v156, v68\n"
    "v_mul_f32 v69, v157, v69\n"
    "v_and_b32 v253, s42, v252\n"
    "v_lshlrev_b32 v252, 16, v252\n"
    "v_add_u32 v4, 38912, v251\n"
    "v_mul_f32 v70, v158, v70\n"
    "v_mul_f32 v71, v159, v71\n"
    "v_exp_f32_e64 v68, -v68\n"
    "v_exp_f32_e64 v69, -v69\n"
    "v_exp_f32_e64 v70, -v70\n"
    "v_exp_f32_e64 v71, -v71\n"
    "v_fma_f32 v68, v68, s88, s88\n"
    "v_fma_f32 v69, v69, s88, s88\n"
    "v_fma_f32 v70, v70, s88, s88\n"
    "v_fma_f32 v71, v71, s88, s88\n"
    "v_rcp_f32 v68, v68\n"
    "v_rcp_f32 v69, v69\n"
    "v_rcp_f32 v70, v70\n"
    "v_rcp_f32 v71, v71\n"
    "v_mul_f32 v156, v156, v68\n"
    "v_mul_f32 v157, v157, v69\n"
    "v_mul_f32 v158, v158, v70\n"
    "v_mul_f32 v159, v159, v71\n"
    "v_cvt_pk_bf16_f32 v162, v156, v157\n"
    "v_cvt_pk_bf16_f32 v163, v158, v159\n"
    "v_min_u32 v162, -2, v162\n"
    "v_min_u32 v163, -2, v163\n"
    "s_add_u32 s54, s76, 344064\n"
    "buffer_store_dwordx4 v[160:163], v2, s[12:15], s54 offen sc1\n"
    "s_add_u32 s86, s75, 229376\n"
    "buffer_load_dwordx4 v[184:187], v248, s[12:15], s86 offen sc1\n"
    "s_setprio 1\n"
    "s_waitcnt vmcnt(8)\n"
    "v_mfma_f32_16x16x32_bf16 v[152:155], v[72:75], v[212:215], 0\n"
    "v_mfma_f32_16x16x32_bf16 v[156:159], v[76:79], v[212:215], 0\n"
    "v_mfma_f32_16x16x32_bf16 v[160:163], v[112:115], v[212:215], 0\n"
    "v_mfma_f32_16x16x32_bf16 v[164:167], v[116:119], v[212:215], 0\n"
    "s_add_u32 s86, s75, 231424\n"
    "buffer_load_dwordx4 v[188:191], v248, s[12:15], s86 offen sc1\n"
    "s_waitcnt vmcnt(8)\n"
    "v_mfma_f32_16x16x32_bf16 v[152:155], v[32:35], v[216:219], v[152:155]\n"
    "v_mfma_f32_16x16x32_bf16 v[156:159], v[36:39], v[216:219], v[156:159]\n"
    "v_mfma_f32_16x16x32_bf16 v[160:163], v[40:43], v[216:219], v[160:163]\n"
    "v_mfma_f32_16x16x32_bf16 v[164:167], v[44:47], v[216:219], v[164:167]\n"
    "s_add_u32 s86, s75, 233472\n"
    "buffer_load_dwordx4 v[192:195], v248, s[12:15], s86 offen sc1\n"
    "s_setprio 2\n"
    "s_waitcnt vmcnt(8)\n"
    "v_mfma_f32_16x16x32_bf16 v[152:155], v[232:235], v[220:223], v[152:155]\n"
    "v_mfma_f32_16x16x32_bf16 v[156:159], v[236:239], v[220:223], v[156:159]\n"
    "v_mfma_f32_16x16x32_bf16 v[160:163], v[240:243], v[220:223], v[160:163]\n"
    "v_mfma_f32_16x16x32_bf16 v[164:167], v[244:247], v[220:223], v[164:167]\n"
    "s_add_u32 s86, s75, 235520\n"
    "buffer_load_dwordx4 v[196:199], v248, s[12:15], s86 offen sc1\n"
    "s_waitcnt vmcnt(8)\n"
    "v_mfma_f32_16x16x32_bf16 v[152:155], v[16:19], v[224:227], v[152:155]\n"
    "v_mfma_f32_16x16x32_bf16 v[156:159], v[20:23], v[224:227], v[156:159]\n"
    "v_mfma_f32_16x16x32_bf16 v[160:163], v[24:27], v[224:227], v[160:163]\n"
    "v_mfma_f32_16x16x32_bf16 v[164:167], v[28:31], v[224:227], v[164:167]\n"
    "s_add_u32 s86, s75, 237568\n"
    "buffer_load_dwordx4 v[200:203], v248, s[12:15], s86 offen sc1\n"
    "s_waitcnt vmcnt(8)\n"
    "v_mfma_f32_16x16x32_bf16 v[152:155], v[136:139], v[228:231], v[152:155]\n"
    "v_mfma_f32_16x16x32_bf16 v[156:159], v[140:143], v[228:231], v[156:159]\n"
    "v_mfma_f32_16x16x32_bf16 v[160:163], v[144:147], v[228:231], v[160:163]\n"
    "v_mfma_f32_16x16x32_bf16 v[164:167], v[148:151], v[228:231], v[164:167]\n"
    "s_add_u32 s86, s75, 239616\n"
    "buffer_load_dwordx4 v[204:207], v248, s[12:15], s86 offen sc1\n"
    "s_waitcnt lgkmcnt(0)\n"
    "s_barrier\n"
    "s_waitcnt vmcnt(8)\n"
    "v_mfma_f32_16x16x32_bf16 v[152:155], v[80:83], v[48:51], v[152:155]\n"
    "v_mfma_f32_16x16x32_bf16 v[156:159], v[84:87], v[48:51], v[156:159]\n"
    "v_mfma_f32_16x16x32_bf16 v[160:163], v[88:91], v[48:51], v[160:163]\n"
    "s_add_u32 s86, s75, 241664\n"
    "buffer_load_dwordx4 v[208:211], v248, s[12:15], s86 offen sc1\n"
    "v_mfma_f32_16x16x32_bf16 v[164:167], v[92:95], v[48:51], v[164:167]\n"
    "s_waitcnt vmcnt(8)\n"
    "v_mfma_f32_16x16x32_bf16 v[152:155], v[96:99], v[52:55], v[152:155]\n"
    "v_mfma_f32_16x16x32_bf16 v[156:159], v[100:103], v[52:55], v[156:159]\n"
    "v_mfma_f32_16x16x32_bf16 v[160:163], v[104:107], v[52:55], v[160:163]\n"
    "v_mfma_f32_16x16x32_bf16 v[164:167], v[108:111], v[52:55], v[164:167]\n"
    "s_nop 4\n"
    "v_cmp_u_f32 vcc, v152, v152\n"
    "s_cbranch_vccnz gp_68_%=\n"
    "gb_69_%=:\n"
    "s_setprio 0\n"
    "ds_read_b64 v[56:57], v251 offset:0\n"
    "ds_read_b64 v[58:59], v251 offset:4608\n"
    "ds_read_b64 v[60:61], v251 offset:9216\n"
    "ds_read_b64 v[62:63], v251 offset:13824\n"
    "ds_write_b128 v250, v[152:155] offset:38912\n"
    "ds_read_b64 v[64:65], v251 offset:18432\n"
    "ds_read_b64 v[66:67], v251 offset:23040\n"
    "ds_read_b64 v[68:69], v251 offset:27648\n"
    "ds_read_b64 v[70:71], v251 offset:32256\n"
    "ds_write_b128 v250, v[156:159] offset:40000\n"
    "v_lshlrev_b32 v254, 16, v120\n"
    "v_and_b32 v120, s42, v120\n"
    "v_add_f32 v254, v254, v252\n"
    "v_add_f32 v120, v120, v253\n"
    "s_waitcnt lgkmcnt(8)\n"
    "v_add_f32 v56, v56, v58\n"
    "v_add_f32 v57, v57, v59\n"
    "ds_write_b128 v250, v[160:163] offset:41088\n"
    "s_waitcnt lgkmcnt(7)\n"
    "v_add_f32 v60, v60, v62\n"
    "v_add_f32 v61, v61, v63\n"
    "s_waitcnt lgkmcnt(4)\n"
    "v_add_f32 v64, v64, v66\n"
    "v_add_f32 v65, v65, v67\n"
    "s_waitcnt lgkmcnt(2)\n"
    "v_add_f32 v68, v68, v70\n"
    "v_add_f32 v69, v69, v71\n"
    "ds_write_b128 v250, v[164:167] offset:42176\n"
    "v_add_f32 v56, v56, v60\n"
    "v_add_f32 v57, v57, v61\n"
    "v_add_f32 v64, v64, v68\n"
    "v_add_f32 v65, v65, v69\n"
    "v_add_f32 v56, v56, v64\n"
    "v_add_f32 v57, v57, v65\n"
    "v_add_f32 v56, v56, v254\n"
    "v_add_f32 v57, v57, v120\n"
    "v_cvt_pk_bf16_f32 v56, v56, v57\n"
    "s_add_u32 s54, s77, 0\n"
    "buffer_store_dword v56, v0, s[16:19], s54 offen sc0 sc1\n"
    "s_setprio 1\n"
    "s_waitcnt vmcnt(7)\n"
    "v_mfma_f32_16x16x32_bf16 v[168:171], v[72:75], v[184:187], 0\n"
    "v_mfma_f32_16x16x32_bf16 v[172:175], v[76:79], v[184:187], 0\n"
    "v_mfma_f32_16x16x32_bf16 v[176:179], v[112:115], v[184:187], 0\n"
    "v_mfma_f32_16x16x32_bf16 v[180:183], v[116:119], v[184:187], 0\n"
    "s_waitcnt vmcnt(6)\n"
    "v_mfma_f32_16x16x32_bf16 v[168:171], v[32:35], v[188:191], v[168:171]\n"
    "v_mfma_f32_16x16x32_bf16 v[172:175], v[36:39], v[188:191], v[172:175]\n"
    "v_mfma_f32_16x16x32_bf16 v[176:179], v[40:43], v[188:191], v[176:179]\n"
    "v_mfma_f32_16x16x32_bf16 v[180:183], v[44:47], v[188:191], v[180:183]\n"
    "s_add_u32 s86, s75, 344064\n"
    "buffer_load_dwordx4 v[212:215], v248, s[12:15], s86 offen sc1\n"
    "s_setprio 2\n"
    "s_waitcnt vmcnt(6)\n"
    "v_mfma_f32_16x16x32_bf16 v[168:171], v[232:235], v[192:195], v[168:171]\n"
    "v_mfma_f32_16x16x32_bf16 v[172:175], v[236:239], v[192:195], v[172:175]\n"
    "v_mfma_f32_16x16x32_bf16 v[176:179], v[240:243], v[192:195], v[176:179]\n"
    "v_mfma_f32_16x16x32_bf16 v[180:183], v[244:247], v[192:195], v[180:183]\n"
    "s_add_u32 s86, s75, 346112\n"
    "buffer_load_dwordx4 v[216:219], v248, s[12:15], s86 offen sc1\n"
    "s_waitcnt vmcnt(6)\n"
    "v_mfma_f32_16x16x32_bf16 v[168:171], v[16:19], v[196:199], v[168:171]\n"
    "v_mfma_f32_16x16x32_bf16 v[172:175], v[20:23], v[196:199], v[172:175]\n"
    "v_mfma_f32_16x16x32_bf16 v[176:179], v[24:27], v[196:199], v[176:179]\n"
    "s_add_u32 s86, s75, 348160\n"
    "buffer_load_dwordx4 v[220:223], v248, s[12:15], s86 offen sc1\n"
    "v_mfma_f32_16x16x32_bf16 v[180:183], v[28:31], v[196:199], v[180:183]\n"
    "s_waitcnt vmcnt(6)\n"
    "v_mfma_f32_16x16x32_bf16 v[168:171], v[136:139], v[200:203], v[168:171]\n"
    "v_mfma_f32_16x16x32_bf16 v[172:175], v[140:143], v[200:203], v[172:175]\n"
    "v_mfma_f32_16x16x32_bf16 v[176:179], v[144:147], v[200:203], v[176:179]\n"
    "s_add_u32 s86, s75, 350208\n"
    "buffer_load_dwordx4 v[224:227], v248, s[12:15], s86 offen sc1\n"
    "v_mfma_f32_16x16x32_bf16 v[180:183], v[148:151], v[200:203], v[180:183]\n"
    "s_waitcnt lgkmcnt(0)\n"
    "s_barrier\n"
    "s_waitcnt vmcnt(6)\n"
    "v_mfma_f32_16x16x32_bf16 v[168:171], v[80:83], v[204:207], v[168:171]\n"
    "v_mfma_f32_16x16x32_bf16 v[172:175], v[84:87], v[204:207], v[172:175]\n"
    "s_add_u32 s86, s75, 352256\n"
    "buffer_load_dwordx4 v[228:231], v248, s[12:15], s86 offen sc1\n"
    "v_mfma_f32_16x16x32_bf16 v[176:179], v[88:91], v[204:207], v[176:179]\n"
    "v_mfma_f32_16x16x32_bf16 v[180:183], v[92:95], v[204:207], v[180:183]\n"
    "s_waitcnt vmcnt(6)\n"
    "v_mfma_f32_16x16x32_bf16 v[168:171], v[96:99], v[208:211], v[168:171]\n"
    "v_mfma_f32_16x16x32_bf16 v[172:175], v[100:103], v[208:211], v[172:175]\n"
    "s_add_u32 s86, s75, 354304\n"
    "buffer_load_dwordx4 v[48:51], v248, s[12:15], s86 offen sc1\n"
    "v_mfma_f32_16x16x32_bf16 v[176:179], v[104:107], v[208:211], v[176:179]\n"
    "v_mfma_f32_16x16x32_bf16 v[180:183], v[108:111], v[208:211], v[180:183]\n"
    "s_nop 2\n"
    "v_cmp_u_f32 vcc, v168, v168\n"
    "s_cbranch_vccnz gp_71_%=\n"
    "gb_72_%=:\n"
    "s_setprio 0\n"
    "ds_read_b64 v[56:57], v4 offset:0\n"
    "s_add_u32 s86, s75, 356352\n"
    "buffer_load_dwordx4 v[52:55], v248, s[12:15], s86 offen sc1\n"
    "ds_read_b64 v[58:59], v4 offset:4608\n"
    "ds_read_b64 v[60:61], v4 offset:9216\n"
    "ds_read_b64 v[62:63], v4 offset:13824\n"
    "ds_write_b128 v250, v[168:171] offset:0\n"
    "ds_read_b64 v[64:65], v4 offset:18432\n"
    "ds_read_b64 v[66:67], v4 offset:23040\n"
    "ds_read_b64 v[68:69], v4 offset:27648\n"
    "ds_read_b64 v[70:71], v4 offset:32256\n"
    "ds_write_b128 v250, v[172:175] offset:1088\n"
    "v_lshlrev_b32 v254, 16, v121\n"
    "v_and_b32 v121, s42, v121\n"
    "v_add_f32 v254, v254, v252\n"
    "v_add_f32 v121, v121, v253\n"
    "s_waitcnt lgkmcnt(8)\n"
    "v_add_f32 v56, v56, v58\n"
    "v_add_f32 v57, v57, v59\n"
    "ds_write_b128 v250, v[176:179] offset:2176\n"
    "s_waitcnt lgkmcnt(7)\n"
    "v_add_f32 v60, v60, v62\n"
    "v_add_f32 v61, v61, v63\n"
    "s_waitcnt lgkmcnt(4)\n"
    "v_add_f32 v64, v64, v66\n"
    "v_add_f32 v65, v65, v67\n"
    "s_waitcnt lgkmcnt(2)\n"
    "v_add_f32 v68, v68, v70\n"
    "v_add_f32 v69, v69, v71\n"
    "ds_write_b128 v250, v[180:183] offset:3264\n"
    "v_add_f32 v56, v56, v60\n"
    "v_add_f32 v57, v57, v61\n"
    "v_add_f32 v64, v64, v68\n"
    "v_add_f32 v65, v65, v69\n"
    "v_add_f32 v56, v56, v64\n"
    "v_add_f32 v57, v57, v65\n"
    "v_add_f32 v56, v56, v254\n"
    "v_add_f32 v57, v57, v121\n"
    "v_cvt_pk_bf16_f32 v56, v56, v57\n"
    "s_add_u32 s54, s77, 32768\n"
    "buffer_store_dword v56, v0, s[16:19], s54 offen sc0 sc1\n"
    "s_setprio 1\n"
    "s_waitcnt vmcnt(7)\n"
    "v_mfma_f32_16x16x32_bf16 v[152:155], v[72:75], v[212:215], 0\n"
    "v_mfma_f32_16x16x32_bf16 v[156:159], v[76:79], v[212:215], 0\n"
    "v_mfma_f32_16x16x32_bf16 v[160:163], v[112:115], v[212:215], 0\n"
    "v_mfma_f32_16x16x32_bf16 v[164:167], v[116:119], v[212:215], 0\n"
    "s_waitcnt vmcnt(6)\n"
    "v_mfma_f32_16x16x32_bf16 v[152:155], v[32:35], v[216:219], v[152:155]\n"
    "v_mfma_f32_16x16x32_bf16 v[156:159], v[36:39], v[216:219], v[156:159]\n"
    "v_mfma_f32_16x16x32_bf16 v[160:163], v[40:43], v[216:219], v[160:163]\n"
    "v_mfma_f32_16x16x32_bf16 v[164:167], v[44:47], v[216:219], v[164:167]\n"
    "s_setprio 2\n"
    "s_waitcnt vmcnt(5)\n"
    "v_mfma_f32_16x16x32_bf16 v[152:155], v[232:235], v[220:223], v[152:155]\n"
    "v_mfma_f32_16x16x32_bf16 v[156:159], v[236:239], v[220:223], v[156:159]\n"
    "v_mfma_f32_16x16x32_bf16 v[160:163], v[240:243], v[220:223], v[160:163]\n"
    "v_mfma_f32_16x16x32_bf16 v[164:167], v[244:247], v[220:223], v[164:167]\n"
    "s_waitcnt vmcnt(4)\n"
    "v_mfma_f32_16x16x32_bf16 v[152:155], v[16:19], v[224:227], v[152:155]\n"
    "v_mfma_f32_16x16x32_bf16 v[156:159], v[20:23], v[224:227], v[156:159]\n"
    "v_mfma_f32_16x16x32_bf16 v[160:163], v[24:27], v[224:227], v[160:163]\n"
    "v_mfma_f32_16x16x32_bf16 v[164:167], v[28:31], v[224:227], v[164:167]\n"
    "s_waitcnt vmcnt(3)\n"
    "v_mfma_f32_16x16x32_bf16 v[152:155], v[136:139], v[228:231], v[152:155]\n"
    "v_mfma_f32_16x16x32_bf16 v[156:159], v[140:143], v[228:231], v[156:159]\n"
    "v_mfma_f32_16x16x32_bf16 v[160:163], v[144:147], v[228:231], v[160:163]\n"
    "v_mfma_f32_16x16x32_bf16 v[164:167], v[148:151], v[228:231], v[164:167]\n"
    "s_waitcnt lgkmcnt(0)\n"
    "s_barrier\n"
    "s_waitcnt vmcnt(2)\n"
    "v_mfma_f32_16x16x32_bf16 v[152:155], v[80:83], v[48:51], v[152:155]\n"
    "v_mfma_f32_16x16x32_bf16 v[156:159], v[84:87], v[48:51], v[156:159]\n"
    "v_mfma_f32_16x16x32_bf16 v[160:163], v[88:91], v[48:51], v[160:163]\n"
    "v_mfma_f32_16x16x32_bf16 v[164:167], v[92:95], v[48:51], v[164:167]\n"
    "s_waitcnt vmcnt(1)\n"
    "v_mfma_f32_16x16x32_bf16 v[152:155], v[96:99], v[52:55], v[152:155]\n"
    "v_mfma_f32_16x16x32_bf16 v[156:159], v[100:103], v[52:55], v[156:159]\n"
    "v_mfma_f32_16x16x32_bf16 v[160:163], v[104:107], v[52:55], v[160:163]\n"
    "v_mfma_f32_16x16x32_bf16 v[164:167], v[108:111], v[52:55], v[164:167]\n"
    "s_nop 4\n"
    "v_cmp_u_f32 vcc, v152, v152\n"
    "s_cbranch_vccnz gp_74_%=\n"
    "gb_75_%=:\n"
    "s_setprio 0\n"
    "ds_read_b64 v[56:57], v251 offset:0\n"
    "ds_read_b64 v[58:59], v251 offset:4608\n"
    "ds_read_b64 v[60:61], v251 offset:9216\n"
    "ds_read_b64 v[62:63], v251 offset:13824\n"
    "ds_write_b128 v250, v[152:155] offset:38912\n"
    "ds_read_b64 v[64:65], v251 offset:18432\n"
    "ds_read_b64 v[66:67], v251 offset:23040\n"
    "ds_read_b64 v[68:69], v251 offset:27648\n"
    "ds_read_b64 v[70:71], v251 offset:32256\n"
    "ds_write_b128 v250, v[156:159] offset:40000\n"
    "v_lshlrev_b32 v254, 16, v122\n"
    "v_and_b32 v122, s42, v122\n"
    "v_add_f32 v254, v254, v252\n"
    "v_add_f32 v122, v122, v253\n"
    "s_waitcnt lgkmcnt(8)\n"
    "v_add_f32 v56, v56, v58\n"
    "v_add_f32 v57, v57, v59\n"
    "ds_write_b128 v250, v[160:163] offset:41088\n"
    "s_waitcnt lgkmcnt(7)\n"
    "v_add_f32 v60, v60, v62\n"
    "v_add_f32 v61, v61, v63\n"
    "s_waitcnt lgkmcnt(4)\n"
    "v_add_f32 v64, v64, v66\n"
    "v_add_f32 v65, v65, v67\n"
    "s_waitcnt lgkmcnt(2)\n"
    "v_add_f32 v68, v68, v70\n"
    "v_add_f32 v69, v69, v71\n"
    "ds_write_b128 v250, v[164:167] offset:42176\n"
    "v_add_f32 v56, v56, v60\n"
    "v_add_f32 v57, v57, v61\n"
    "v_add_f32 v64, v64, v68\n"
    "v_add_f32 v65, v65, v69\n"
    "v_add_f32 v56, v56, v64\n"
    "v_add_f32 v57, v57, v65\n"
    "v_add_f32 v56, v56, v254\n"
    "v_add_f32 v57, v57, v122\n"
    "v_cvt_pk_bf16_f32 v56, v56, v57\n"
    "s_add_u32 s54, s77, 65536\n"
    "buffer_store_dword v56, v0, s[16:19], s54 offen sc0 sc1\n"
    "s_waitcnt lgkmcnt(0)\n"
    "s_barrier\n"
    "ds_read_b64 v[56:57], v4 offset:0\n"
    "ds_read_b64 v[58:59], v4 offset:4608\n"
    "ds_read_b64 v[60:61], v4 offset:9216\n"
    "ds_read_b64 v[62:63], v4 offset:13824\n"
    "ds_read_b64 v[64:65], v4 offset:18432\n"
    "ds_read_b64 v[66:67], v4 offset:23040\n"
    "ds_read_b64 v[68:69], v4 offset:27648\n"
    "ds_read_b64 v[70:71], v4 offset:32256\n"
    "v_lshlrev_b32 v254, 16, v123\n"
    "v_and_b32 v123, s42, v123\n"
    "v_add_f32 v254, v254, v252\n"
    "v_add_f32 v123, v123, v253\n"
    "s_waitcnt lgkmcnt(6)\n"
    "v_add_f32 v56, v56, v58\n"
    "v_add_f32 v57, v57, v59\n"
    "s_waitcnt lgkmcnt(4)\n"
    "v_add_f32 v60, v60, v62\n"
    "v_add_f32 v61, v61, v63\n"
    "s_waitcnt lgkmcnt(2)\n"
    "v_add_f32 v64, v64, v66\n"
    "v_add_f32 v65, v65, v67\n"
    "s_waitcnt lgkmcnt(0)\n"
    "v_add_f32 v68, v68, v70\n"
    "v_add_f32 v69, v69, v71\n"
    "v_add_f32 v56, v56, v60\n"
    "v_add_f32 v57, v57, v61\n"
    "v_add_f32 v64, v64, v68\n"
    "v_add_f32 v65, v65, v69\n"
    "v_add_f32 v56, v56, v64\n"
    "v_add_f32 v57, v57, v65\n"
    "v_add_f32 v56, v56, v254\n"
    "v_add_f32 v57, v57, v123\n"
    "v_cvt_pk_bf16_f32 v56, v56, v57\n"
    "s_add_u32 s54, s77, 98304\n"
    "buffer_store_dword v56, v0, s[16:19], s54 offen sc0 sc1\n"
    "s_branch r0_52_%=\n"
    "a1_51_%=:\n"
    "s_setprio 1\n"
    "ds_read_b128 v[96:99], v4 offset:0\n"
    "ds_read_b128 v[100:103], v4 offset:1024\n"
    "ds_read_b64 v[16:17], v252 offset:0\n"
    "s_waitcnt lgkmcnt(0)\n"
    "s_add_u32 m0, s78, 32768\n"
    "s_add_u32 s87, s74, 32768\n"
    "buffer_load_dwordx4 v2, s[12:15], s87 offen offset:0 sc1 lds\n"
    "v_mfma_f32_32x32x16_bf16 v[80:95], v[184:187], v[136:139], 0\n"
    "ds_read_b64 v[18:19], v252 offset:512\n"
    "ds_read_b64 v[20:21], v252 offset:1024\n"
    "ds_read_b64 v[22:23], v252 offset:1536\n"
    "ds_read_b64 v[24:25], v252 offset:2048\n"
    "ds_read_b64 v[26:27], v252 offset:2560\n"
    "ds_read_b64 v[28:29], v252 offset:3072\n"
    "ds_read_b64 v[30:31], v252 offset:3584\n"
    "ds_read_b128 v[104:107], v4 offset:2048\n"
    "v_mfma_f32_32x32x16_bf16 v[80:95], v[188:191], v[140:143], v[80:95]\n"
    "buffer_load_dwordx4 v2, s[12:15], s87 offen offset:1024 sc1 lds\n"
    "ds_read_b128 v[108:111], v4 offset:3072\n"
    "v_mfma_f32_32x32x16_bf16 v[80:95], v[192:195], v[144:147], v[80:95]\n"
    "buffer_load_dwordx4 v2, s[12:15], s87 offen offset:2048 sc1 lds\n"
    "s_waitcnt lgkmcnt(8)\n"
    "v_add_f32 v16, v16, v18\n"
    "v_mfma_f32_32x32x16_bf16 v[80:95], v[196:199], v[148:151], v[80:95]\n"
    "v_add_f32 v17, v17, v19\n"
    "s_waitcnt lgkmcnt(6)\n"
    "v_add_f32 v20, v20, v22\n"
    "v_add_f32 v21, v21, v23\n"
    "s_waitcnt lgkmcnt(4)\n"
    "v_add_f32 v24, v24, v26\n"
    "v_add_f32 v25, v25, v27\n"
    "v_mfma_f32_32x32x16_bf16 v[80:95], v[200:203], v[96:99], v[80:95]\n"
    "ds_read_b128 v[96:99], v5 offset:0\n"
    "s_waitcnt lgkmcnt(3)\n"
    "v_add_f32 v28, v28, v30\n"
    "v_add_f32 v29, v29, v31\n"
    "v_add_f32 v16, v16, v20\n"
    "buffer_load_dwordx4 v2, s[12:15], s87 offen offset:3072 sc1 lds\n"
    "v_add_f32 v17, v17, v21\n"
    "v_add_f32 v24, v24, v28\n"
    "v_add_f32 v25, v25, v29\n"
    "v_mfma_f32_32x32x16_bf16 v[80:95], v[204:207], v[100:103], v[80:95]\n"
    "ds_read_b128 v[100:103], v5 offset:1024\n"
    "v_add_f32 v16, v16, v24\n"
    "v_add_f32 v17, v17, v25\n"
    "v_lshlrev_b32 v18, 16, v122\n"
    "v_and_b32 v19, s42, v122\n"
    "v_add_f32 v16, v16, v18\n"
    "v_add_f32 v17, v17, v19\n"
    "s_waitcnt lgkmcnt(3)\n"
    "v_mfma_f32_32x32x16_bf16 v[80:95], v[208:211], v[104:107], v[80:95]\n"
    "ds_read_b128 v[104:107], v5 offset:2048\n"
    "v_cvt_pk_bf16_f32 v16, v16, v17\n"
    "v_min_u32 v16, -2, v16\n"
    "s_nop 1\n"
    "v_mov_b32_dpp v17, v16 quad_perm:[1,1,1,1] row_mask:0xf bank_mask:0xf\n"
    "v_mov_b32_dpp v18, v16 quad_perm:[2,2,2,2] row_mask:0xf bank_mask:0xf\n"
    "v_mov_b32_dpp v19, v16 quad_perm:[3,3,3,3] row_mask:0xf bank_mask:0xf\n"
    "s_add_u32 s56, s69, 65536\n"
    "buffer_store_dwordx4 v[16:19], v253, s[12:15], s56 offen sc1\n"
    "ds_read_b128 v[176:179], v10 offset:0\n"
    "ds_read_b128 v[180:183], v10 offset:1024\n"
    "s_waitcnt lgkmcnt(5)\n"
    "v_mfma_f32_32x32x16_bf16 v[80:95], v[212:215], v[108:111], v[80:95]\n"
    "ds_read_b128 v[108:111], v5 offset:3072\n"
    "s_add_u32 s85, s73, 98304\n"
    "buffer_load_dwordx4 v[32:35], v2, s[12:15], s85 offen offset:0 sc1\n"
    "s_waitcnt lgkmcnt(5)\n"
    "v_mfma_f32_32x32x16_bf16 v[80:95], v[216:219], v[96:99], v[80:95]\n"
    "ds_read_b128 v[96:99], v6 offset:0\n"
    "s_waitcnt lgkmcnt(5)\n"
    "v_mfma_f32_32x32x16_bf16 v[80:95], v[220:223], v[100:103], v[80:95]\n"
    "ds_read_b128 v[100:103], v6 offset:1024\n"
    "s_add_u32 s85, s73, 98304\n"
    "buffer_load_dwordx4 v[36:39], v2, s[12:15], s85 offen offset:1024 sc1\n"
    "s_waitcnt lgkmcnt(3)\n"
    "v_add_f32 v176, v176, v180\n"
    "v_mfma_f32_32x32x16_bf16 v[80:95], v[224:227], v[104:107], v[80:95]\n"
    "ds_read_b128 v[104:107], v6 offset:2048\n"
    "v_add_f32 v177, v177, v181\n"
    "v_add_f32 v178, v178, v182\n"
    "v_add_f32 v179, v179, v183\n"
    "s_add_u32 s85, s73, 98304\n"
    "buffer_load_dwordx4 v[40:43], v2, s[12:15], s85 offen offset:2048 sc1\n"
    "s_waitcnt lgkmcnt(3)\n"
    "v_mfma_f32_32x32x16_bf16 v[80:95], v[228:231], v[108:111], v[80:95]\n"
    "ds_read_b128 v[108:111], v6 offset:3072\n"
    "v_add_f32 v176, v176, v177\n"
    "v_add_f32 v178, v178, v179\n"
    "v_add_f32 v176, v176, v178\n"
    "v_mov_b32 v177, v176\n"
    "s_waitcnt lgkmcnt(3)\n"
    "v_mfma_f32_32x32x16_bf16 v[80:95], v[48:51], v[96:99], v[80:95]\n"
    "v_permlane32_swap_b32 v176, v177\n"
    "v_mul_f32 v176, 0xbb000000, v176\n"
    "s_add_u32 s85, s73, 98304\n"
    "buffer_load_dwordx4 v[44:47], v2, s[12:15], s85 offen offset:3072 sc1\n"
    "v_mul_f32 v177, 0x3b000000, v177\n"
    "v_cvt_pk_bf16_f32 v169, v176, v176\n"
    "ds_read_b32 v120, v14 offset:0\n"
    "s_waitcnt lgkmcnt(3)\n"
    "v_mfma_f32_32x32x16_bf16 v[80:95], v[52:55], v[100:103], v[80:95]\n"
    "v_fma_f32 v177, -v176, v176, v177\n"
    "v_and_b32 v178, s42, v169\n"
    "v_max_f32 v177, 0, v177\n"
    "v_sub_f32 v178, v176, v178\n"
    "s_waitcnt lgkmcnt(2)\n"
    "v_mfma_f32_32x32x16_bf16 v[80:95], v[56:59], v[104:107], v[80:95]\n"
    "v_add_f32 v177, 0x3727c5ac, v177\n"
    "v_cvt_pk_bf16_f32 v168, v176, v178\n"
    "v_rsq_f32 v177, v177\n"
    "s_nop 0\n"
    "v_mul_f32 v11, s88, v177\n"
    "s_waitcnt lgkmcnt(1)\n"
    "v_mfma_f32_32x32x16_bf16 v[80:95], v[60:63], v[108:111], v[80:95]\n"
    "v_mfma_f32_32x32x16_bf16 v[80:95], v[170:173], v[168:171], v[80:95]\n"
    "s_setprio 0\n"
    "s_waitcnt vmcnt(10)\n"
    "v_lshlrev_b32 v128, 16, v132\n"
    "s_add_u32 s84, s67, 0\n"
    "buffer_load_dwordx4 v[72:75], v248, s[12:15], s84 offen sc1\n"
    "v_and_b32 v129, s42, v132\n"
    "v_lshlrev_b32 v130, 16, v133\n"
    "v_and_b32 v131, s42, v133\n"
    "s_waitcnt vmcnt(10)\n"
    "v_lshlrev_b32 v132, 16, v134\n"
    "v_and_b32 v133, s42, v134\n"
    "s_add_u32 s84, s67, 256\n"
    "buffer_load_dwordx4 v[76:79], v248, s[12:15], s84 offen sc1\n"
    "v_lshlrev_b32 v134, 16, v135\n"
    "v_and_b32 v135, s42, v135\n"
    "v_mul_f32 v128, s88, v128\n"
    "ds_write_b128 v7, v[88:91]\n"
    "v_mul_f32 v129, s88, v129\n"
    "v_mul_f32 v130, s88, v130\n"
    "s_add_u32 s84, s67, 114688\n"
    "buffer_load_dwordx4 v[112:115], v248, s[12:15], s84 offen sc1\n"
    "v_mul_f32 v131, s88, v131\n"
    "v_mul_f32 v132, s88, v132\n"
    "v_mul_f32 v133, s88, v133\n"
    "v_mul_f32 v134, s88, v134\n"
    "ds_write_b128 v7, v[92:95] offset:1024\n"
    "v_mul_f32 v135, s88, v135\n"
    "s_add_u32 s84, s67, 114944\n"
    "buffer_load_dwordx4 v[116:119], v248, s[12:15], s84 offen sc1\n"
    "s_waitcnt vmcnt(9)\n"
    "ds_read_b128 v[136:139], v3 offset:32768\n"
    "ds_read_b128 v[140:143], v3 offset:33792\n"
    "ds_read_b128 v[144:147], v3 offset:34816\n"
    "ds_read_b128 v[148:151], v3 offset:35840\n"
    "s_waitcnt vmcnt(7)\n"
    "v_mfma_f32_32x32x16_bf16 v[16:31], v[232:235], v[32:35], 0\n"
    "s_waitcnt lgkmcnt(3)\n"
    "v_mfma_f32_4x4x4_16b_bf16 v[176:179], v[136:137], v[136:137], 0\n"
    "v_mfma_f32_16x16x32_bf16 v[180:183], v[124:127], v[136:139], 0\n"
    "s_nop 0\n"
    "v_mfma_f32_4x4x4_16b_bf16 v[176:179], v[138:139], v[138:139], v[176:179]\n"
    "s_waitcnt vmcnt(6)\n"
    "v_mfma_f32_32x32x16_bf16 v[16:31], v[236:239], v[36:39], v[16:31]\n"
    "s_waitcnt lgkmcnt(2)\n"
    "v_mfma_f32_4x4x4_16b_bf16 v[176:179], v[140:141], v[140:141], v[176:179]\n"
    "v_mfma_f32_16x16x32_bf16 v[180:183], v[124:127], v[140:143], v[180:183]\n"
    "s_nop 0\n"
    "v_mfma_f32_4x4x4_16b_bf16 v[176:179], v[142:143], v[142:143], v[176:179]\n"
    "s_waitcnt vmcnt(5)\n"
    "v_mfma_f32_32x32x16_bf16 v[16:31], v[240:243], v[40:43], v[16:31]\n"
    "s_waitcnt lgkmcnt(1)\n"
    "v_mfma_f32_4x4x4_16b_bf16 v[176:179], v[144:145], v[144:145], v[176:179]\n"
    "v_mfma_f32_16x16x32_bf16 v[180:183], v[124:127], v[144:147], v[180:183]\n"
    "s_nop 0\n"
    "v_mfma_f32_4x4x4_16b_bf16 v[176:179], v[146:147], v[146:147], v[176:179]\n"
    "s_waitcnt vmcnt(4)\n"
    "v_mfma_f32_32x32x16_bf16 v[16:31], v[244:247], v[44:47], v[16:31]\n"
    "s_waitcnt lgkmcnt(0)\n"
    "v_mfma_f32_4x4x4_16b_bf16 v[176:179], v[148:149], v[148:149], v[176:179]\n"
    "v_mfma_f32_16x16x32_bf16 v[180:183], v[124:127], v[148:151], v[180:183]\n"
    "s_nop 0\n"
    "v_mfma_f32_4x4x4_16b_bf16 v[176:179], v[150:151], v[150:151], v[176:179]\n"
    "s_nop 6\n"
    "v_cmp_u_f32 vcc, v16, v16\n"
    "s_cbranch_vccnz op_77_%=\n"
    "ob_78_%=:\n"
    "v_add_u32 v254, 88064, v254\n"
    "ds_write_b128 v254, v[16:19] offset:0\n"
    "ds_write_b128 v254, v[20:23] offset:16\n"
    "ds_write_b128 v254, v[24:27] offset:16384\n"
    "ds_write_b128 v254, v[28:31] offset:16400\n"
    "v_mfma_f32_32x32x16_bf16 v[152:167], v[184:187], v[136:139], 0\n"
    "v_cmp_u_f32 vcc, v180, v180\n"
    "s_cbranch_vccnz hp_80_%=\n"
    "hb_81_%=:\n"
    "v_cndmask_b32 v176, v176, v177, s[8:9]\n"
    "v_cndmask_b32 v178, v178, v179, s[8:9]\n"
    "v_cndmask_b32 v176, v176, v178, s[10:11]\n"
    "v_mov_b32 v177, v176\n"
    "s_nop 1\n"
    "v_permlane32_swap_b32 v176, v177\n"
    "v_add_f32 v176, v176, v177\n"
    "v_cndmask_b32 v176, v180, v176, s[60:61]\n"
    "ds_write_b32 v9, v176 offset:2048\n"
    "v_mfma_f32_32x32x16_bf16 v[152:167], v[188:191], v[140:143], v[152:167]\n"
    "v_mfma_f32_32x32x16_bf16 v[152:167], v[192:195], v[144:147], v[152:167]\n"
    "v_mfma_f32_32x32x16_bf16 v[152:167], v[196:199], v[148:151], v[152:167]\n"
    "s_waitcnt lgkmcnt(0)\n"
    "s_barrier\n"
    "s_setprio 0\n"
    "ds_read_b64 v[16:17], v252 offset:55296\n"
    "ds_read_b64 v[18:19], v252 offset:55808\n"
    "ds_read_b64 v[20:21], v252 offset:56320\n"
    "ds_read_b64 v[22:23], v252 offset:56832\n"
    "ds_read_b64 v[24:25], v252 offset:57344\n"
    "ds_read_b64 v[26:27], v252 offset:57856\n"
    "ds_read_b64 v[28:29], v252 offset:58368\n"
    "ds_read_b64 v[30:31], v252 offset:58880\n"
    "ds_read_b128 v[64:67], v8\n"
    "ds_read_b128 v[68:71], v8 offset:1024\n"
    "s_add_u32 m0, s78, 0\n"
    "s_add_u32 s87, s74, 65536\n"
    "buffer_load_dwordx4 v2, s[12:15], s87 offen offset:0 sc1 lds\n"
    "s_waitcnt lgkmcnt(8)\n"
    "v_add_f32 v16, v16, v18\n"
    "v_add_f32 v17, v17, v19\n"
    "s_waitcnt lgkmcnt(6)\n"
    "v_add_f32 v20, v20, v22\n"
    "v_add_f32 v21, v21, v23\n"
    "s_waitcnt lgkmcnt(4)\n"
    "v_add_f32 v24, v24, v26\n"
    "v_add_f32 v25, v25, v27\n"
    "s_waitcnt lgkmcnt(2)\n"
    "v_add_f32 v28, v28, v30\n"
    "v_add_f32 v29, v29, v31\n"
    "v_add_f32 v16, v16, v20\n"
    "v_add_f32 v17, v17, v21\n"
    "v_add_f32 v24, v24, v28\n"
    "v_add_f32 v25, v25, v29\n"
    "buffer_load_dwordx4 v2, s[12:15], s87 offen offset:1024 sc1 lds\n"
    "v_add_f32 v16, v16, v24\n"
    "v_add_f32 v17, v17, v25\n"
    "v_lshlrev_b32 v18, 16, v123\n"
    "v_and_b32 v19, s42, v123\n"
    "v_add_f32 v16, v16, v18\n"
    "v_add_f32 v17, v17, v19\n"
    "v_cvt_pk_bf16_f32 v16, v16, v17\n"
    "v_min_u32 v16, -2, v16\n"
    "s_nop 1\n"
    "v_mov_b32_dpp v17, v16 quad_perm:[1,1,1,1] row_mask:0xf bank_mask:0xf\n"
    "v_mov_b32_dpp v18, v16 quad_perm:[2,2,2,2] row_mask:0xf bank_mask:0xf\n"
    "v_mov_b32_dpp v19, v16 quad_perm:[3,3,3,3] row_mask:0xf bank_mask:0xf\n"
    "s_add_u32 s56, s69, 98304\n"
    "buffer_store_dwordx4 v[16:19], v253, s[12:15], s56 offen sc1\n"
    "buffer_load_dwordx4 v2, s[12:15], s87 offen offset:2048 sc1 lds\n"
    "buffer_load_dwordx4 v2, s[12:15], s87 offen offset:3072 sc1 lds\n"
    "s_waitcnt lgkmcnt(1)\n"
    "v_add_f32 v80, v80, v64\n"
    "v_add_f32 v81, v81, v65\n"
    "v_add_f32 v82, v82, v66\n"
    "v_add_f32 v83, v83, v67\n"
    "ds_read_b128 v[96:99], v4 offset:32768\n"
    "v_fma_f32 v80, v80, v11, v128\n"
    "v_fma_f32 v81, v81, v11, v129\n"
    "v_fma_f32 v82, v82, v11, v130\n"
    "v_fma_f32 v83, v83, v11, v131\n"
    "v_fmaak_f32 v64, v80, v80, 0x409d30fc\n"
    "v_fmaak_f32 v65, v81, v81, 0x409d30fc\n"
    "ds_read_b128 v[100:103], v4 offset:33792\n"
    "v_fmaak_f32 v66, v82, v82, 0x409d30fc\n"
    "v_fmaak_f32 v67, v83, v83, 0x409d30fc\n"
    "v_mul_f32 v64, v80, v64\n"
    "v_mul_f32 v65, v81, v65\n"
    "v_mul_f32 v66, v82, v66\n"
    "ds_read_b128 v[104:107], v4 offset:34816\n"
    "v_mul_f32 v67, v83, v67\n"
    "v_exp_f32_e64 v64, -v64\n"
    "v_exp_f32_e64 v65, -v65\n"
    "ds_read_b128 v[108:111], v4 offset:35840\n"
    "v_exp_f32_e64 v66, -v66\n"
    "v_exp_f32_e64 v67, -v67\n"
    "v_fma_f32 v64, v64, s88, s88\n"
    "v_fma_f32 v65, v65, s88, s88\n"
    "v_fma_f32 v66, v66, s88, s88\n"
    "v_fma_f32 v67, v67, s88, s88\n"
    "v_rcp_f32 v64, v64\n"
    "v_rcp_f32 v65, v65\n"
    "v_rcp_f32 v66, v66\n"
    "v_rcp_f32 v67, v67\n"
    "s_add_u32 s84, s67, 2048\n"
    "buffer_load_dwordx4 v[32:35], v248, s[12:15], s84 offen sc1\n"
    "s_setprio 1\n"
    "s_waitcnt lgkmcnt(3)\n"
    "v_mfma_f32_32x32x16_bf16 v[152:167], v[200:203], v[96:99], v[152:167]\n"
    "ds_read_b128 v[96:99], v5 offset:32768\n"
    "v_mul_f32 v80, v80, v64\n"
    "ds_read_b128 v[176:179], v10 offset:2048\n"
    "ds_read_b128 v[180:183], v10 offset:3072\n"
    "ds_read_b32 v121, v14 offset:32768\n"
    "s_waitcnt lgkmcnt(6)\n"
    "v_mfma_f32_32x32x16_bf16 v[152:167], v[204:207], v[100:103], v[152:167]\n"
    "ds_read_b128 v[100:103], v5 offset:33792\n"
    "v_mul_f32 v81, v81, v65\n"
    "v_mul_f32 v82, v82, v66\n"
    "v_mul_f32 v83, v83, v67\n"
    "v_cvt_pk_bf16_f32 v88, v80, v81\n"
    "v_cvt_pk_bf16_f32 v89, v82, v83\n"
    "v_min_u32 v88, -2, v88\n"
    "v_min_u32 v89, -2, v89\n"
    "v_add_f32 v84, v84, v68\n"
    "v_add_f32 v85, v85, v69\n"
    "v_add_f32 v86, v86, v70\n"
    "v_add_f32 v87, v87, v71\n"
    "v_fma_f32 v84, v84, v11, v132\n"
    "v_fma_f32 v85, v85, v11, v133\n"
    "v_fma_f32 v86, v86, v11, v134\n"
    "v_fma_f32 v87, v87, v11, v135\n"
    "v_fmaak_f32 v68, v84, v84, 0x409d30fc\n"
    "v_fmaak_f32 v69, v85, v85, 0x409d30fc\n"
    "v_fmaak_f32 v70, v86, v86, 0x409d30fc\n"
    "v_fmaak_f32 v71, v87, v87, 0x409d30fc\n"
    "v_mul_f32 v68, v84, v68\n"
    "v_mul_f32 v69, v85, v69\n"
    "v_mul_f32 v70, v86, v70\n"
    "s_waitcnt lgkmcnt(6)\n"
    "v_mfma_f32_32x32x16_bf16 v[152:167], v[208:211], v[104:107], v[152:167]\n"
    "ds_read_b128 v[104:107], v5 offset:34816\n"
    "v_mul_f32 v71, v87, v71\n"
    "v_exp_f32_e64 v68, -v68\n"
    "v_exp_f32_e64 v69, -v69\n"
    "v_exp_f32_e64 v70, -v70\n"
    "v_exp_f32_e64 v71, -v71\n"
    "v_fma_f32 v68, v68, s88, s88\n"
    "v_fma_f32 v69, v69, s88, s88\n"
    "s_add_u32 s84, s67, 2304\n"
    "buffer_load_dwordx4 v[36:39], v248, s[12:15], s84 offen sc1\n"
    "v_fma_f32 v70, v70, s88, s88\n"
    "v_fma_f32 v71, v71, s88, s88\n"
    "v_rcp_f32 v68, v68\n"
    "v_rcp_f32 v69, v69\n"
    "v_rcp_f32 v70, v70\n"
    "v_rcp_f32 v71, v71\n"
    "v_mul_f32 v84, v84, v68\n"
    "v_mul_f32 v85, v85, v69\n"
    "v_mul_f32 v86, v86, v70\n"
    "v_mul_f32 v87, v87, v71\n"
    "v_cvt_pk_bf16_f32 v90, v84, v85\n"
    "v_cvt_pk_bf16_f32 v91, v86, v87\n"
    "v_min_u32 v90, -2, v90\n"
    "v_min_u32 v91, -2, v91\n"
    "s_add_u32 s54, s76, 0\n"
    "buffer_store_dwordx4 v[88:91], v2, s[12:15], s54 offen sc1\n"
    "s_waitcnt lgkmcnt(6)\n"
    "v_mfma_f32_32x32x16_bf16 v[152:167], v[212:215], v[108:111], v[152:167]\n"
    "s_waitcnt lgkmcnt(3)\n"
    "v_add_f32 v176, v176, v180\n"
    "ds_read_b128 v[108:111], v5 offset:35840\n"
    "v_add_f32 v177, v177, v181\n"
    "v_add_f32 v178, v178, v182\n"
    "v_add_f32 v179, v179, v183\n"
    "v_add_f32 v176, v176, v177\n"
    "v_add_f32 v178, v178, v179\n"
    "v_add_f32 v176, v176, v178\n"
    "v_mov_b32 v177, v176\n"
    "s_nop 1\n"
    "v_permlane32_swap_b32 v176, v177\n"
    "v_mul_f32 v176, 0xbb000000, v176\n"
    "v_mfma_f32_32x32x16_bf16 v[152:167], v[216:219], v[96:99], v[152:167]\n"
    "ds_read_b128 v[96:99], v6 offset:32768\n"
    "v_mul_f32 v177, 0x3b000000, v177\n"
    "v_cvt_pk_bf16_f32 v169, v176, v176\n"
    "v_fma_f32 v177, -v176, v176, v177\n"
    "v_and_b32 v178, s42, v169\n"
    "v_max_f32 v177, 0, v177\n"
    "v_sub_f32 v178, v176, v178\n"
    "v_add_f32 v177, 0x3727c5ac, v177\n"
    "s_add_u32 s84, s67, 116736\n"
    "buffer_load_dwordx4 v[40:43], v248, s[12:15], s84 offen sc1\n"
    "v_cvt_pk_bf16_f32 v168, v176, v178\n"
    "v_rsq_f32 v177, v177\n"
    "s_waitcnt vmcnt(4)\n"
    "ds_read_b128 v[136:139], v3 offset:0\n"
    "s_waitcnt lgkmcnt(4)\n"
    "v_mfma_f32_32x32x16_bf16 v[152:167], v[220:223], v[100:103], v[152:167]\n"
    "v_mul_f32 v12, s88, v177\n"
    "ds_read_b128 v[100:103], v6 offset:33792\n"
    "ds_read_b128 v[140:143], v3 offset:1024\n"
    "v_mfma_f32_32x32x16_bf16 v[152:167], v[170:173], v[168:171], v[152:167]\n"
    "ds_read_b128 v[144:147], v3 offset:2048\n"
    "ds_read_b128 v[148:151], v3 offset:3072\n"
    "s_waitcnt lgkmcnt(7)\n"
    "v_mfma_f32_32x32x16_bf16 v[152:167], v[224:227], v[104:107], v[152:167]\n"
    "ds_read_b128 v[104:107], v6 offset:34816\n"
    "s_add_u32 s84, s67, 116992\n"
    "buffer_load_dwordx4 v[44:47], v248, s[12:15], s84 offen sc1\n"
    "s_waitcnt lgkmcnt(5)\n"
    "v_mfma_f32_4x4x4_16b_bf16 v[176:179], v[136:137], v[136:137], 0\n"
    "v_mfma_f32_16x16x32_bf16 v[180:183], v[124:127], v[136:139], 0\n"
    "v_mfma_f32_32x32x16_bf16 v[152:167], v[228:231], v[108:111], v[152:167]\n"
    "v_mfma_f32_4x4x4_16b_bf16 v[176:179], v[138:139], v[138:139], v[176:179]\n"
    "ds_read_b128 v[108:111], v6 offset:35840\n"
    "s_waitcnt lgkmcnt(4)\n"
    "v_mfma_f32_4x4x4_16b_bf16 v[176:179], v[140:141], v[140:141], v[176:179]\n"
    "v_mfma_f32_16x16x32_bf16 v[180:183], v[124:127], v[140:143], v[180:183]\n"
    "v_mfma_f32_32x32x16_bf16 v[152:167], v[48:51], v[96:99], v[152:167]\n"
    "v_mfma_f32_4x4x4_16b_bf16 v[176:179], v[142:143], v[142:143], v[176:179]\n"
    "s_add_u32 s84, s67, 4096\n"
    "buffer_load_dwordx4 v[232:235], v248, s[12:15], s84 offen sc1\n"
    "s_waitcnt lgkmcnt(3)\n"
    "v_mfma_f32_4x4x4_16b_bf16 v[176:179], v[144:145], v[144:145], v[176:179]\n"
    "v_mfma_f32_16x16x32_bf16 v[180:183], v[124:127], v[144:147], v[180:183]\n"
    "v_mfma_f32_32x32x16_bf16 v[152:167], v[52:55], v[100:103], v[152:167]\n"
    "v_mfma_f32_4x4x4_16b_bf16 v[176:179], v[146:147], v[146:147], v[176:179]\n"
    "s_waitcnt lgkmcnt(2)\n"
    "s_nop 0\n"
    "v_mfma_f32_4x4x4_16b_bf16 v[176:179], v[148:149], v[148:149], v[176:179]\n"
    "v_mfma_f32_16x16x32_bf16 v[180:183], v[124:127], v[148:151], v[180:183]\n"
    "s_waitcnt lgkmcnt(1)\n"
    "v_mfma_f32_32x32x16_bf16 v[152:167], v[56:59], v[104:107], v[152:167]\n"
    "v_mfma_f32_4x4x4_16b_bf16 v[176:179], v[150:151], v[150:151], v[176:179]\n"
    "s_waitcnt lgkmcnt(0)\n"
    "v_mfma_f32_32x32x16_bf16 v[152:167], v[60:63], v[108:111], v[152:167]\n"
    "s_nop 2\n"
    "v_cmp_u_f32 vcc, v180, v180\n"
    "s_cbranch_vccnz hp_83_%=\n"
    "hb_84_%=:\n"
    "s_add_u32 s84, s67, 4352\n"
    "buffer_load_dwordx4 v[236:239], v248, s[12:15], s84 offen sc1\n"
    "v_cndmask_b32 v176, v176, v177, s[8:9]\n"
    "v_cndmask_b32 v178, v178, v179, s[8:9]\n"
    "v_cndmask_b32 v176, v176, v178, s[10:11]\n"
    "v_mfma_f32_32x32x16_bf16 v[80:95], v[184:187], v[136:139], 0\n"
    "v_mov_b32 v177, v176\n"
    "s_nop 1\n"
    "v_permlane32_swap_b32 v176, v177\n"
    "v_add_f32 v176, v176, v177\n"
    "v_cndmask_b32 v176, v180, v176, s[60:61]\n"
    "ds_write_b32 v9, v176 offset:0\n"
    "v_mfma_f32_32x32x16_bf16 v[80:95], v[188:191], v[140:143], v[80:95]\n"
    "s_add_u32 s84, s67, 118784\n"
    "buffer_load_dwordx4 v[240:243], v248, s[12:15], s84 offen sc1\n"
    "ds_write_b128 v8, v[160:163]\n"
    "v_mfma_f32_32x32x16_bf16 v[80:95], v[192:195], v[144:147], v[80:95]\n"
    "ds_write_b128 v8, v[164:167] offset:1024\n"
    "v_mfma_f32_32x32x16_bf16 v[80:95], v[196:199], v[148:151], v[80:95]\n"
    "s_add_u32 s84, s67, 119040\n"
    "buffer_load_dwordx4 v[244:247], v248, s[12:15], s84 offen sc1\n"
    "s_waitcnt lgkmcnt(0)\n"
    "s_barrier\n"
    "s_setprio 0\n"
    "ds_read_b128 v[64:67], v7\n"
    "ds_read_b128 v[68:71], v7 offset:1024\n"
    "s_add_u32 m0, s78, 32768\n"
    "s_add_u32 s87, s74, 98304\n"
    "buffer_load_dwordx4 v2, s[12:15], s87 offen offset:0 sc1 lds\n"
    "s_waitcnt lgkmcnt(1)\n"
    "v_add_f32 v152, v152, v64\n"
    "v_add_f32 v153, v153, v65\n"
    "v_add_f32 v154, v154, v66\n"
    "v_add_f32 v155, v155, v67\n"
    "v_fma_f32 v152, v152, v12, v128\n"
    "v_fma_f32 v153, v153, v12, v129\n"
    "v_fma_f32 v154, v154, v12, v130\n"
    "v_fma_f32 v155, v155, v12, v131\n"
    "v_fmaak_f32 v64, v152, v152, 0x409d30fc\n"
    "v_fmaak_f32 v65, v153, v153, 0x409d30fc\n"
    "v_fmaak_f32 v66, v154, v154, 0x409d30fc\n"
    "v_fmaak_f32 v67, v155, v155, 0x409d30fc\n"
    "buffer_load_dwordx4 v2, s[12:15], s87 offen offset:1024 sc1 lds\n"
    "v_mul_f32 v64, v152, v64\n"
    "v_mul_f32 v65, v153, v65\n"
    "v_mul_f32 v66, v154, v66\n"
    "v_mul_f32 v67, v155, v67\n"
    "ds_read_b128 v[96:99], v4 offset:0\n"
    "v_exp_f32_e64 v64, -v64\n"
    "v_exp_f32_e64 v65, -v65\n"
    "v_exp_f32_e64 v66, -v66\n"
    "ds_read_b128 v[100:103], v4 offset:1024\n"
    "v_exp_f32_e64 v67, -v67\n"
    "buffer_load_dwordx4 v2, s[12:15], s87 offen offset:2048 sc1 lds\n"
    "v_fma_f32 v64, v64, s88, s88\n"
    "v_fma_f32 v65, v65, s88, s88\n"
    "v_fma_f32 v66, v66, s88, s88\n"
    "ds_read_b128 v[104:107], v4 offset:2048\n"
    "v_fma_f32 v67, v67, s88, s88\n"
    "v_rcp_f32 v64, v64\n"
    "v_rcp_f32 v65, v65\n"
    "ds_read_b128 v[108:111], v4 offset:3072\n"
    "v_rcp_f32 v66, v66\n"
    "v_rcp_f32 v67, v67\n"
    "buffer_load_dwordx4 v2, s[12:15], s87 offen offset:3072 sc1 lds\n"
    "v_mul_f32 v152, v152, v64\n"
    "v_mul_f32 v153, v153, v65\n"
    "v_mul_f32 v154, v154, v66\n"
    "v_mul_f32 v155, v155, v67\n"
    "v_cvt_pk_bf16_f32 v160, v152, v153\n"
    "v_cvt_pk_bf16_f32 v161, v154, v155\n"
    "v_min_u32 v160, -2, v160\n"
    "v_min_u32 v161, -2, v161\n"
    "s_waitcnt lgkmcnt(4)\n"
    "v_add_f32 v156, v156, v68\n"
    "v_add_f32 v157, v157, v69\n"
    "v_add_f32 v158, v158, v70\n"
    "v_add_f32 v159, v159, v71\n"
    "s_add_u32 s84, s67, 6144\n"
    "buffer_load_dwordx4 v[16:19], v248, s[12:15], s84 offen sc1\n"
    "s_setprio 1\n"
    "s_waitcnt lgkmcnt(3)\n"
    "v_mfma_f32_32x32x16_bf16 v[80:95], v[200:203], v[96:99], v[80:95]\n"
    "ds_read_b128 v[96:99], v5 offset:0\n"
    "v_fma_f32 v156, v156, v12, v132\n"
    "ds_read_b128 v[176:179], v10 offset:0\n"
    "ds_read_b128 v[180:183], v10 offset:1024\n"
    "ds_read_b32 v122, v14 offset:0\n"
    "s_waitcnt lgkmcnt(6)\n"
    "v_mfma_f32_32x32x16_bf16 v[80:95], v[204:207], v[100:103], v[80:95]\n"
    "ds_read_b128 v[100:103], v5 offset:1024\n"
    "v_fma_f32 v157, v157, v12, v133\n"
    "v_fma_f32 v158, v158, v12, v134\n"
    "v_fma_f32 v159, v159, v12, v135\n"
    "v_fmaak_f32 v68, v156, v156, 0x409d30fc\n"
    "v_fmaak_f32 v69, v157, v157, 0x409d30fc\n"
    "v_fmaak_f32 v70, v158, v158, 0x409d30fc\n"
    "v_fmaak_f32 v71, v159, v159, 0x409d30fc\n"
    "v_mul_f32 v68, v156, v68\n"
    "v_mul_f32 v69, v157, v69\n"
    "v_mul_f32 v70, v158, v70\n"
    "v_mul_f32 v71, v159, v71\n"
    "v_exp_f32_e64 v68, -v68\n"
    "v_exp_f32_e64 v69, -v69\n"
    "v_exp_f32_e64 v70, -v70\n"
    "v_exp_f32_e64 v71, -v71\n"
    "v_fma_f32 v68, v68, s88, s88\n"
    "s_waitcnt lgkmcnt(6)\n"
    "v_mfma_f32_32x32x16_bf16 v[80:95], v[208:211], v[104:107], v[80:95]\n"
    "ds_read_b128 v[104:107], v5 offset:2048\n"
    "v_fma_f32 v69, v69, s88, s88\n"
    "v_fma_f32 v70, v70, s88, s88\n"
    "v_fma_f32 v71, v71, s88, s88\n"
    "v_rcp_f32 v68, v68\n"
    "v_rcp_f32 v69, v69\n"
    "s_add_u32 s84, s67, 6400\n"
    "buffer_load_dwordx4 v[20:23], v248, s[12:15], s84 offen sc1\n"
    "v_rcp_f32 v70, v70\n"
    "v_rcp_f32 v71, v71\n"
    "v_mul_f32 v156, v156, v68\n"
    "v_mul_f32 v157, v157, v69\n"
    "v_mul_f32 v158, v158, v70\n"
    "v_mul_f32 v159, v159, v71\n"
    "v_cvt_pk_bf16_f32 v162, v156, v157\n"
    "v_cvt_pk_bf16_f32 v163, v158, v159\n"
    "v_min_u32 v162, -2, v162\n"
    "v_min_u32 v163, -2, v163\n"
    "s_add_u32 s54, s76, 114688\n"
    "buffer_store_dwordx4 v[160:163], v2, s[12:15], s54 offen sc1\n"
    "s_waitcnt lgkmcnt(6)\n"
    "v_mfma_f32_32x32x16_bf16 v[80:95], v[212:215], v[108:111], v[80:95]\n"
    "s_waitcnt lgkmcnt(3)\n"
    "v_add_f32 v176, v176, v180\n"
    "ds_read_b128 v[108:111], v5 offset:3072\n"
    "v_add_f32 v177, v177, v181\n"
    "v_add_f32 v178, v178, v182\n"
    "v_add_f32 v179, v179, v183\n"
    "v_add_f32 v176, v176, v177\n"
    "v_add_f32 v178, v178, v179\n"
    "v_add_f32 v176, v176, v178\n"
    "v_mov_b32 v177, v176\n"
    "s_nop 1\n"
    "v_permlane32_swap_b32 v176, v177\n"
    "v_mul_f32 v176, 0xbb000000, v176\n"
    "v_mfma_f32_32x32x16_bf16 v[80:95], v[216:219], v[96:99], v[80:95]\n"
    "ds_read_b128 v[96:99], v6 offset:0\n"
    "v_mul_f32 v177, 0x3b000000, v177\n"
    "v_cvt_pk_bf16_f32 v169, v176, v176\n"
    "v_fma_f32 v177, -v176, v176, v177\n"
    "v_and_b32 v178, s42, v169\n"
    "v_max_f32 v177, 0, v177\n"
    "v_sub_f32 v178, v176, v178\n"
    "v_add_f32 v177, 0x3727c5ac, v177\n"
    "s_add_u32 s84, s67, 120832\n"
    "buffer_load_dwordx4 v[24:27], v248, s[12:15], s84 offen sc1\n"
    "v_cvt_pk_bf16_f32 v168, v176, v178\n"
    "v_rsq_f32 v177, v177\n"
    "s_waitcnt vmcnt(4)\n"
    "ds_read_b128 v[136:139], v3 offset:32768\n"
    "s_waitcnt lgkmcnt(4)\n"
    "v_mfma_f32_32x32x16_bf16 v[80:95], v[220:223], v[100:103], v[80:95]\n"
    "v_mul_f32 v11, s88, v177\n"
    "ds_read_b128 v[100:103], v6 offset:1024\n"
    "ds_read_b128 v[140:143], v3 offset:33792\n"
    "v_mfma_f32_32x32x16_bf16 v[80:95], v[170:173], v[168:171], v[80:95]\n"
    "ds_read_b128 v[144:147], v3 offset:34816\n"
    "ds_read_b128 v[148:151], v3 offset:35840\n"
    "s_waitcnt lgkmcnt(7)\n"
    "v_mfma_f32_32x32x16_bf16 v[80:95], v[224:227], v[104:107], v[80:95]\n"
    "ds_read_b128 v[104:107], v6 offset:2048\n"
    "s_add_u32 s84, s67, 121088\n"
    "buffer_load_dwordx4 v[28:31], v248, s[12:15], s84 offen sc1\n"
    "s_waitcnt lgkmcnt(5)\n"
    "v_mfma_f32_4x4x4_16b_bf16 v[176:179], v[136:137], v[136:137], 0\n"
    "v_mfma_f32_16x16x32_bf16 v[180:183], v[124:127], v[136:139], 0\n"
    "v_mfma_f32_32x32x16_bf16 v[80:95], v[228:231], v[108:111], v[80:95]\n"
    "v_mfma_f32_4x4x4_16b_bf16 v[176:179], v[138:139], v[138:139], v[176:179]\n"
    "ds_read_b128 v[108:111], v6 offset:3072\n"
    "s_waitcnt lgkmcnt(4)\n"
    "v_mfma_f32_4x4x4_16b_bf16 v[176:179], v[140:141], v[140:141], v[176:179]\n"
    "v_mfma_f32_16x16x32_bf16 v[180:183], v[124:127], v[140:143], v[180:183]\n"
    "v_mfma_f32_32x32x16_bf16 v[80:95], v[48:51], v[96:99], v[80:95]\n"
    "v_mfma_f32_4x4x4_16b_bf16 v[176:179], v[142:143], v[142:143], v[176:179]\n"
    "s_add_u32 m0, s78, 88064\n"
    "s_add_u32 s87, s67, 12288\n"
    "buffer_load_dwordx4 v248, s[12:15], s87 offen sc1 lds\n"
    "s_waitcnt lgkmcnt(3)\n"
    "v_mfma_f32_4x4x4_16b_bf16 v[176:179], v[144:145], v[144:145], v[176:179]\n"
    "v_mfma_f32_16x16x32_bf16 v[180:183], v[124:127], v[144:147], v[180:183]\n"
    "v_mfma_f32_32x32x16_bf16 v[80:95], v[52:55], v[100:103], v[80:95]\n"
    "v_mfma_f32_4x4x4_16b_bf16 v[176:179], v[146:147], v[146:147], v[176:179]\n"
    "s_waitcnt lgkmcnt(2)\n"
    "s_nop 0\n"
    "v_mfma_f32_4x4x4_16b_bf16 v[176:179], v[148:149], v[148:149], v[176:179]\n"
    "v_mfma_f32_16x16x32_bf16 v[180:183], v[124:127], v[148:151], v[180:183]\n"
    "s_waitcnt lgkmcnt(1)\n"
    "v_mfma_f32_32x32x16_bf16 v[80:95], v[56:59], v[104:107], v[80:95]\n"
    "v_mfma_f32_4x4x4_16b_bf16 v[176:179], v[150:151], v[150:151], v[176:179]\n"
    "s_waitcnt lgkmcnt(0)\n"
    "v_mfma_f32_32x32x16_bf16 v[80:95], v[60:63], v[108:111], v[80:95]\n"
    "s_nop 2\n"
    "v_cmp_u_f32 vcc, v180, v180\n"
    "s_cbranch_vccnz hp_86_%=\n"
    "hb_87_%=:\n"
    "s_add_u32 m0, s78, 89088\n"
    "s_add_u32 s87, s67, 12544\n"
    "buffer_load_dwordx4 v248, s[12:15], s87 offen sc1 lds\n"
    "v_cndmask_b32 v176, v176, v177, s[8:9]\n"
    "v_cndmask_b32 v178, v178, v179, s[8:9]\n"
    "v_cndmask_b32 v176, v176, v178, s[10:11]\n"
    "v_mfma_f32_32x32x16_bf16 v[152:167], v[184:187], v[136:139], 0\n"
    "v_mov_b32 v177, v176\n"
    "s_nop 1\n"
    "v_permlane32_swap_b32 v176, v177\n"
    "v_add_f32 v176, v176, v177\n"
    "v_cndmask_b32 v176, v180, v176, s[60:61]\n"
    "ds_write_b32 v9, v176 offset:2048\n"
    "v_mfma_f32_32x32x16_bf16 v[152:167], v[188:191], v[140:143], v[152:167]\n"
    "s_add_u32 m0, s78, 90112\n"
    "s_add_u32 s87, s67, 126976\n"
    "buffer_load_dwordx4 v248, s[12:15], s87 offen sc1 lds\n"
    "ds_write_b128 v7, v[88:91]\n"
    "v_mfma_f32_32x32x16_bf16 v[152:167], v[192:195], v[144:147], v[152:167]\n"
    "ds_write_b128 v7, v[92:95] offset:1024\n"
    "v_mfma_f32_32x32x16_bf16 v[152:167], v[196:199], v[148:151], v[152:167]\n"
    "s_add_u32 m0, s78, 91136\n"
    "s_add_u32 s87, s67, 127232\n"
    "buffer_load_dwordx4 v248, s[12:15], s87 offen sc1 lds\n"
    "s_waitcnt lgkmcnt(0)\n"
    "s_barrier\n"
    "s_setprio 0\n"
    "ds_read_b128 v[64:67], v8\n"
    "ds_read_b128 v[68:71], v8 offset:1024\n"
    "s_add_u32 s86, s75, 0\n"
    "buffer_load_dwordx4 v[184:187], v248, s[12:15], s86 offen sc1\n"
    "s_waitcnt lgkmcnt(1)\n"
    "v_add_f32 v80, v80, v64\n"
    "v_add_f32 v81, v81, v65\n"
    "v_add_f32 v82, v82, v66\n"
    "v_add_f32 v83, v83, v67\n"
    "v_fma_f32 v80, v80, v11, v128\n"
    "v_fma_f32 v81, v81, v11, v129\n"
    "v_fma_f32 v82, v82, v11, v130\n"
    "v_fma_f32 v83, v83, v11, v131\n"
    "v_fmaak_f32 v64, v80, v80, 0x409d30fc\n"
    "v_fmaak_f32 v65, v81, v81, 0x409d30fc\n"
    "v_fmaak_f32 v66, v82, v82, 0x409d30fc\n"
    "v_fmaak_f32 v67, v83, v83, 0x409d30fc\n"
    "s_add_u32 s86, s75, 2048\n"
    "buffer_load_dwordx4 v[188:191], v248, s[12:15], s86 offen sc1\n"
    "v_mul_f32 v64, v80, v64\n"
    "v_mul_f32 v65, v81, v65\n"
    "v_mul_f32 v66, v82, v66\n"
    "v_mul_f32 v67, v83, v67\n"
    "ds_read_b128 v[96:99], v4 offset:32768\n"
    "v_exp_f32_e64 v64, -v64\n"
    "v_exp_f32_e64 v65, -v65\n"
    "v_exp_f32_e64 v66, -v66\n"
    "ds_read_b128 v[100:103], v4 offset:33792\n"
    "v_exp_f32_e64 v67, -v67\n"
    "s_add_u32 s86, s75, 4096\n"
    "buffer_load_dwordx4 v[192:195], v248, s[12:15], s86 offen sc1\n"
    "v_fma_f32 v64, v64, s88, s88\n"
    "v_fma_f32 v65, v65, s88, s88\n"
    "v_fma_f32 v66, v66, s88, s88\n"
    "ds_read_b128 v[104:107], v4 offset:34816\n"
    "v_fma_f32 v67, v67, s88, s88\n"
    "v_rcp_f32 v64, v64\n"
    "v_rcp_f32 v65, v65\n"
    "ds_read_b128 v[108:111], v4 offset:35840\n"
    "v_rcp_f32 v66, v66\n"
    "v_rcp_f32 v67, v67\n"
    "s_add_u32 s86, s75, 6144\n"
    "buffer_load_dwordx4 v[196:199], v248, s[12:15], s86 offen sc1\n"
    "v_mul_f32 v80, v80, v64\n"
    "v_mul_f32 v81, v81, v65\n"
    "v_mul_f32 v82, v82, v66\n"
    "v_mul_f32 v83, v83, v67\n"
    "v_cvt_pk_bf16_f32 v88, v80, v81\n"
    "v_cvt_pk_bf16_f32 v89, v82, v83\n"
    "v_min_u32 v88, -2, v88\n"
    "v_min_u32 v89, -2, v89\n"
    "s_waitcnt lgkmcnt(4)\n"
    "v_add_f32 v84, v84, v68\n"
    "v_add_f32 v85, v85, v69\n"
    "v_add_f32 v86, v86, v70\n"
    "v_add_f32 v87, v87, v71\n"
    "s_add_u32 s84, s67, 8192\n"
    "buffer_load_dwordx4 v[136:139], v248, s[12:15], s84 offen sc1\n"
    "s_setprio 1\n"
    "s_waitcnt lgkmcnt(3)\n"
    "v_mfma_f32_32x32x16_bf16 v[152:167], v[200:203], v[96:99], v[152:167]\n"
    "ds_read_b128 v[96:99], v5 offset:32768\n"
    "v_fma_f32 v84, v84, v11, v132\n"
    "s_waitcnt lgkmcnt(3)\n"
    "v_mfma_f32_32x32x16_bf16 v[152:167], v[204:207], v[100:103], v[152:167]\n"
    "ds_read_b128 v[100:103], v5 offset:33792\n"
    "v_fma_f32 v85, v85, v11, v133\n"
    "v_fma_f32 v86, v86, v11, v134\n"
    "v_fma_f32 v87, v87, v11, v135\n"
    "v_fmaak_f32 v68, v84, v84, 0x409d30fc\n"
    "s_add_u32 s84, s67, 8448\n"
    "buffer_load_dwordx4 v[140:143], v248, s[12:15], s84 offen sc1\n"
    "v_fmaak_f32 v69, v85, v85, 0x409d30fc\n"
    "v_fmaak_f32 v70, v86, v86, 0x409d30fc\n"
    "v_fmaak_f32 v71, v87, v87, 0x409d30fc\n"
    "v_mul_f32 v68, v84, v68\n"
    "v_mul_f32 v69, v85, v69\n"
    "v_mul_f32 v70, v86, v70\n"
    "ds_read_b128 v[176:179], v10 offset:2048\n"
    "ds_read_b128 v[180:183], v10 offset:3072\n"
    "ds_read_b32 v123, v14 offset:32768\n"
    "s_add_u32 s54, s57, 120832\n"
    "v_add_u32 v249, s54, v2\n"
    "s_waitcnt lgkmcnt(6)\n"
    "v_mfma_f32_32x32x16_bf16 v[152:167], v[208:211], v[104:107], v[152:167]\n"
    "v_mul_f32 v71, v87, v71\n"
    "ds_read_b128 v[104:107], v5 offset:34816\n"
    "v_exp_f32_e64 v68, -v68\n"
    "v_exp_f32_e64 v69, -v69\n"
    "v_add_u32 v15, 88064, v3\n"
    "v_exp_f32_e64 v70, -v70\n"
    "v_exp_f32_e64 v71, -v71\n"
    "v_fma_f32 v68, v68, s88, s88\n"
    "v_and_b32 v0, 31, v1\n"
    "v_lshlrev_b32 v0, 2, v0\n"
    "s_lshr_b32 s55, s65, 1\n"
    "s_lshl_b32 s55, s55, 7\n"
    "buffer_load_dword v252, v0, s[28:31], s55 offen\n"
    "s_and_b32 s54, s65, 1\n"
    "s_lshl3_add_u32 s54, s64, s54\n"
    "s_lshl3_add_u32 s54, s54, s3\n"
    "s_lshl_b32 s54, s54, 11\n"
    "s_add_u32 s77, s54, s55\n"
    "v_fma_f32 v69, v69, s88, s88\n"
    "v_fma_f32 v70, v70, s88, s88\n"
    "v_fma_f32 v71, v71, s88, s88\n"
    "v_and_b32 v250, 48, v1\n"
    "s_mul_i32 s54, s3, 4608\n"
    "s_add_u32 s54, s54, 32768\n"
    "v_add3_u32 v250, v250, v2, s54\n"
    "v_rcp_f32 v68, v68\n"
    "s_add_u32 s84, s67, 122880\n"
    "buffer_load_dwordx4 v[144:147], v248, s[12:15], s84 offen sc1\n"
    "v_rcp_f32 v69, v69\n"
    "s_waitcnt lgkmcnt(6)\n"
    "v_mfma_f32_32x32x16_bf16 v[152:167], v[212:215], v[108:111], v[152:167]\n"
    "ds_read_b128 v[108:111], v5 offset:35840\n"
    "v_rcp_f32 v70, v70\n"
    "v_lshrrev_b32 v254, 5, v1\n"
    "v_rcp_f32 v71, v71\n"
    "v_mul_f32 v84, v84, v68\n"
    "v_mul_f32 v85, v85, v69\n"
    "v_bfe_u32 v251, v1, 1, 4\n"
    "v_mul_f32 v86, v86, v70\n"
    "v_mul_f32 v87, v87, v71\n"
    "v_cvt_pk_bf16_f32 v90, v84, v85\n"
    "v_lshl_or_b32 v0, v254, 10, v0\n"
    "v_cvt_pk_bf16_f32 v91, v86, v87\n"
    "v_min_u32 v90, -2, v90\n"
    "v_min_u32 v91, -2, v91\n"
    "v_mul_u32_u24 v251, 272, v251\n"
    "s_add_u32 s54, s76, 229376\n"
    "buffer_store_dwordx4 v[88:91], v2, s[12:15], s54 offen sc1\n"
    "s_waitcnt lgkmcnt(6)\n"
    "v_mfma_f32_32x32x16_bf16 v[152:167], v[216:219], v[96:99], v[152:167]\n"
    "s_waitcnt lgkmcnt(3)\n"
    "v_add_f32 v176, v176, v180\n"
    "ds_read_b128 v[96:99], v6 offset:32768\n"
    "v_lshl_add_u32 v251, v254, 4, v251\n"
    "v_add_f32 v177, v177, v181\n"
    "v_add_f32 v178, v178, v182\n"
    "s_add_u32 s86, s75, 8192\n"
    "buffer_load_dwordx4 v[200:203], v248, s[12:15], s86 offen sc1\n"
    "v_and_b32 v254, 1, v1\n"
    "v_add_f32 v179, v179, v183\n"
    "v_add_f32 v176, v176, v177\n"
    "v_lshl_add_u32 v251, v254, 3, v251\n"
    "v_add_f32 v178, v178, v179\n"
    "v_add_f32 v176, v176, v178\n"
    "s_lshl_b32 s54, s3, 5\n"
    "s_add_u32 s54, s54, 32768\n"
    "v_add_u32 v251, s54, v251\n"
    "v_mfma_f32_32x32x16_bf16 v[152:167], v[220:223], v[100:103], v[152:167]\n"
    "ds_read_b128 v[100:103], v6 offset:33792\n"
    "v_mov_b32 v177, v176\n"
    "s_nop 1\n"
    "v_permlane32_swap_b32 v176, v177\n"
    "v_mul_f32 v176, 0xbb000000, v176\n"
    "v_mul_f32 v177, 0x3b000000, v177\n"
    "v_cvt_pk_bf16_f32 v169, v176, v176\n"
    "s_add_u32 s86, s75, 10240\n"
    "buffer_load_dwordx4 v[204:207], v248, s[12:15], s86 offen sc1\n"
    "v_fma_f32 v177, -v176, v176, v177\n"
    "ds_read_b128 v[148:151], v249 offset:0\n"
    "ds_read_b128 v[80:83], v249 offset:1024\n"
    "s_waitcnt lgkmcnt(5)\n"
    "v_mfma_f32_32x32x16_bf16 v[152:167], v[224:227], v[104:107], v[152:167]\n"
    "ds_read_b128 v[104:107], v6 offset:34816\n"
    "v_and_b32 v178, s42, v169\n"
    "v_max_f32 v177, 0, v177\n"
    "v_sub_f32 v178, v176, v178\n"
    "v_add_f32 v177, 0x3727c5ac, v177\n"
    "v_cvt_pk_bf16_f32 v168, v176, v178\n"
    "v_rsq_f32 v177, v177\n"
    "s_waitcnt lgkmcnt(5)\n"
    "v_mfma_f32_32x32x16_bf16 v[152:167], v[228:231], v[108:111], v[152:167]\n"
    "v_mul_f32 v12, s88, v177\n"
    "ds_read_b128 v[108:111], v6 offset:35840\n"
    "ds_read_b128 v[84:87], v249 offset:2048\n"
    "s_add_u32 s86, s75, 12288\n"
    "buffer_load_dwordx4 v[208:211], v248, s[12:15], s86 offen sc1\n"
    "v_mfma_f32_32x32x16_bf16 v[152:167], v[170:173], v[168:171], v[152:167]\n"
    "ds_read_b128 v[88:91], v249 offset:3072\n"
    "s_waitcnt lgkmcnt(7)\n"
    "v_mfma_f32_32x32x16_bf16 v[152:167], v[48:51], v[96:99], v[152:167]\n"
    "s_waitcnt vmcnt(12)\n"
    "ds_read_b128 v[96:99], v15 offset:0\n"
    "ds_read_b128 v[92:95], v249 offset:4096\n"
    "s_waitcnt lgkmcnt(8)\n"
    "v_mfma_f32_32x32x16_bf16 v[152:167], v[52:55], v[100:103], v[152:167]\n"
    "ds_read_b128 v[100:103], v15 offset:1024\n"
    "s_waitcnt lgkmcnt(6)\n"
    "v_mfma_f32_32x32x16_bf16 v[152:167], v[56:59], v[104:107], v[152:167]\n"
    "ds_read_b128 v[104:107], v15 offset:2048\n"
    "s_waitcnt lgkmcnt(6)\n"
    "v_mfma_f32_32x32x16_bf16 v[152:167], v[60:63], v[108:111], v[152:167]\n"
    "ds_read_b128 v[108:111], v15 offset:3072\n"
    "s_nop 10\n"
    "ds_write_b128 v8, v[160:163]\n"
    "ds_write_b128 v8, v[164:167] offset:1024\n"
    "s_setprio 1\n"
    "s_waitcnt lgkmcnt(0)\n"
    "s_barrier\n"
    "ds_read_b128 v[64:67], v7\n"
    "ds_read_b128 v[68:71], v7 offset:1024\n"
    "s_waitcnt vmcnt(5)\n"
    "v_and_b32 v253, s42, v252\n"
    "v_lshlrev_b32 v252, 16, v252\n"
    "v_add_u32 v4, 38912, v251\n"
    "s_waitcnt lgkmcnt(1)\n"
    "v_add_f32 v152, v152, v64\n"
    "v_add_f32 v153, v153, v65\n"
    "v_add_f32 v154, v154, v66\n"
    "v_add_f32 v155, v155, v67\n"
    "v_fma_f32 v152, v152, v12, v128\n"
    "v_fma_f32 v153, v153, v12, v129\n"
    "v_fma_f32 v154, v154, v12, v130\n"
    "v_fma_f32 v155, v155, v12, v131\n"
    "v_fmaak_f32 v64, v152, v152, 0x409d30fc\n"
    "v_fmaak_f32 v65, v153, v153, 0x409d30fc\n"
    "v_fmaak_f32 v66, v154, v154, 0x409d30fc\n"
    "v_fmaak_f32 v67, v155, v155, 0x409d30fc\n"
    "v_mul_f32 v64, v152, v64\n"
    "v_mul_f32 v65, v153, v65\n"
    "v_mul_f32 v66, v154, v66\n"
    "v_mul_f32 v67, v155, v67\n"
    "v_exp_f32_e64 v64, -v64\n"
    "v_exp_f32_e64 v65, -v65\n"
    "v_exp_f32_e64 v66, -v66\n"
    "v_exp_f32_e64 v67, -v67\n"
    "v_fma_f32 v64, v64, s88, s88\n"
    "v_fma_f32 v65, v65, s88, s88\n"
    "v_fma_f32 v66, v66, s88, s88\n"
    "v_fma_f32 v67, v67, s88, s88\n"
    "v_rcp_f32 v64, v64\n"
    "v_rcp_f32 v65, v65\n"
    "v_rcp_f32 v66, v66\n"
    "v_rcp_f32 v67, v67\n"
    "v_mul_f32 v152, v152, v64\n"
    "v_mul_f32 v153, v153, v65\n"
    "v_mul_f32 v154, v154, v66\n"
    "v_mul_f32 v155, v155, v67\n"
    "v_cvt_pk_bf16_f32 v160, v152, v153\n"
    "v_cvt_pk_bf16_f32 v161, v154, v155\n"
    "v_min_u32 v160, -2, v160\n"
    "v_min_u32 v161, -2, v161\n"
    "s_waitcnt lgkmcnt(0)\n"
    "v_add_f32 v156, v156, v68\n"
    "v_add_f32 v157, v157, v69\n"
    "v_add_f32 v158, v158, v70\n"
    "v_add_f32 v159, v159, v71\n"
    "v_fma_f32 v156, v156, v12, v132\n"
    "v_fma_f32 v157, v157, v12, v133\n"
    "v_fma_f32 v158, v158, v12, v134\n"
    "v_fma_f32 v159, v159, v12, v135\n"
    "v_fmaak_f32 v68, v156, v156, 0x409d30fc\n"
    "v_fmaak_f32 v69, v157, v157, 0x409d30fc\n"
    "v_fmaak_f32 v70, v158, v158, 0x409d30fc\n"
    "v_fmaak_f32 v71, v159, v159, 0x409d30fc\n"
    "v_mul_f32 v68, v156, v68\n"
    "v_mul_f32 v69, v157, v69\n"
    "v_mul_f32 v70, v158, v70\n"
    "v_mul_f32 v71, v159, v71\n"
    "v_exp_f32_e64 v68, -v68\n"
    "v_exp_f32_e64 v69, -v69\n"
    "v_exp_f32_e64 v70, -v70\n"
    "v_exp_f32_e64 v71, -v71\n"
    "v_fma_f32 v68, v68, s88, s88\n"
    "v_fma_f32 v69, v69, s88, s88\n"
    "v_fma_f32 v70, v70, s88, s88\n"
    "v_fma_f32 v71, v71, s88, s88\n"
    "v_rcp_f32 v68, v68\n"
    "v_rcp_f32 v69, v69\n"
    "v_rcp_f32 v70, v70\n"
    "v_rcp_f32 v71, v71\n"
    "v_mul_f32 v156, v156, v68\n"
    "v_mul_f32 v157, v157, v69\n"
    "v_mul_f32 v158, v158, v70\n"
    "v_mul_f32 v159, v159, v71\n"
    "v_cvt_pk_bf16_f32 v162, v156, v157\n"
    "v_cvt_pk_bf16_f32 v163, v158, v159\n"
    "v_min_u32 v162, -2, v162\n"
    "v_min_u32 v163, -2, v163\n"
    "s_add_u32 s54, s76, 344064\n"
    "buffer_store_dwordx4 v[160:163], v2, s[12:15], s54 offen sc1\n"
    "s_add_u32 s86, s75, 114688\n"
    "buffer_load_dwordx4 v[212:215], v248, s[12:15], s86 offen sc1\n"
    "s_setprio 1\n"
    "v_mfma_f32_16x16x32_bf16 v[168:171], v[72:75], v[184:187], 0\n"
    "v_mfma_f32_16x16x32_bf16 v[168:171], v[32:35], v[188:191], v[168:171]\n"
    "v_mfma_f32_16x16x32_bf16 v[168:171], v[232:235], v[192:195], v[168:171]\n"
    "v_mfma_f32_16x16x32_bf16 v[168:171], v[16:19], v[196:199], v[168:171]\n"
    "s_add_u32 s86, s75, 116736\n"
    "buffer_load_dwordx4 v[216:219], v248, s[12:15], s86 offen sc1\n"
    "s_waitcnt vmcnt(5)\n"
    "v_mfma_f32_16x16x32_bf16 v[168:171], v[136:139], v[200:203], v[168:171]\n"
    "s_waitcnt vmcnt(4)\n"
    "v_mfma_f32_16x16x32_bf16 v[168:171], v[80:83], v[204:207], v[168:171]\n"
    "s_waitcnt vmcnt(3)\n"
    "v_mfma_f32_16x16x32_bf16 v[168:171], v[96:99], v[208:211], v[168:171]\n"
    "v_mfma_f32_16x16x32_bf16 v[172:175], v[76:79], v[184:187], 0\n"
    "s_add_u32 s86, s75, 118784\n"
    "buffer_load_dwordx4 v[220:223], v248, s[12:15], s86 offen sc1\n"
    "s_setprio 2\n"
    "v_mfma_f32_16x16x32_bf16 v[172:175], v[36:39], v[188:191], v[172:175]\n"
    "s_nop 2\n"
    "v_cmp_u_f32 vcc, v168, v168\n"
    "s_cbranch_vccnz gp_89_%=\n"
    "gb_90_%=:\n"
    "ds_write_b128 v250, v[168:171] offset:0\n"
    "v_mfma_f32_16x16x32_bf16 v[172:175], v[236:239], v[192:195], v[172:175]\n"
    "v_mfma_f32_16x16x32_bf16 v[172:175], v[20:23], v[196:199], v[172:175]\n"
    "s_add_u32 s86, s75, 120832\n"
    "buffer_load_dwordx4 v[224:227], v248, s[12:15], s86 offen sc1\n"
    "v_mfma_f32_16x16x32_bf16 v[172:175], v[140:143], v[200:203], v[172:175]\n"
    "v_mfma_f32_16x16x32_bf16 v[172:175], v[84:87], v[204:207], v[172:175]\n"
    "v_mfma_f32_16x16x32_bf16 v[172:175], v[100:103], v[208:211], v[172:175]\n"
    "v_mfma_f32_16x16x32_bf16 v[176:179], v[112:115], v[184:187], 0\n"
    "s_add_u32 s86, s75, 122880\n"
    "buffer_load_dwordx4 v[228:231], v248, s[12:15], s86 offen sc1\n"
    "v_mfma_f32_16x16x32_bf16 v[176:179], v[40:43], v[188:191], v[176:179]\n"
    "s_nop 3\n"
    "ds_write_b128 v250, v[172:175] offset:1088\n"
    "v_mfma_f32_16x16x32_bf16 v[176:179], v[240:243], v[192:195], v[176:179]\n"
    "v_mfma_f32_16x16x32_bf16 v[176:179], v[24:27], v[196:199], v[176:179]\n"
    "v_mfma_f32_16x16x32_bf16 v[176:179], v[144:147], v[200:203], v[176:179]\n"
    "s_add_u32 s86, s75, 124928\n"
    "buffer_load_dwordx4 v[48:51], v248, s[12:15], s86 offen sc1\n"
    "v_mfma_f32_16x16x32_bf16 v[176:179], v[88:91], v[204:207], v[176:179]\n"
    "v_mfma_f32_16x16x32_bf16 v[176:179], v[104:107], v[208:211], v[176:179]\n"
    "v_mfma_f32_16x16x32_bf16 v[180:183], v[116:119], v[184:187], 0\n"
    "v_mfma_f32_16x16x32_bf16 v[180:183], v[44:47], v[188:191], v[180:183]\n"
    "s_add_u32 s86, s75, 126976\n"
    "buffer_load_dwordx4 v[52:55], v248, s[12:15], s86 offen sc1\n"
    "s_nop 3\n"
    "ds_write_b128 v250, v[176:179] offset:2176\n"
    "v_mfma_f32_16x16x32_bf16 v[180:183], v[244:247], v[192:195], v[180:183]\n"
    "v_mfma_f32_16x16x32_bf16 v[180:183], v[28:31], v[196:199], v[180:183]\n"
    "v_mfma_f32_16x16x32_bf16 v[180:183], v[148:151], v[200:203], v[180:183]\n"
    "v_mfma_f32_16x16x32_bf16 v[180:183], v[92:95], v[204:207], v[180:183]\n"
    "v_mfma_f32_16x16x32_bf16 v[180:183], v[108:111], v[208:211], v[180:183]\n"
    "s_nop 7\n"
    "ds_write_b128 v250, v[180:183] offset:3264\n"
    "s_setprio 0\n"
    "s_waitcnt lgkmcnt(0)\n"
    "s_barrier\n"
    "s_add_u32 s86, s75, 229376\n"
    "buffer_load_dwordx4 v[184:187], v248, s[12:15], s86 offen sc1\n"
    "ds_read_b64 v[56:57], v251 offset:0\n"
    "s_setprio 1\n"
    "s_waitcnt vmcnt(7)\n"
    "v_mfma_f32_16x16x32_bf16 v[152:155], v[72:75], v[212:215], 0\n"
    "ds_read_b64 v[58:59], v251 offset:4608\n"
    "ds_read_b64 v[60:61], v251 offset:9216\n"
    "s_waitcnt vmcnt(6)\n"
    "v_mfma_f32_16x16x32_bf16 v[152:155], v[32:35], v[216:219], v[152:155]\n"
    "ds_read_b64 v[62:63], v251 offset:13824\n"
    "ds_read_b64 v[64:65], v251 offset:18432\n"
    "s_waitcnt vmcnt(5)\n"
    "v_mfma_f32_16x16x32_bf16 v[152:155], v[232:235], v[220:223], v[152:155]\n"
    "ds_read_b64 v[66:67], v251 offset:23040\n"
    "ds_read_b64 v[68:69], v251 offset:27648\n"
    "ds_read_b64 v[70:71], v251 offset:32256\n"
    "s_waitcnt vmcnt(4)\n"
    "v_mfma_f32_16x16x32_bf16 v[152:155], v[16:19], v[224:227], v[152:155]\n"
    "s_add_u32 s86, s75, 231424\n"
    "buffer_load_dwordx4 v[188:191], v248, s[12:15], s86 offen sc1\n"
    "s_waitcnt vmcnt(4)\n"
    "v_mfma_f32_16x16x32_bf16 v[152:155], v[136:139], v[228:231], v[152:155]\n"
    "s_waitcnt vmcnt(3)\n"
    "v_mfma_f32_16x16x32_bf16 v[152:155], v[80:83], v[48:51], v[152:155]\n"
    "v_lshlrev_b32 v254, 16, v120\n"
    "s_waitcnt vmcnt(2)\n"
    "v_mfma_f32_16x16x32_bf16 v[152:155], v[96:99], v[52:55], v[152:155]\n"
    "v_mfma_f32_16x16x32_bf16 v[156:159], v[76:79], v[212:215], 0\n"
    "v_and_b32 v120, s42, v120\n"
    "s_add_u32 s86, s75, 233472\n"
    "buffer_load_dwordx4 v[192:195], v248, s[12:15], s86 offen sc1\n"
    "s_setprio 2\n"
    "v_mfma_f32_16x16x32_bf16 v[156:159], v[36:39], v[216:219], v[156:159]\n"
    "v_add_f32 v254, v254, v252\n"
    "v_add_f32 v120, v120, v253\n"
    "v_cmp_u_f32 vcc, v152, v152\n"
    "s_cbranch_vccnz gp_92_%=\n"
    "gb_93_%=:\n"
    "ds_write_b128 v250, v[152:155] offset:38912\n"
    "v_mfma_f32_16x16x32_bf16 v[156:159], v[236:239], v[220:223], v[156:159]\n"
    "s_waitcnt lgkmcnt(7)\n"
    "v_add_f32 v56, v56, v58\n"
    "v_mfma_f32_16x16x32_bf16 v[156:159], v[20:23], v[224:227], v[156:159]\n"
    "v_add_f32 v57, v57, v59\n"
    "s_add_u32 s86, s75, 235520\n"
    "buffer_load_dwordx4 v[196:199], v248, s[12:15], s86 offen sc1\n"
    "v_mfma_f32_16x16x32_bf16 v[156:159], v[140:143], v[228:231], v[156:159]\n"
    "s_waitcnt lgkmcnt(5)\n"
    "v_add_f32 v60, v60, v62\n"
    "v_mfma_f32_16x16x32_bf16 v[156:159], v[84:87], v[48:51], v[156:159]\n"
    "v_add_f32 v61, v61, v63\n"
    "v_mfma_f32_16x16x32_bf16 v[156:159], v[100:103], v[52:55], v[156:159]\n"
    "s_waitcnt lgkmcnt(3)\n"
    "v_add_f32 v64, v64, v66\n"
    "v_mfma_f32_16x16x32_bf16 v[160:163], v[112:115], v[212:215], 0\n"
    "v_add_f32 v65, v65, v67\n"
    "s_add_u32 s86, s75, 237568\n"
    "buffer_load_dwordx4 v[200:203], v248, s[12:15], s86 offen sc1\n"
    "v_mfma_f32_16x16x32_bf16 v[160:163], v[40:43], v[216:219], v[160:163]\n"
    "s_waitcnt lgkmcnt(1)\n"
    "v_add_f32 v68, v68, v70\n"
    "ds_write_b128 v250, v[156:159] offset:40000\n"
    "v_mfma_f32_16x16x32_bf16 v[160:163], v[240:243], v[220:223], v[160:163]\n"
    "v_add_f32 v69, v69, v71\n"
    "v_mfma_f32_16x16x32_bf16 v[160:163], v[24:27], v[224:227], v[160:163]\n"
    "v_add_f32 v56, v56, v60\n"
    "v_mfma_f32_16x16x32_bf16 v[160:163], v[144:147], v[228:231], v[160:163]\n"
    "v_add_f32 v57, v57, v61\n"
    "s_add_u32 s86, s75, 239616\n"
    "buffer_load_dwordx4 v[204:207], v248, s[12:15], s86 offen sc1\n"
    "v_mfma_f32_16x16x32_bf16 v[160:163], v[88:91], v[48:51], v[160:163]\n"
    "v_add_f32 v64, v64, v68\n"
    "v_mfma_f32_16x16x32_bf16 v[160:163], v[104:107], v[52:55], v[160:163]\n"
    "v_add_f32 v65, v65, v69\n"
    "v_mfma_f32_16x16x32_bf16 v[164:167], v[116:119], v[212:215], 0\n"
    "v_add_f32 v56, v56, v64\n"
    "v_mfma_f32_16x16x32_bf16 v[164:167], v[44:47], v[216:219], v[164:167]\n"
    "v_add_f32 v57, v57, v65\n"
    "s_add_u32 s86, s75, 241664\n"
    "buffer_load_dwordx4 v[208:211], v248, s[12:15], s86 offen sc1\n"
    "s_nop 0\n"
    "ds_write_b128 v250, v[160:163] offset:41088\n"
    "v_mfma_f32_16x16x32_bf16 v[164:167], v[244:247], v[220:223], v[164:167]\n"
    "v_add_f32 v56, v56, v254\n"
    "v_add_f32 v57, v57, v120\n"
    "v_mfma_f32_16x16x32_bf16 v[164:167], v[28:31], v[224:227], v[164:167]\n"
    "v_cvt_pk_bf16_f32 v56, v56, v57\n"
    "v_mfma_f32_16x16x32_bf16 v[164:167], v[148:151], v[228:231], v[164:167]\n"
    "s_add_u32 s54, s77, 0\n"
    "buffer_store_dword v56, v0, s[16:19], s54 offen sc0 sc1\n"
    "v_mfma_f32_16x16x32_bf16 v[164:167], v[92:95], v[48:51], v[164:167]\n"
    "v_mfma_f32_16x16x32_bf16 v[164:167], v[108:111], v[52:55], v[164:167]\n"
    "s_nop 7\n"
    "ds_write_b128 v250, v[164:167] offset:42176\n"
    "s_setprio 0\n"
    "s_waitcnt lgkmcnt(0)\n"
    "s_barrier\n"
    "s_add_u32 s86, s75, 344064\n"
    "buffer_load_dwordx4 v[212:215], v248, s[12:15], s86 offen sc1\n"
    "ds_read_b64 v[56:57], v4 offset:0\n"
    "s_setprio 1\n"
    "s_waitcnt vmcnt(8)\n"
    "v_mfma_f32_16x16x32_bf16 v[168:171], v[72:75], v[184:187], 0\n"
    "ds_read_b64 v[58:59], v4 offset:4608\n"
    "ds_read_b64 v[60:61], v4 offset:9216\n"
    "s_waitcnt vmcnt(7)\n"
    "v_mfma_f32_16x16x32_bf16 v[168:171], v[32:35], v[188:191], v[168:171]\n"
    "ds_read_b64 v[62:63], v4 offset:13824\n"
    "ds_read_b64 v[64:65], v4 offset:18432\n"
    "s_waitcnt vmcnt(6)\n"
    "v_mfma_f32_16x16x32_bf16 v[168:171], v[232:235], v[192:195], v[168:171]\n"
    "ds_read_b64 v[66:67], v4 offset:23040\n"
    "ds_read_b64 v[68:69], v4 offset:27648\n"
    "ds_read_b64 v[70:71], v4 offset:32256\n"
    "s_waitcnt vmcnt(5)\n"
    "v_mfma_f32_16x16x32_bf16 v[168:171], v[16:19], v[196:199], v[168:171]\n"
    "s_add_u32 s86, s75, 346112\n"
    "buffer_load_dwordx4 v[216:219], v248, s[12:15], s86 offen sc1\n"
    "s_waitcnt vmcnt(5)\n"
    "v_mfma_f32_16x16x32_bf16 v[168:171], v[136:139], v[200:203], v[168:171]\n"
    "s_waitcnt vmcnt(4)\n"
    "v_mfma_f32_16x16x32_bf16 v[168:171], v[80:83], v[204:207], v[168:171]\n"
    "v_lshlrev_b32 v254, 16, v121\n"
    "s_waitcnt vmcnt(3)\n"
    "v_mfma_f32_16x16x32_bf16 v[168:171], v[96:99], v[208:211], v[168:171]\n"
    "v_mfma_f32_16x16x32_bf16 v[172:175], v[76:79], v[184:187], 0\n"
    "v_and_b32 v121, s42, v121\n"
    "s_add_u32 s86, s75, 348160\n"
    "buffer_load_dwordx4 v[220:223], v248, s[12:15], s86 offen sc1\n"
    "s_setprio 2\n"
    "v_mfma_f32_16x16x32_bf16 v[172:175], v[36:39], v[188:191], v[172:175]\n"
    "v_add_f32 v254, v254, v252\n"
    "v_add_f32 v121, v121, v253\n"
    "v_cmp_u_f32 vcc, v168, v168\n"
    "s_cbranch_vccnz gp_95_%=\n"
    "gb_96_%=:\n"
    "ds_write_b128 v250, v[168:171] offset:0\n"
    "v_mfma_f32_16x16x32_bf16 v[172:175], v[236:239], v[192:195], v[172:175]\n"
    "s_waitcnt lgkmcnt(7)\n"
    "v_add_f32 v56, v56, v58\n"
    "v_mfma_f32_16x16x32_bf16 v[172:175], v[20:23], v[196:199], v[172:175]\n"
    "v_add_f32 v57, v57, v59\n"
    "s_add_u32 s86, s75, 350208\n"
    "buffer_load_dwordx4 v[224:227], v248, s[12:15], s86 offen sc1\n"
    "v_mfma_f32_16x16x32_bf16 v[172:175], v[140:143], v[200:203], v[172:175]\n"
    "s_waitcnt lgkmcnt(5)\n"
    "v_add_f32 v60, v60, v62\n"
    "v_mfma_f32_16x16x32_bf16 v[172:175], v[84:87], v[204:207], v[172:175]\n"
    "v_add_f32 v61, v61, v63\n"
    "v_mfma_f32_16x16x32_bf16 v[172:175], v[100:103], v[208:211], v[172:175]\n"
    "s_waitcnt lgkmcnt(3)\n"
    "v_add_f32 v64, v64, v66\n"
    "v_mfma_f32_16x16x32_bf16 v[176:179], v[112:115], v[184:187], 0\n"
    "v_add_f32 v65, v65, v67\n"
    "s_add_u32 s86, s75, 352256\n"
    "buffer_load_dwordx4 v[228:231], v248, s[12:15], s86 offen sc1\n"
    "v_mfma_f32_16x16x32_bf16 v[176:179], v[40:43], v[188:191], v[176:179]\n"
    "s_waitcnt lgkmcnt(1)\n"
    "v_add_f32 v68, v68, v70\n"
    "ds_write_b128 v250, v[172:175] offset:1088\n"
    "v_mfma_f32_16x16x32_bf16 v[176:179], v[240:243], v[192:195], v[176:179]\n"
    "v_add_f32 v69, v69, v71\n"
    "v_mfma_f32_16x16x32_bf16 v[176:179], v[24:27], v[196:199], v[176:179]\n"
    "v_add_f32 v56, v56, v60\n"
    "v_mfma_f32_16x16x32_bf16 v[176:179], v[144:147], v[200:203], v[176:179]\n"
    "v_add_f32 v57, v57, v61\n"
    "s_add_u32 s86, s75, 354304\n"
    "buffer_load_dwordx4 v[48:51], v248, s[12:15], s86 offen sc1\n"
    "v_mfma_f32_16x16x32_bf16 v[176:179], v[88:91], v[204:207], v[176:179]\n"
    "v_add_f32 v64, v64, v68\n"
    "v_mfma_f32_16x16x32_bf16 v[176:179], v[104:107], v[208:211], v[176:179]\n"
    "v_add_f32 v65, v65, v69\n"
    "v_mfma_f32_16x16x32_bf16 v[180:183], v[116:119], v[184:187], 0\n"
    "v_add_f32 v56, v56, v64\n"
    "v_mfma_f32_16x16x32_bf16 v[180:183], v[44:47], v[188:191], v[180:183]\n"
    "v_add_f32 v57, v57, v65\n"
    "s_add_u32 s86, s75, 356352\n"
    "buffer_load_dwordx4 v[52:55], v248, s[12:15], s86 offen sc1\n"
    "s_nop 0\n"
    "ds_write_b128 v250, v[176:179] offset:2176\n"
    "v_mfma_f32_16x16x32_bf16 v[180:183], v[244:247], v[192:195], v[180:183]\n"
    "v_add_f32 v56, v56, v254\n"
    "v_add_f32 v57, v57, v121\n"
    "v_mfma_f32_16x16x32_bf16 v[180:183], v[28:31], v[196:199], v[180:183]\n"
    "v_cvt_pk_bf16_f32 v56, v56, v57\n"
    "v_mfma_f32_16x16x32_bf16 v[180:183], v[148:151], v[200:203], v[180:183]\n"
    "s_add_u32 s54, s77, 32768\n"
    "buffer_store_dword v56, v0, s[16:19], s54 offen sc0 sc1\n"
    "v_mfma_f32_16x16x32_bf16 v[180:183], v[92:95], v[204:207], v[180:183]\n"
    "v_mfma_f32_16x16x32_bf16 v[180:183], v[108:111], v[208:211], v[180:183]\n"
    "s_nop 7\n"
    "ds_write_b128 v250, v[180:183] offset:3264\n"
    "s_setprio 0\n"
    "s_waitcnt lgkmcnt(0)\n"
    "s_barrier\n"
    "ds_read_b64 v[56:57], v251 offset:0\n"
    "s_setprio 1\n"
    "s_waitcnt vmcnt(7)\n"
    "v_mfma_f32_16x16x32_bf16 v[152:155], v[72:75], v[212:215], 0\n"
    "ds_read_b64 v[58:59], v251 offset:4608\n"
    "ds_read_b64 v[60:61], v251 offset:9216\n"
    "s_waitcnt vmcnt(6)\n"
    "v_mfma_f32_16x16x32_bf16 v[152:155], v[32:35], v[216:219], v[152:155]\n"
    "ds_read_b64 v[62:63], v251 offset:13824\n"
    "ds_read_b64 v[64:65], v251 offset:18432\n"
    "s_waitcnt vmcnt(5)\n"
    "v_mfma_f32_16x16x32_bf16 v[152:155], v[232:235], v[220:223], v[152:155]\n"
    "ds_read_b64 v[66:67], v251 offset:23040\n"
    "ds_read_b64 v[68:69], v251 offset:27648\n"
    "ds_read_b64 v[70:71], v251 offset:32256\n"
    "s_waitcnt vmcnt(4)\n"
    "v_mfma_f32_16x16x32_bf16 v[152:155], v[16:19], v[224:227], v[152:155]\n"
    "s_waitcnt vmcnt(3)\n"
    "v_mfma_f32_16x16x32_bf16 v[152:155], v[136:139], v[228:231], v[152:155]\n"
    "s_waitcnt vmcnt(2)\n"
    "v_mfma_f32_16x16x32_bf16 v[152:155], v[80:83], v[48:51], v[152:155]\n"
    "v_lshlrev_b32 v254, 16, v122\n"
    "s_waitcnt vmcnt(1)\n"
    "v_mfma_f32_16x16x32_bf16 v[152:155], v[96:99], v[52:55], v[152:155]\n"
    "v_mfma_f32_16x16x32_bf16 v[156:159], v[76:79], v[212:215], 0\n"
    "v_and_b32 v122, s42, v122\n"
    "s_setprio 2\n"
    "v_mfma_f32_16x16x32_bf16 v[156:159], v[36:39], v[216:219], v[156:159]\n"
    "v_add_f32 v254, v254, v252\n"
    "v_add_f32 v122, v122, v253\n"
    "s_nop 1\n"
    "v_cmp_u_f32 vcc, v152, v152\n"
    "s_cbranch_vccnz gp_98_%=\n"
    "gb_99_%=:\n"
    "ds_write_b128 v250, v[152:155] offset:38912\n"
    "v_mfma_f32_16x16x32_bf16 v[156:159], v[236:239], v[220:223], v[156:159]\n"
    "s_waitcnt lgkmcnt(7)\n"
    "v_add_f32 v56, v56, v58\n"
    "v_mfma_f32_16x16x32_bf16 v[156:159], v[20:23], v[224:227], v[156:159]\n"
    "v_add_f32 v57, v57, v59\n"
    "v_mfma_f32_16x16x32_bf16 v[156:159], v[140:143], v[228:231], v[156:159]\n"
    "s_waitcnt lgkmcnt(5)\n"
    "v_add_f32 v60, v60, v62\n"
    "v_mfma_f32_16x16x32_bf16 v[156:159], v[84:87], v[48:51], v[156:159]\n"
    "v_add_f32 v61, v61, v63\n"
    "v_mfma_f32_16x16x32_bf16 v[156:159], v[100:103], v[52:55], v[156:159]\n"
    "s_waitcnt lgkmcnt(3)\n"
    "v_add_f32 v64, v64, v66\n"
    "v_mfma_f32_16x16x32_bf16 v[160:163], v[112:115], v[212:215], 0\n"
    "v_add_f32 v65, v65, v67\n"
    "v_mfma_f32_16x16x32_bf16 v[160:163], v[40:43], v[216:219], v[160:163]\n"
    "s_waitcnt lgkmcnt(1)\n"
    "v_add_f32 v68, v68, v70\n"
    "s_nop 0\n"
    "ds_write_b128 v250, v[156:159] offset:40000\n"
    "v_mfma_f32_16x16x32_bf16 v[160:163], v[240:243], v[220:223], v[160:163]\n"
    "v_add_f32 v69, v69, v71\n"
    "v_mfma_f32_16x16x32_bf16 v[160:163], v[24:27], v[224:227], v[160:163]\n"
    "v_add_f32 v56, v56, v60\n"
    "v_mfma_f32_16x16x32_bf16 v[160:163], v[144:147], v[228:231], v[160:163]\n"
    "v_add_f32 v57, v57, v61\n"
    "v_mfma_f32_16x16x32_bf16 v[160:163], v[88:91], v[48:51], v[160:163]\n"
    "v_add_f32 v64, v64, v68\n"
    "v_mfma_f32_16x16x32_bf16 v[160:163], v[104:107], v[52:55], v[160:163]\n"
    "v_add_f32 v65, v65, v69\n"
    "v_mfma_f32_16x16x32_bf16 v[164:167], v[116:119], v[212:215], 0\n"
    "v_add_f32 v56, v56, v64\n"
    "v_mfma_f32_16x16x32_bf16 v[164:167], v[44:47], v[216:219], v[164:167]\n"
    "v_add_f32 v57, v57, v65\n"
    "s_nop 2\n"
    "ds_write_b128 v250, v[160:163] offset:41088\n"
    "v_mfma_f32_16x16x32_bf16 v[164:167], v[244:247], v[220:223], v[164:167]\n"
    "v_add_f32 v56, v56, v254\n"
    "v_add_f32 v57, v57, v122\n"
    "v_mfma_f32_16x16x32_bf16 v[164:167], v[28:31], v[224:227], v[164:167]\n"
    "v_cvt_pk_bf16_f32 v56, v56, v57\n"
    "v_mfma_f32_16x16x32_bf16 v[164:167], v[148:151], v[228:231], v[164:167]\n"
    "s_add_u32 s54, s77, 65536\n"
    "buffer_store_dword v56, v0, s[16:19], s54 offen sc0 sc1\n"
    "v_mfma_f32_16x16x32_bf16 v[164:167], v[92:95], v[48:51], v[164:167]\n"
    "v_mfma_f32_16x16x32_bf16 v[164:167], v[108:111], v[52:55], v[164:167]\n"
    "s_nop 7\n"
    "ds_write_b128 v250, v[164:167] offset:42176\n"
    "s_setprio 0\n"
    "s_waitcnt lgkmcnt(0)\n"
    "s_barrier\n"
    "ds_read_b64 v[56:57], v4 offset:0\n"
    "ds_read_b64 v[58:59], v4 offset:4608\n"
    "ds_read_b64 v[60:61], v4 offset:9216\n"
    "ds_read_b64 v[62:63], v4 offset:13824\n"
    "ds_read_b64 v[64:65], v4 offset:18432\n"
    "ds_read_b64 v[66:67], v4 offset:23040\n"
    "ds_read_b64 v[68:69], v4 offset:27648\n"
    "ds_read_b64 v[70:71], v4 offset:32256\n"
    "v_lshlrev_b32 v254, 16, v123\n"
    "v_and_b32 v123, s42, v123\n"
    "v_add_f32 v254, v254, v252\n"
    "v_add_f32 v123, v123, v253\n"
    "s_waitcnt lgkmcnt(6)\n"
    "v_add_f32 v56, v56, v58\n"
    "v_add_f32 v57, v57, v59\n"
    "s_waitcnt lgkmcnt(4)\n"
    "v_add_f32 v60, v60, v62\n"
    "v_add_f32 v61, v61, v63\n"
    "s_waitcnt lgkmcnt(2)\n"
    "v_add_f32 v64, v64, v66\n"
    "v_add_f32 v65, v65, v67\n"
    "s_waitcnt lgkmcnt(0)\n"
    "v_add_f32 v68, v68, v70\n"
    "v_add_f32 v69, v69, v71\n"
    "v_add_f32 v56, v56, v60\n"
    "v_add_f32 v57, v57, v61\n"
    "v_add_f32 v64, v64, v68\n"
    "v_add_f32 v65, v65, v69\n"
    "v_add_f32 v56, v56, v64\n"
    "v_add_f32 v57, v57, v65\n"
    "v_add_f32 v56, v56, v254\n"
    "v_add_f32 v57, v57, v123\n"
    "v_cvt_pk_bf16_f32 v56, v56, v57\n"
    "s_add_u32 s54, s77, 98304\n"
    "buffer_store_dword v56, v0, s[16:19], s54 offen sc0 sc1\n"
    "r0_52_%=:\n"
    "v_mov_b32 v252, s40\n"
    "v_mov_b32 v253, s41\n"
    "Lfin_%=:\n"
    "s_or_b32 s43, s2, s3\n"
    "s_cbranch_scc1 Lep_%=\n"
    "s_mov_b32 s43, 19922944\n"
    "s_mov_b64 exec, 1\n"
    "buffer_store_dwordx2 v[252:253], off, s[12:15], s43 sc1\n"
    "Lep_%=:\n"
    "s_endpgm\n"
    "Lhot_%=:\n"
    "xp_2_%=:\n"
    "buffer_load_dwordx3 v[204:206], v187, s[12:15], s86 offen sc1\n"
    "s_waitcnt vmcnt(0)\n"
    "v_cmp_ne_u32 vcc, s40, v205\n"
    "v_cmp_ne_u32 s[44:45], s41, v206\n"
    "s_or_b64 vcc, vcc, s[44:45]\n"
    "s_cbranch_scc0 xb_3_%=\n"
    "s_sleep 2\n"
    "s_sub_i32 s0, s0, 1\n"
    "s_cmp_lt_i32 s0, 0\n"
    "s_cbranch_scc0 xp_2_%=\n"
    "s_branch xb_3_%=\n"
    "kp_4_%=:\n"
    "s_xor_b32 s53, s89, 4096\n"
    "buffer_load_dwordx4 v[148:151], v2, s[12:15], s53 offen sc1\n"
    "s_mov_b64 exec, s[46:47]\n"
    "buffer_load_dwordx4 v[152:155], v248, s[12:15], s53 offen offset:0 sc1\n"
    "s_mov_b64 exec, -1\n"
    "s_mov_b64 exec, s[46:47]\n"
    "buffer_load_dwordx4 v[156:159], v248, s[12:15], s53 offen offset:256 sc1\n"
    "s_mov_b64 exec, -1\n"
    "s_waitcnt vmcnt(2)\n"
    "v_max3_u32 v0, v148, v149, v150\n"
    "s_waitcnt vmcnt(1)\n"
    "v_max3_u32 v0, v151, v152, v0\n"
    "v_max3_u32 v0, v153, v154, v0\n"
    "s_waitcnt vmcnt(0)\n"
    "v_max3_u32 v0, v155, v156, v0\n"
    "v_max3_u32 v0, v157, v158, v0\n"
    "v_max_u32 v0, v159, v0\n"
    "v_cmp_eq_u32 vcc, -1, v0\n"
    "s_cbranch_vccz kb_5_%=\n"
    "s_sleep 2\n"
    "s_sub_i32 s0, s0, 1\n"
    "s_cmp_lt_i32 s0, 0\n"
    "s_cbranch_scc0 kp_4_%=\n"
    "s_branch kb_5_%=\n"
    "kp_6_%=:\n"
    "s_xor_b32 s53, s89, 8192\n"
    "buffer_load_dwordx4 v[4:7], v2, s[12:15], s53 offen sc1\n"
    "s_mov_b64 exec, s[46:47]\n"
    "buffer_load_dwordx4 v[8:11], v248, s[12:15], s53 offen offset:0 sc1\n"
    "s_mov_b64 exec, -1\n"
    "s_mov_b64 exec, s[46:47]\n"
    "buffer_load_dwordx4 v[12:15], v248, s[12:15], s53 offen offset:256 sc1\n"
    "s_mov_b64 exec, -1\n"
    "s_waitcnt vmcnt(2)\n"
    "v_max3_u32 v0, v4, v5, v6\n"
    "s_waitcnt vmcnt(1)\n"
    "v_max3_u32 v0, v7, v8, v0\n"
    "v_max3_u32 v0, v9, v10, v0\n"
    "s_waitcnt vmcnt(0)\n"
    "v_max3_u32 v0, v11, v12, v0\n"
    "v_max3_u32 v0, v13, v14, v0\n"
    "v_max_u32 v0, v15, v0\n"
    "v_cmp_eq_u32 vcc, -1, v0\n"
    "s_cbranch_vccz kb_7_%=\n"
    "s_sleep 2\n"
    "s_sub_i32 s0, s0, 1\n"
    "s_cmp_lt_i32 s0, 0\n"
    "s_cbranch_scc0 kp_6_%=\n"
    "s_branch kb_7_%=\n"
    "kp_8_%=:\n"
    "s_xor_b32 s53, s89, 12288\n"
    "buffer_load_dwordx4 v[160:163], v2, s[12:15], s53 offen sc1\n"
    "s_mov_b64 exec, s[46:47]\n"
    "buffer_load_dwordx4 v[164:167], v248, s[12:15], s53 offen offset:0 sc1\n"
    "s_mov_b64 exec, -1\n"
    "s_mov_b64 exec, s[46:47]\n"
    "buffer_load_dwordx4 v[168:171], v248, s[12:15], s53 offen offset:256 sc1\n"
    "s_mov_b64 exec, -1\n"
    "s_waitcnt vmcnt(2)\n"
    "v_max3_u32 v0, v160, v161, v162\n"
    "s_waitcnt vmcnt(1)\n"
    "v_max3_u32 v0, v163, v164, v0\n"
    "v_max3_u32 v0, v165, v166, v0\n"
    "s_waitcnt vmcnt(0)\n"
    "v_max3_u32 v0, v167, v168, v0\n"
    "v_max3_u32 v0, v169, v170, v0\n"
    "v_max_u32 v0, v171, v0\n"
    "v_cmp_eq_u32 vcc, -1, v0\n"
    "s_cbranch_vccz kb_9_%=\n"
    "s_sleep 2\n"
    "s_sub_i32 s0, s0, 1\n"
    "s_cmp_lt_i32 s0, 0\n"
    "s_cbranch_scc0 kp_8_%=\n"
    "s_branch kb_9_%=\n"
    "kp_10_%=:\n"
    "s_xor_b32 s53, s89, 16384\n"
    "buffer_load_dwordx4 v[172:175], v2, s[12:15], s53 offen sc1\n"
    "s_mov_b64 exec, s[46:47]\n"
    "buffer_load_dwordx4 v[176:179], v248, s[12:15], s53 offen offset:0 sc1\n"
    "s_mov_b64 exec, -1\n"
    "s_mov_b64 exec, s[46:47]\n"
    "buffer_load_dwordx4 v[180:183], v248, s[12:15], s53 offen offset:256 sc1\n"
    "s_mov_b64 exec, -1\n"
    "s_waitcnt vmcnt(2)\n"
    "v_max3_u32 v0, v172, v173, v174\n"
    "s_waitcnt vmcnt(1)\n"
    "v_max3_u32 v0, v175, v176, v0\n"
    "v_max3_u32 v0, v177, v178, v0\n"
    "s_waitcnt vmcnt(0)\n"
    "v_max3_u32 v0, v179, v180, v0\n"
    "v_max3_u32 v0, v181, v182, v0\n"
    "v_max_u32 v0, v183, v0\n"
    "v_cmp_eq_u32 vcc, -1, v0\n"
    "s_cbranch_vccz kb_11_%=\n"
    "s_sleep 2\n"
    "s_sub_i32 s0, s0, 1\n"
    "s_cmp_lt_i32 s0, 0\n"
    "s_cbranch_scc0 kp_10_%=\n"
    "s_branch kb_11_%=\n"
    "kp_12_%=:\n"
    "s_xor_b32 s53, s89, 20480\n"
    "buffer_load_dwordx4 v[136:139], v2, s[12:15], s53 offen sc1\n"
    "s_mov_b64 exec, s[46:47]\n"
    "buffer_load_dwordx4 v[140:143], v248, s[12:15], s53 offen offset:0 sc1\n"
    "s_mov_b64 exec, -1\n"
    "s_mov_b64 exec, s[46:47]\n"
    "buffer_load_dwordx4 v[144:147], v248, s[12:15], s53 offen offset:256 sc1\n"
    "s_mov_b64 exec, -1\n"
    "s_waitcnt vmcnt(2)\n"
    "v_max3_u32 v0, v136, v137, v138\n"
    "s_waitcnt vmcnt(1)\n"
    "v_max3_u32 v0, v139, v140, v0\n"
    "v_max3_u32 v0, v141, v142, v0\n"
    "s_waitcnt vmcnt(0)\n"
    "v_max3_u32 v0, v143, v144, v0\n"
    "v_max3_u32 v0, v145, v146, v0\n"
    "v_max_u32 v0, v147, v0\n"
    "v_cmp_eq_u32 vcc, -1, v0\n"
    "s_cbranch_vccz kb_13_%=\n"
    "s_sleep 2\n"
    "s_sub_i32 s0, s0, 1\n"
    "s_cmp_lt_i32 s0, 0\n"
    "s_cbranch_scc0 kp_12_%=\n"
    "s_branch kb_13_%=\n"
    "kvp_14_%=:\n"
    "buffer_load_dwordx2 v[250:251], v244, s[12:15], s1 offen offset:8 sc1\n"
    "s_waitcnt vmcnt(0)\n"
    "v_cmp_eq_u64 s[48:49], s[40:41], v[250:251]\n"
    "s_andn2_b32 s54, 62, s48\n"
    "s_cbranch_scc0 kvb_15_%=\n"
    "s_sleep 2\n"
    "s_sub_i32 s0, s0, 1\n"
    "s_cmp_lt_i32 s0, 0\n"
    "s_cbranch_scc0 kvp_14_%=\n"
    "s_branch kvb_15_%=\n"
    "kvp_17_%=:\n"
    "buffer_load_dwordx2 v[250:251], v244, s[12:15], s1 offen offset:0 sc1\n"
    "s_waitcnt vmcnt(0)\n"
    "v_cmp_eq_u64 s[48:49], s[40:41], v[250:251]\n"
    "s_andn2_b32 s54, 65535, s48\n"
    "s_cbranch_scc0 kvb_18_%=\n"
    "s_sleep 2\n"
    "s_sub_i32 s0, s0, 1\n"
    "s_cmp_lt_i32 s0, 0\n"
    "s_cbranch_scc0 kvp_17_%=\n"
    "s_branch kvb_18_%=\n"
    "rp_21_%=:\n"
    "s_mov_b32 s54, 19988480\n"
    "s_mov_b32 exec_hi, 63\n"
    "buffer_load_dwordx2 v[184:185], v0, s[12:15], s54 offen sc1\n"
    "s_mov_b32 exec_hi, -1\n"
    "s_waitcnt vmcnt(0)\n"
    "v_cmp_ne_u64 vcc, s[40:41], v[184:185]\n"
    "s_and_b32 s54, vcc_hi, 63\n"
    "s_or_b32 s54, s54, vcc_lo\n"
    "s_cbranch_scc0 rb_22_%=\n"
    "s_sleep 2\n"
    "s_sub_i32 s0, s0, 1\n"
    "s_cmp_lt_i32 s0, 0\n"
    "s_cbranch_scc0 rp_21_%=\n"
    "s_branch rb_22_%=\n"
    "fb_23_%=:\n"
    "s_and_b32 s54, s89, 0xffff0000\n"
    "s_waitcnt vmcnt(8)\n"
    "v_mov_b32 v212, 0xff800000\n"
    "f1_25_%=:\n"
    "buffer_load_dwordx4 v[200:203], v2, s[12:15], s54 offen sc1\n"
    "s_add_u32 s54, s54, 1024\n"
    "s_waitcnt vmcnt(0)\n"
    "v_mfma_f32_32x32x16_bf16 v[184:199], v[200:203], v[120:123], 0\n"
    "s_nop 11\n"
    "v_max3_f32 v212, v184, v185, v212\n"
    "v_max3_f32 v212, v186, v187, v212\n"
    "v_max3_f32 v212, v188, v189, v212\n"
    "v_max3_f32 v212, v190, v191, v212\n"
    "v_max3_f32 v212, v192, v193, v212\n"
    "v_max3_f32 v212, v194, v195, v212\n"
    "v_max3_f32 v212, v196, v197, v212\n"
    "v_max3_f32 v212, v198, v199, v212\n"
    "s_and_b32 s55, s54, 0xffff\n"
    "s_cbranch_scc1 f1_25_%=\n"
    "s_sub_u32 s54, s54, 0x10000\n"
    "v_mov_b32 v213, v212\n"
    "s_nop 1\n"
    "v_permlane32_swap_b32 v212, v213\n"
    "v_max_f32 v212, v212, v213\n"
    "v_and_b32 v204, 11, v1\n"
    "v_mov_b32 v205, s62\n"
    "v_cmp_eq_u32 vcc, 0, v204\n"
    "s_nop 1\n"
    "v_cndmask_b32 v204, 0, v205, vcc\n"
    "v_mov_b32 v205, v204\n"
    "v_mov_b32 v206, v204\n"
    "v_mov_b32 v207, v204\n"
    "v_mov_b32 v208, v204\n"
    "v_mov_b32 v209, v204\n"
    "v_mov_b32 v210, v204\n"
    "v_mov_b32 v211, v204\n"
    "v_mov_b32 v16, 0\n"
    "v_mov_b32 v17, 0\n"
    "v_mov_b32 v18, 0\n"
    "v_mov_b32 v19, 0\n"
    "v_mov_b32 v20, 0\n"
    "v_mov_b32 v21, 0\n"
    "v_mov_b32 v22, 0\n"
    "v_mov_b32 v23, 0\n"
    "v_mov_b32 v24, 0\n"
    "v_mov_b32 v25, 0\n"
    "v_mov_b32 v26, 0\n"
    "v_mov_b32 v27, 0\n"
    "v_mov_b32 v28, 0\n"
    "v_mov_b32 v29, 0\n"
    "v_mov_b32 v30, 0\n"
    "v_mov_b32 v31, 0\n"
    "f2_26_%=:\n"
    "buffer_load_dwordx4 v[200:203], v2, s[12:15], s54 offen sc1\n"
    "s_mov_b64 exec, s[46:47]\n"
    "buffer_load_dwordx4 v[204:207], v248, s[12:15], s54 offen sc1\n"
    "buffer_load_dwordx4 v[208:211], v248, s[12:15], s54 offen offset:256 sc1\n"
    "s_mov_b64 exec, -1\n"
    "s_add_u32 s54, s54, 1024\n"
    "s_waitcnt vmcnt(2)\n"
    "v_mfma_f32_32x32x16_bf16 v[184:199], v[200:203], v[120:123], 0\n"
    "s_nop 11\n"
    "v_sub_f32 v184, v184, v212\n"
    "v_sub_f32 v185, v185, v212\n"
    "v_sub_f32 v186, v186, v212\n"
    "v_sub_f32 v187, v187, v212\n"
    "v_sub_f32 v188, v188, v212\n"
    "v_sub_f32 v189, v189, v212\n"
    "v_sub_f32 v190, v190, v212\n"
    "v_sub_f32 v191, v191, v212\n"
    "v_sub_f32 v192, v192, v212\n"
    "v_sub_f32 v193, v193, v212\n"
    "v_sub_f32 v194, v194, v212\n"
    "v_sub_f32 v195, v195, v212\n"
    "v_sub_f32 v196, v196, v212\n"
    "v_sub_f32 v197, v197, v212\n"
    "v_sub_f32 v198, v198, v212\n"
    "v_sub_f32 v199, v199, v212\n"
    "v_exp_f32 v184, v184\n"
    "v_exp_f32 v185, v185\n"
    "v_exp_f32 v186, v186\n"
    "v_exp_f32 v187, v187\n"
    "v_exp_f32 v188, v188\n"
    "v_exp_f32 v189, v189\n"
    "v_exp_f32 v190, v190\n"
    "v_exp_f32 v191, v191\n"
    "v_exp_f32 v192, v192\n"
    "v_exp_f32 v193, v193\n"
    "v_exp_f32 v194, v194\n"
    "v_exp_f32 v195, v195\n"
    "v_exp_f32 v196, v196\n"
    "v_exp_f32 v197, v197\n"
    "v_exp_f32 v198, v198\n"
    "v_exp_f32 v199, v199\n"
    "s_waitcnt vmcnt(1)\n"
    "v_smfmac_f32_32x32x32_bf16 v[16:31], v[204:207], v[184:191], v112\n"
    "s_waitcnt vmcnt(0)\n"
    "v_smfmac_f32_32x32x32_bf16 v[16:31], v[208:211], v[192:199], v112\n"
    "s_nop 7\n"
    "s_and_b32 s55, s54, 0xffff\n"
    "s_cbranch_scc1 f2_26_%=\n"
    "s_nop 1\n"
    "v_rcp_f32 v212, v24\n"
    "s_nop 0\n"
    "v_mul_f32 v16, v16, v212\n"
    "v_mul_f32 v17, v17, v212\n"
    "v_mul_f32 v18, v18, v212\n"
    "v_mul_f32 v19, v19, v212\n"
    "v_mul_f32 v20, v20, v212\n"
    "v_mul_f32 v21, v21, v212\n"
    "v_mul_f32 v22, v22, v212\n"
    "v_mul_f32 v23, v23, v212\n"
    "v_cvt_pk_bf16_f32 v16, v16, v17\n"
    "v_min_u32 v16, -2, v16\n"
    "v_cvt_pk_bf16_f32 v17, v18, v19\n"
    "v_min_u32 v17, -2, v17\n"
    "v_cvt_pk_bf16_f32 v18, v20, v21\n"
    "v_min_u32 v18, -2, v18\n"
    "v_cvt_pk_bf16_f32 v19, v22, v23\n"
    "v_min_u32 v19, -2, v19\n"
    "s_lshl_b32 s54, s38, 4\n"
    "s_mov_b64 exec, 0xffff\n"
    "s_lshl_b64 exec, exec, s54\n"
    "s_and_b32 s56, s56, 0xfffffc00\n"
    "buffer_store_dwordx4 v[16:19], v2, s[12:15], s56 offen sc1\n"
    "s_mov_b64 exec, -1\n"
    "s_mov_b32 s55, s84\n"
    "s_add_u32 s84, s38, 0\n"
    "s_and_b32 s84, s84, 3\n"
    "s_lshl_b32 s84, s84, 12\n"
    "s_add_u32 s84, s84, s66\n"
    "buffer_load_dwordx4 v[184:187], v249, s[12:15], s84 offen offset:0 sc1\n"
    "s_add_u32 s84, s38, 0\n"
    "s_and_b32 s84, s84, 3\n"
    "s_lshl_b32 s84, s84, 12\n"
    "s_add_u32 s84, s84, s66\n"
    "buffer_load_dwordx4 v[188:191], v249, s[12:15], s84 offen offset:1024 sc1\n"
    "s_add_u32 s84, s38, 0\n"
    "s_and_b32 s84, s84, 3\n"
    "s_lshl_b32 s84, s84, 12\n"
    "s_add_u32 s84, s84, s66\n"
    "buffer_load_dwordx4 v[192:195], v249, s[12:15], s84 offen offset:2048 sc1\n"
    "s_add_u32 s84, s38, 0\n"
    "s_and_b32 s84, s84, 3\n"
    "s_lshl_b32 s84, s84, 12\n"
    "s_add_u32 s84, s84, s66\n"
    "buffer_load_dwordx4 v[196:199], v249, s[12:15], s84 offen offset:3072 sc1\n"
    "s_add_u32 s84, s38, 1\n"
    "s_and_b32 s84, s84, 3\n"
    "s_lshl_b32 s84, s84, 12\n"
    "s_add_u32 s84, s84, s66\n"
    "buffer_load_dwordx4 v[200:203], v249, s[12:15], s84 offen offset:0 sc1\n"
    "s_add_u32 s84, s38, 1\n"
    "s_and_b32 s84, s84, 3\n"
    "s_lshl_b32 s84, s84, 12\n"
    "s_add_u32 s84, s84, s66\n"
    "buffer_load_dwordx4 v[204:207], v249, s[12:15], s84 offen offset:1024 sc1\n"
    "s_add_u32 s84, s38, 1\n"
    "s_and_b32 s84, s84, 3\n"
    "s_lshl_b32 s84, s84, 12\n"
    "s_add_u32 s84, s84, s66\n"
    "buffer_load_dwordx4 v[208:211], v249, s[12:15], s84 offen offset:2048 sc1\n"
    "s_add_u32 s84, s38, 1\n"
    "s_and_b32 s84, s84, 3\n"
    "s_lshl_b32 s84, s84, 12\n"
    "s_add_u32 s84, s84, s66\n"
    "buffer_load_dwordx4 v[212:215], v249, s[12:15], s84 offen offset:3072 sc1\n"
    "s_mov_b32 s84, s55\n"
    "s_waitcnt vmcnt(0)\n"
    "s_branch fr_24_%=\n"
    "fb_27_%=:\n"
    "s_and_b32 s54, s89, 0xffff0000\n"
    "v_mov_b32 v212, 0xff800000\n"
    "f1_29_%=:\n"
    "buffer_load_dwordx4 v[200:203], v2, s[12:15], s54 offen sc1\n"
    "s_add_u32 s54, s54, 1024\n"
    "s_waitcnt vmcnt(0)\n"
    "v_mfma_f32_32x32x16_bf16 v[184:199], v[200:203], v[124:127], 0\n"
    "s_nop 11\n"
    "v_max3_f32 v212, v184, v185, v212\n"
    "v_max3_f32 v212, v186, v187, v212\n"
    "v_max3_f32 v212, v188, v189, v212\n"
    "v_max3_f32 v212, v190, v191, v212\n"
    "v_max3_f32 v212, v192, v193, v212\n"
    "v_max3_f32 v212, v194, v195, v212\n"
    "v_max3_f32 v212, v196, v197, v212\n"
    "v_max3_f32 v212, v198, v199, v212\n"
    "s_and_b32 s55, s54, 0xffff\n"
    "s_cbranch_scc1 f1_29_%=\n"
    "s_sub_u32 s54, s54, 0x10000\n"
    "v_mov_b32 v213, v212\n"
    "s_nop 1\n"
    "v_permlane32_swap_b32 v212, v213\n"
    "v_max_f32 v212, v212, v213\n"
    "v_and_b32 v204, 11, v1\n"
    "v_mov_b32 v205, s62\n"
    "v_cmp_eq_u32 vcc, 0, v204\n"
    "s_nop 1\n"
    "v_cndmask_b32 v204, 0, v205, vcc\n"
    "v_mov_b32 v205, v204\n"
    "v_mov_b32 v206, v204\n"
    "v_mov_b32 v207, v204\n"
    "v_mov_b32 v208, v204\n"
    "v_mov_b32 v209, v204\n"
    "v_mov_b32 v210, v204\n"
    "v_mov_b32 v211, v204\n"
    "v_mov_b32 v32, 0\n"
    "v_mov_b32 v33, 0\n"
    "v_mov_b32 v34, 0\n"
    "v_mov_b32 v35, 0\n"
    "v_mov_b32 v36, 0\n"
    "v_mov_b32 v37, 0\n"
    "v_mov_b32 v38, 0\n"
    "v_mov_b32 v39, 0\n"
    "v_mov_b32 v40, 0\n"
    "v_mov_b32 v41, 0\n"
    "v_mov_b32 v42, 0\n"
    "v_mov_b32 v43, 0\n"
    "v_mov_b32 v44, 0\n"
    "v_mov_b32 v45, 0\n"
    "v_mov_b32 v46, 0\n"
    "v_mov_b32 v47, 0\n"
    "f2_30_%=:\n"
    "buffer_load_dwordx4 v[200:203], v2, s[12:15], s54 offen sc1\n"
    "s_mov_b64 exec, s[46:47]\n"
    "buffer_load_dwordx4 v[204:207], v248, s[12:15], s54 offen sc1\n"
    "buffer_load_dwordx4 v[208:211], v248, s[12:15], s54 offen offset:256 sc1\n"
    "s_mov_b64 exec, -1\n"
    "s_add_u32 s54, s54, 1024\n"
    "s_waitcnt vmcnt(2)\n"
    "v_mfma_f32_32x32x16_bf16 v[184:199], v[200:203], v[124:127], 0\n"
    "s_nop 11\n"
    "v_sub_f32 v184, v184, v212\n"
    "v_sub_f32 v185, v185, v212\n"
    "v_sub_f32 v186, v186, v212\n"
    "v_sub_f32 v187, v187, v212\n"
    "v_sub_f32 v188, v188, v212\n"
    "v_sub_f32 v189, v189, v212\n"
    "v_sub_f32 v190, v190, v212\n"
    "v_sub_f32 v191, v191, v212\n"
    "v_sub_f32 v192, v192, v212\n"
    "v_sub_f32 v193, v193, v212\n"
    "v_sub_f32 v194, v194, v212\n"
    "v_sub_f32 v195, v195, v212\n"
    "v_sub_f32 v196, v196, v212\n"
    "v_sub_f32 v197, v197, v212\n"
    "v_sub_f32 v198, v198, v212\n"
    "v_sub_f32 v199, v199, v212\n"
    "v_exp_f32 v184, v184\n"
    "v_exp_f32 v185, v185\n"
    "v_exp_f32 v186, v186\n"
    "v_exp_f32 v187, v187\n"
    "v_exp_f32 v188, v188\n"
    "v_exp_f32 v189, v189\n"
    "v_exp_f32 v190, v190\n"
    "v_exp_f32 v191, v191\n"
    "v_exp_f32 v192, v192\n"
    "v_exp_f32 v193, v193\n"
    "v_exp_f32 v194, v194\n"
    "v_exp_f32 v195, v195\n"
    "v_exp_f32 v196, v196\n"
    "v_exp_f32 v197, v197\n"
    "v_exp_f32 v198, v198\n"
    "v_exp_f32 v199, v199\n"
    "s_waitcnt vmcnt(1)\n"
    "v_smfmac_f32_32x32x32_bf16 v[32:47], v[204:207], v[184:191], v112\n"
    "s_waitcnt vmcnt(0)\n"
    "v_smfmac_f32_32x32x32_bf16 v[32:47], v[208:211], v[192:199], v112\n"
    "s_nop 7\n"
    "s_and_b32 s55, s54, 0xffff\n"
    "s_cbranch_scc1 f2_30_%=\n"
    "s_nop 1\n"
    "v_rcp_f32 v212, v40\n"
    "s_nop 0\n"
    "v_mul_f32 v32, v32, v212\n"
    "v_mul_f32 v33, v33, v212\n"
    "v_mul_f32 v34, v34, v212\n"
    "v_mul_f32 v35, v35, v212\n"
    "v_mul_f32 v36, v36, v212\n"
    "v_mul_f32 v37, v37, v212\n"
    "v_mul_f32 v38, v38, v212\n"
    "v_mul_f32 v39, v39, v212\n"
    "v_cvt_pk_bf16_f32 v32, v32, v33\n"
    "v_min_u32 v32, -2, v32\n"
    "v_cvt_pk_bf16_f32 v33, v34, v35\n"
    "v_min_u32 v33, -2, v33\n"
    "v_cvt_pk_bf16_f32 v34, v36, v37\n"
    "v_min_u32 v34, -2, v34\n"
    "v_cvt_pk_bf16_f32 v35, v38, v39\n"
    "v_min_u32 v35, -2, v35\n"
    "s_lshl_b32 s54, s38, 4\n"
    "s_mov_b64 exec, 0xffff\n"
    "s_lshl_b64 exec, exec, s54\n"
    "s_and_b32 s56, s56, 0xfffffc00\n"
    "buffer_store_dwordx4 v[32:35], v2, s[12:15], s56 offen sc1\n"
    "s_mov_b64 exec, -1\n"
    "s_mov_b32 s55, s84\n"
    "s_add_u32 s84, s38, 0\n"
    "s_and_b32 s84, s84, 3\n"
    "s_lshl_b32 s84, s84, 12\n"
    "s_add_u32 s84, s84, s66\n"
    "buffer_load_dwordx4 v[184:187], v249, s[12:15], s84 offen offset:0 sc1\n"
    "s_add_u32 s84, s38, 0\n"
    "s_and_b32 s84, s84, 3\n"
    "s_lshl_b32 s84, s84, 12\n"
    "s_add_u32 s84, s84, s66\n"
    "buffer_load_dwordx4 v[188:191], v249, s[12:15], s84 offen offset:1024 sc1\n"
    "s_add_u32 s84, s38, 0\n"
    "s_and_b32 s84, s84, 3\n"
    "s_lshl_b32 s84, s84, 12\n"
    "s_add_u32 s84, s84, s66\n"
    "buffer_load_dwordx4 v[192:195], v249, s[12:15], s84 offen offset:2048 sc1\n"
    "s_add_u32 s84, s38, 0\n"
    "s_and_b32 s84, s84, 3\n"
    "s_lshl_b32 s84, s84, 12\n"
    "s_add_u32 s84, s84, s66\n"
    "buffer_load_dwordx4 v[196:199], v249, s[12:15], s84 offen offset:3072 sc1\n"
    "s_add_u32 s84, s38, 1\n"
    "s_and_b32 s84, s84, 3\n"
    "s_lshl_b32 s84, s84, 12\n"
    "s_add_u32 s84, s84, s66\n"
    "buffer_load_dwordx4 v[200:203], v249, s[12:15], s84 offen offset:0 sc1\n"
    "s_add_u32 s84, s38, 1\n"
    "s_and_b32 s84, s84, 3\n"
    "s_lshl_b32 s84, s84, 12\n"
    "s_add_u32 s84, s84, s66\n"
    "buffer_load_dwordx4 v[204:207], v249, s[12:15], s84 offen offset:1024 sc1\n"
    "s_add_u32 s84, s38, 1\n"
    "s_and_b32 s84, s84, 3\n"
    "s_lshl_b32 s84, s84, 12\n"
    "s_add_u32 s84, s84, s66\n"
    "buffer_load_dwordx4 v[208:211], v249, s[12:15], s84 offen offset:2048 sc1\n"
    "s_add_u32 s84, s38, 1\n"
    "s_and_b32 s84, s84, 3\n"
    "s_lshl_b32 s84, s84, 12\n"
    "s_add_u32 s84, s84, s66\n"
    "buffer_load_dwordx4 v[212:215], v249, s[12:15], s84 offen offset:3072 sc1\n"
    "s_mov_b32 s84, s55\n"
    "s_waitcnt vmcnt(0)\n"
    "s_branch fr_28_%=\n"
    "fb_31_%=:\n"
    "s_and_b32 s54, s89, 0xffff0000\n"
    "v_mov_b32 v212, 0xff800000\n"
    "f1_33_%=:\n"
    "buffer_load_dwordx4 v[200:203], v2, s[12:15], s54 offen sc1\n"
    "s_add_u32 s54, s54, 1024\n"
    "s_waitcnt vmcnt(0)\n"
    "v_mfma_f32_32x32x16_bf16 v[184:199], v[200:203], v[128:131], 0\n"
    "s_nop 11\n"
    "v_max3_f32 v212, v184, v185, v212\n"
    "v_max3_f32 v212, v186, v187, v212\n"
    "v_max3_f32 v212, v188, v189, v212\n"
    "v_max3_f32 v212, v190, v191, v212\n"
    "v_max3_f32 v212, v192, v193, v212\n"
    "v_max3_f32 v212, v194, v195, v212\n"
    "v_max3_f32 v212, v196, v197, v212\n"
    "v_max3_f32 v212, v198, v199, v212\n"
    "s_and_b32 s55, s54, 0xffff\n"
    "s_cbranch_scc1 f1_33_%=\n"
    "s_sub_u32 s54, s54, 0x10000\n"
    "v_mov_b32 v213, v212\n"
    "s_nop 1\n"
    "v_permlane32_swap_b32 v212, v213\n"
    "v_max_f32 v212, v212, v213\n"
    "v_and_b32 v204, 11, v1\n"
    "v_mov_b32 v205, s62\n"
    "v_cmp_eq_u32 vcc, 0, v204\n"
    "s_nop 1\n"
    "v_cndmask_b32 v204, 0, v205, vcc\n"
    "v_mov_b32 v205, v204\n"
    "v_mov_b32 v206, v204\n"
    "v_mov_b32 v207, v204\n"
    "v_mov_b32 v208, v204\n"
    "v_mov_b32 v209, v204\n"
    "v_mov_b32 v210, v204\n"
    "v_mov_b32 v211, v204\n"
    "v_mov_b32 v48, 0\n"
    "v_mov_b32 v49, 0\n"
    "v_mov_b32 v50, 0\n"
    "v_mov_b32 v51, 0\n"
    "v_mov_b32 v52, 0\n"
    "v_mov_b32 v53, 0\n"
    "v_mov_b32 v54, 0\n"
    "v_mov_b32 v55, 0\n"
    "v_mov_b32 v56, 0\n"
    "v_mov_b32 v57, 0\n"
    "v_mov_b32 v58, 0\n"
    "v_mov_b32 v59, 0\n"
    "v_mov_b32 v60, 0\n"
    "v_mov_b32 v61, 0\n"
    "v_mov_b32 v62, 0\n"
    "v_mov_b32 v63, 0\n"
    "f2_34_%=:\n"
    "buffer_load_dwordx4 v[200:203], v2, s[12:15], s54 offen sc1\n"
    "s_mov_b64 exec, s[46:47]\n"
    "buffer_load_dwordx4 v[204:207], v248, s[12:15], s54 offen sc1\n"
    "buffer_load_dwordx4 v[208:211], v248, s[12:15], s54 offen offset:256 sc1\n"
    "s_mov_b64 exec, -1\n"
    "s_add_u32 s54, s54, 1024\n"
    "s_waitcnt vmcnt(2)\n"
    "v_mfma_f32_32x32x16_bf16 v[184:199], v[200:203], v[128:131], 0\n"
    "s_nop 11\n"
    "v_sub_f32 v184, v184, v212\n"
    "v_sub_f32 v185, v185, v212\n"
    "v_sub_f32 v186, v186, v212\n"
    "v_sub_f32 v187, v187, v212\n"
    "v_sub_f32 v188, v188, v212\n"
    "v_sub_f32 v189, v189, v212\n"
    "v_sub_f32 v190, v190, v212\n"
    "v_sub_f32 v191, v191, v212\n"
    "v_sub_f32 v192, v192, v212\n"
    "v_sub_f32 v193, v193, v212\n"
    "v_sub_f32 v194, v194, v212\n"
    "v_sub_f32 v195, v195, v212\n"
    "v_sub_f32 v196, v196, v212\n"
    "v_sub_f32 v197, v197, v212\n"
    "v_sub_f32 v198, v198, v212\n"
    "v_sub_f32 v199, v199, v212\n"
    "v_exp_f32 v184, v184\n"
    "v_exp_f32 v185, v185\n"
    "v_exp_f32 v186, v186\n"
    "v_exp_f32 v187, v187\n"
    "v_exp_f32 v188, v188\n"
    "v_exp_f32 v189, v189\n"
    "v_exp_f32 v190, v190\n"
    "v_exp_f32 v191, v191\n"
    "v_exp_f32 v192, v192\n"
    "v_exp_f32 v193, v193\n"
    "v_exp_f32 v194, v194\n"
    "v_exp_f32 v195, v195\n"
    "v_exp_f32 v196, v196\n"
    "v_exp_f32 v197, v197\n"
    "v_exp_f32 v198, v198\n"
    "v_exp_f32 v199, v199\n"
    "s_waitcnt vmcnt(1)\n"
    "v_smfmac_f32_32x32x32_bf16 v[48:63], v[204:207], v[184:191], v112\n"
    "s_waitcnt vmcnt(0)\n"
    "v_smfmac_f32_32x32x32_bf16 v[48:63], v[208:211], v[192:199], v112\n"
    "s_nop 7\n"
    "s_and_b32 s55, s54, 0xffff\n"
    "s_cbranch_scc1 f2_34_%=\n"
    "s_nop 1\n"
    "v_rcp_f32 v212, v56\n"
    "s_nop 0\n"
    "v_mul_f32 v48, v48, v212\n"
    "v_mul_f32 v49, v49, v212\n"
    "v_mul_f32 v50, v50, v212\n"
    "v_mul_f32 v51, v51, v212\n"
    "v_mul_f32 v52, v52, v212\n"
    "v_mul_f32 v53, v53, v212\n"
    "v_mul_f32 v54, v54, v212\n"
    "v_mul_f32 v55, v55, v212\n"
    "v_cvt_pk_bf16_f32 v48, v48, v49\n"
    "v_min_u32 v48, -2, v48\n"
    "v_cvt_pk_bf16_f32 v49, v50, v51\n"
    "v_min_u32 v49, -2, v49\n"
    "v_cvt_pk_bf16_f32 v50, v52, v53\n"
    "v_min_u32 v50, -2, v50\n"
    "v_cvt_pk_bf16_f32 v51, v54, v55\n"
    "v_min_u32 v51, -2, v51\n"
    "s_lshl_b32 s54, s38, 4\n"
    "s_mov_b64 exec, 0xffff\n"
    "s_lshl_b64 exec, exec, s54\n"
    "s_and_b32 s56, s56, 0xfffffc00\n"
    "buffer_store_dwordx4 v[48:51], v2, s[12:15], s56 offen sc1\n"
    "s_mov_b64 exec, -1\n"
    "s_mov_b32 s55, s84\n"
    "s_add_u32 s84, s38, 0\n"
    "s_and_b32 s84, s84, 3\n"
    "s_lshl_b32 s84, s84, 12\n"
    "s_add_u32 s84, s84, s66\n"
    "buffer_load_dwordx4 v[184:187], v249, s[12:15], s84 offen offset:0 sc1\n"
    "s_add_u32 s84, s38, 0\n"
    "s_and_b32 s84, s84, 3\n"
    "s_lshl_b32 s84, s84, 12\n"
    "s_add_u32 s84, s84, s66\n"
    "buffer_load_dwordx4 v[188:191], v249, s[12:15], s84 offen offset:1024 sc1\n"
    "s_add_u32 s84, s38, 0\n"
    "s_and_b32 s84, s84, 3\n"
    "s_lshl_b32 s84, s84, 12\n"
    "s_add_u32 s84, s84, s66\n"
    "buffer_load_dwordx4 v[192:195], v249, s[12:15], s84 offen offset:2048 sc1\n"
    "s_add_u32 s84, s38, 0\n"
    "s_and_b32 s84, s84, 3\n"
    "s_lshl_b32 s84, s84, 12\n"
    "s_add_u32 s84, s84, s66\n"
    "buffer_load_dwordx4 v[196:199], v249, s[12:15], s84 offen offset:3072 sc1\n"
    "s_add_u32 s84, s38, 1\n"
    "s_and_b32 s84, s84, 3\n"
    "s_lshl_b32 s84, s84, 12\n"
    "s_add_u32 s84, s84, s66\n"
    "buffer_load_dwordx4 v[200:203], v249, s[12:15], s84 offen offset:0 sc1\n"
    "s_add_u32 s84, s38, 1\n"
    "s_and_b32 s84, s84, 3\n"
    "s_lshl_b32 s84, s84, 12\n"
    "s_add_u32 s84, s84, s66\n"
    "buffer_load_dwordx4 v[204:207], v249, s[12:15], s84 offen offset:1024 sc1\n"
    "s_add_u32 s84, s38, 1\n"
    "s_and_b32 s84, s84, 3\n"
    "s_lshl_b32 s84, s84, 12\n"
    "s_add_u32 s84, s84, s66\n"
    "buffer_load_dwordx4 v[208:211], v249, s[12:15], s84 offen offset:2048 sc1\n"
    "s_add_u32 s84, s38, 1\n"
    "s_and_b32 s84, s84, 3\n"
    "s_lshl_b32 s84, s84, 12\n"
    "s_add_u32 s84, s84, s66\n"
    "buffer_load_dwordx4 v[212:215], v249, s[12:15], s84 offen offset:3072 sc1\n"
    "s_mov_b32 s84, s55\n"
    "s_waitcnt vmcnt(0)\n"
    "s_branch fr_32_%=\n"
    "op_35_%=:\n"
    "s_add_u32 s85, s73, 0\n"
    "buffer_load_dwordx4 v[32:35], v2, s[12:15], s85 offen offset:0 sc1\n"
    "buffer_load_dwordx4 v[36:39], v2, s[12:15], s85 offen offset:1024 sc1\n"
    "buffer_load_dwordx4 v[40:43], v2, s[12:15], s85 offen offset:2048 sc1\n"
    "buffer_load_dwordx4 v[44:47], v2, s[12:15], s85 offen offset:3072 sc1\n"
    "s_waitcnt vmcnt(3)\n"
    "v_max3_u32 v16, v32, v33, v34\n"
    "s_waitcnt vmcnt(2)\n"
    "v_max3_u32 v16, v35, v36, v16\n"
    "v_max3_u32 v16, v37, v38, v16\n"
    "s_waitcnt vmcnt(1)\n"
    "v_max3_u32 v16, v39, v40, v16\n"
    "v_max3_u32 v16, v41, v42, v16\n"
    "s_waitcnt vmcnt(0)\n"
    "v_max3_u32 v16, v43, v44, v16\n"
    "v_max3_u32 v16, v45, v46, v16\n"
    "v_max_u32 v16, v47, v16\n"
    "v_cmp_eq_u32 vcc, -1, v16\n"
    "s_cbranch_vccz og_37_%=\n"
    "s_sleep 2\n"
    "s_sub_i32 s0, s0, 1\n"
    "s_cmp_lt_i32 s0, 0\n"
    "s_cbranch_scc0 op_35_%=\n"
    "s_branch og_37_%=\n"
    "og_37_%=:\n"
    "v_mfma_f32_32x32x16_bf16 v[16:31], v[232:235], v[32:35], 0\n"
    "v_mfma_f32_32x32x16_bf16 v[16:31], v[236:239], v[36:39], v[16:31]\n"
    "v_mfma_f32_32x32x16_bf16 v[16:31], v[240:243], v[40:43], v[16:31]\n"
    "v_mfma_f32_32x32x16_bf16 v[16:31], v[244:247], v[44:47], v[16:31]\n"
    "s_nop 7\n"
    "s_nop 7\n"
    "s_branch ob_36_%=\n"
    "op_38_%=:\n"
    "s_add_u32 s85, s73, 32768\n"
    "buffer_load_dwordx4 v[32:35], v2, s[12:15], s85 offen offset:0 sc1\n"
    "buffer_load_dwordx4 v[36:39], v2, s[12:15], s85 offen offset:1024 sc1\n"
    "buffer_load_dwordx4 v[40:43], v2, s[12:15], s85 offen offset:2048 sc1\n"
    "buffer_load_dwordx4 v[44:47], v2, s[12:15], s85 offen offset:3072 sc1\n"
    "s_waitcnt vmcnt(3)\n"
    "v_max3_u32 v16, v32, v33, v34\n"
    "s_waitcnt vmcnt(2)\n"
    "v_max3_u32 v16, v35, v36, v16\n"
    "v_max3_u32 v16, v37, v38, v16\n"
    "s_waitcnt vmcnt(1)\n"
    "v_max3_u32 v16, v39, v40, v16\n"
    "v_max3_u32 v16, v41, v42, v16\n"
    "s_waitcnt vmcnt(0)\n"
    "v_max3_u32 v16, v43, v44, v16\n"
    "v_max3_u32 v16, v45, v46, v16\n"
    "v_max_u32 v16, v47, v16\n"
    "v_cmp_eq_u32 vcc, -1, v16\n"
    "s_cbranch_vccz og_40_%=\n"
    "s_sleep 2\n"
    "s_sub_i32 s0, s0, 1\n"
    "s_cmp_lt_i32 s0, 0\n"
    "s_cbranch_scc0 op_38_%=\n"
    "s_branch og_40_%=\n"
    "og_40_%=:\n"
    "v_mfma_f32_32x32x16_bf16 v[16:31], v[232:235], v[32:35], 0\n"
    "v_mfma_f32_32x32x16_bf16 v[16:31], v[236:239], v[36:39], v[16:31]\n"
    "v_mfma_f32_32x32x16_bf16 v[16:31], v[240:243], v[40:43], v[16:31]\n"
    "v_mfma_f32_32x32x16_bf16 v[16:31], v[244:247], v[44:47], v[16:31]\n"
    "s_nop 7\n"
    "s_nop 7\n"
    "s_branch ob_39_%=\n"
    "fb_41_%=:\n"
    "s_and_b32 s54, s89, 0xffff0000\n"
    "v_mov_b32 v212, 0xff800000\n"
    "f1_43_%=:\n"
    "buffer_load_dwordx4 v[200:203], v2, s[12:15], s54 offen sc1\n"
    "s_add_u32 s54, s54, 1024\n"
    "s_waitcnt vmcnt(0)\n"
    "v_mfma_f32_32x32x16_bf16 v[184:199], v[200:203], v[132:135], 0\n"
    "s_nop 11\n"
    "v_max3_f32 v212, v184, v185, v212\n"
    "v_max3_f32 v212, v186, v187, v212\n"
    "v_max3_f32 v212, v188, v189, v212\n"
    "v_max3_f32 v212, v190, v191, v212\n"
    "v_max3_f32 v212, v192, v193, v212\n"
    "v_max3_f32 v212, v194, v195, v212\n"
    "v_max3_f32 v212, v196, v197, v212\n"
    "v_max3_f32 v212, v198, v199, v212\n"
    "s_and_b32 s55, s54, 0xffff\n"
    "s_cbranch_scc1 f1_43_%=\n"
    "s_sub_u32 s54, s54, 0x10000\n"
    "v_mov_b32 v213, v212\n"
    "s_nop 1\n"
    "v_permlane32_swap_b32 v212, v213\n"
    "v_max_f32 v212, v212, v213\n"
    "v_and_b32 v204, 11, v1\n"
    "v_mov_b32 v205, s62\n"
    "v_cmp_eq_u32 vcc, 0, v204\n"
    "s_nop 1\n"
    "v_cndmask_b32 v204, 0, v205, vcc\n"
    "v_mov_b32 v205, v204\n"
    "v_mov_b32 v206, v204\n"
    "v_mov_b32 v207, v204\n"
    "v_mov_b32 v208, v204\n"
    "v_mov_b32 v209, v204\n"
    "v_mov_b32 v210, v204\n"
    "v_mov_b32 v211, v204\n"
    "v_mov_b32 v64, 0\n"
    "v_mov_b32 v65, 0\n"
    "v_mov_b32 v66, 0\n"
    "v_mov_b32 v67, 0\n"
    "v_mov_b32 v68, 0\n"
    "v_mov_b32 v69, 0\n"
    "v_mov_b32 v70, 0\n"
    "v_mov_b32 v71, 0\n"
    "v_mov_b32 v72, 0\n"
    "v_mov_b32 v73, 0\n"
    "v_mov_b32 v74, 0\n"
    "v_mov_b32 v75, 0\n"
    "v_mov_b32 v76, 0\n"
    "v_mov_b32 v77, 0\n"
    "v_mov_b32 v78, 0\n"
    "v_mov_b32 v79, 0\n"
    "f2_44_%=:\n"
    "buffer_load_dwordx4 v[200:203], v2, s[12:15], s54 offen sc1\n"
    "s_mov_b64 exec, s[46:47]\n"
    "buffer_load_dwordx4 v[204:207], v248, s[12:15], s54 offen sc1\n"
    "buffer_load_dwordx4 v[208:211], v248, s[12:15], s54 offen offset:256 sc1\n"
    "s_mov_b64 exec, -1\n"
    "s_add_u32 s54, s54, 1024\n"
    "s_waitcnt vmcnt(2)\n"
    "v_mfma_f32_32x32x16_bf16 v[184:199], v[200:203], v[132:135], 0\n"
    "s_nop 11\n"
    "v_sub_f32 v184, v184, v212\n"
    "v_sub_f32 v185, v185, v212\n"
    "v_sub_f32 v186, v186, v212\n"
    "v_sub_f32 v187, v187, v212\n"
    "v_sub_f32 v188, v188, v212\n"
    "v_sub_f32 v189, v189, v212\n"
    "v_sub_f32 v190, v190, v212\n"
    "v_sub_f32 v191, v191, v212\n"
    "v_sub_f32 v192, v192, v212\n"
    "v_sub_f32 v193, v193, v212\n"
    "v_sub_f32 v194, v194, v212\n"
    "v_sub_f32 v195, v195, v212\n"
    "v_sub_f32 v196, v196, v212\n"
    "v_sub_f32 v197, v197, v212\n"
    "v_sub_f32 v198, v198, v212\n"
    "v_sub_f32 v199, v199, v212\n"
    "v_exp_f32 v184, v184\n"
    "v_exp_f32 v185, v185\n"
    "v_exp_f32 v186, v186\n"
    "v_exp_f32 v187, v187\n"
    "v_exp_f32 v188, v188\n"
    "v_exp_f32 v189, v189\n"
    "v_exp_f32 v190, v190\n"
    "v_exp_f32 v191, v191\n"
    "v_exp_f32 v192, v192\n"
    "v_exp_f32 v193, v193\n"
    "v_exp_f32 v194, v194\n"
    "v_exp_f32 v195, v195\n"
    "v_exp_f32 v196, v196\n"
    "v_exp_f32 v197, v197\n"
    "v_exp_f32 v198, v198\n"
    "v_exp_f32 v199, v199\n"
    "s_waitcnt vmcnt(1)\n"
    "v_smfmac_f32_32x32x32_bf16 v[64:79], v[204:207], v[184:191], v112\n"
    "s_waitcnt vmcnt(0)\n"
    "v_smfmac_f32_32x32x32_bf16 v[64:79], v[208:211], v[192:199], v112\n"
    "s_nop 7\n"
    "s_and_b32 s55, s54, 0xffff\n"
    "s_cbranch_scc1 f2_44_%=\n"
    "s_nop 1\n"
    "v_rcp_f32 v212, v72\n"
    "s_nop 0\n"
    "v_mul_f32 v64, v64, v212\n"
    "v_mul_f32 v65, v65, v212\n"
    "v_mul_f32 v66, v66, v212\n"
    "v_mul_f32 v67, v67, v212\n"
    "v_mul_f32 v68, v68, v212\n"
    "v_mul_f32 v69, v69, v212\n"
    "v_mul_f32 v70, v70, v212\n"
    "v_mul_f32 v71, v71, v212\n"
    "v_cvt_pk_bf16_f32 v64, v64, v65\n"
    "v_min_u32 v64, -2, v64\n"
    "v_cvt_pk_bf16_f32 v65, v66, v67\n"
    "v_min_u32 v65, -2, v65\n"
    "v_cvt_pk_bf16_f32 v66, v68, v69\n"
    "v_min_u32 v66, -2, v66\n"
    "v_cvt_pk_bf16_f32 v67, v70, v71\n"
    "v_min_u32 v67, -2, v67\n"
    "s_lshl_b32 s54, s38, 4\n"
    "s_mov_b64 exec, 0xffff\n"
    "s_lshl_b64 exec, exec, s54\n"
    "s_and_b32 s56, s56, 0xfffffc00\n"
    "buffer_store_dwordx4 v[64:67], v2, s[12:15], s56 offen sc1\n"
    "s_mov_b64 exec, -1\n"
    "s_mov_b32 s55, s84\n"
    "s_add_u32 s84, s38, 0\n"
    "s_and_b32 s84, s84, 3\n"
    "s_lshl_b32 s84, s84, 12\n"
    "s_add_u32 s84, s84, s66\n"
    "buffer_load_dwordx4 v[184:187], v249, s[12:15], s84 offen offset:0 sc1\n"
    "s_add_u32 s84, s38, 0\n"
    "s_and_b32 s84, s84, 3\n"
    "s_lshl_b32 s84, s84, 12\n"
    "s_add_u32 s84, s84, s66\n"
    "buffer_load_dwordx4 v[188:191], v249, s[12:15], s84 offen offset:1024 sc1\n"
    "s_add_u32 s84, s38, 0\n"
    "s_and_b32 s84, s84, 3\n"
    "s_lshl_b32 s84, s84, 12\n"
    "s_add_u32 s84, s84, s66\n"
    "buffer_load_dwordx4 v[192:195], v249, s[12:15], s84 offen offset:2048 sc1\n"
    "s_add_u32 s84, s38, 0\n"
    "s_and_b32 s84, s84, 3\n"
    "s_lshl_b32 s84, s84, 12\n"
    "s_add_u32 s84, s84, s66\n"
    "buffer_load_dwordx4 v[196:199], v249, s[12:15], s84 offen offset:3072 sc1\n"
    "s_add_u32 s84, s38, 1\n"
    "s_and_b32 s84, s84, 3\n"
    "s_lshl_b32 s84, s84, 12\n"
    "s_add_u32 s84, s84, s66\n"
    "buffer_load_dwordx4 v[200:203], v249, s[12:15], s84 offen offset:0 sc1\n"
    "s_add_u32 s84, s38, 1\n"
    "s_and_b32 s84, s84, 3\n"
    "s_lshl_b32 s84, s84, 12\n"
    "s_add_u32 s84, s84, s66\n"
    "buffer_load_dwordx4 v[204:207], v249, s[12:15], s84 offen offset:1024 sc1\n"
    "s_add_u32 s84, s38, 1\n"
    "s_and_b32 s84, s84, 3\n"
    "s_lshl_b32 s84, s84, 12\n"
    "s_add_u32 s84, s84, s66\n"
    "buffer_load_dwordx4 v[208:211], v249, s[12:15], s84 offen offset:2048 sc1\n"
    "s_add_u32 s84, s38, 1\n"
    "s_and_b32 s84, s84, 3\n"
    "s_lshl_b32 s84, s84, 12\n"
    "s_add_u32 s84, s84, s66\n"
    "buffer_load_dwordx4 v[212:215], v249, s[12:15], s84 offen offset:3072 sc1\n"
    "s_mov_b32 s84, s55\n"
    "s_waitcnt vmcnt(0)\n"
    "s_branch fr_42_%=\n"
    "hp_45_%=:\n"
    "s_add_u32 m0, s78, 0\n"
    "s_add_u32 s87, s74, 0\n"
    "buffer_load_dwordx4 v2, s[12:15], s87 offen offset:0 sc1 lds\n"
    "buffer_load_dwordx4 v2, s[12:15], s87 offen offset:1024 sc1 lds\n"
    "buffer_load_dwordx4 v2, s[12:15], s87 offen offset:2048 sc1 lds\n"
    "buffer_load_dwordx4 v2, s[12:15], s87 offen offset:3072 sc1 lds\n"
    "s_waitcnt vmcnt(0)\n"
    "ds_read_b128 v[136:139], v3 offset:0\n"
    "ds_read_b128 v[140:143], v3 offset:1024\n"
    "ds_read_b128 v[144:147], v3 offset:2048\n"
    "ds_read_b128 v[148:151], v3 offset:3072\n"
    "s_waitcnt lgkmcnt(3)\n"
    "v_max3_u32 v176, v136, v137, v138\n"
    "s_waitcnt lgkmcnt(2)\n"
    "v_max3_u32 v176, v139, v140, v176\n"
    "v_max3_u32 v176, v141, v142, v176\n"
    "s_waitcnt lgkmcnt(1)\n"
    "v_max3_u32 v176, v143, v144, v176\n"
    "v_max3_u32 v176, v145, v146, v176\n"
    "s_waitcnt lgkmcnt(0)\n"
    "v_max3_u32 v176, v147, v148, v176\n"
    "v_max3_u32 v176, v149, v150, v176\n"
    "v_max_u32 v176, v151, v176\n"
    "v_cmp_eq_u32 vcc, -1, v176\n"
    "s_cbranch_vccz hg_47_%=\n"
    "s_sleep 2\n"
    "s_sub_i32 s0, s0, 1\n"
    "s_cmp_lt_i32 s0, 0\n"
    "s_cbranch_scc0 hp_45_%=\n"
    "s_branch hg_47_%=\n"
    "hg_47_%=:\n"
    "v_mfma_f32_4x4x4_16b_bf16 v[176:179], v[136:137], v[136:137], 0\n"
    "v_mfma_f32_16x16x32_bf16 v[180:183], v[124:127], v[136:139], 0\n"
    "s_nop 0\n"
    "v_mfma_f32_4x4x4_16b_bf16 v[176:179], v[138:139], v[138:139], v[176:179]\n"
    "s_nop 1\n"
    "v_mfma_f32_4x4x4_16b_bf16 v[176:179], v[140:141], v[140:141], v[176:179]\n"
    "v_mfma_f32_16x16x32_bf16 v[180:183], v[124:127], v[140:143], v[180:183]\n"
    "s_nop 0\n"
    "v_mfma_f32_4x4x4_16b_bf16 v[176:179], v[142:143], v[142:143], v[176:179]\n"
    "s_nop 1\n"
    "v_mfma_f32_4x4x4_16b_bf16 v[176:179], v[144:145], v[144:145], v[176:179]\n"
    "v_mfma_f32_16x16x32_bf16 v[180:183], v[124:127], v[144:147], v[180:183]\n"
    "s_nop 0\n"
    "v_mfma_f32_4x4x4_16b_bf16 v[176:179], v[146:147], v[146:147], v[176:179]\n"
    "s_nop 1\n"
    "v_mfma_f32_4x4x4_16b_bf16 v[176:179], v[148:149], v[148:149], v[176:179]\n"
    "v_mfma_f32_16x16x32_bf16 v[180:183], v[124:127], v[148:151], v[180:183]\n"
    "s_nop 0\n"
    "v_mfma_f32_4x4x4_16b_bf16 v[176:179], v[150:151], v[150:151], v[176:179]\n"
    "s_nop 7\n"
    "s_nop 7\n"
    "s_branch hb_46_%=\n"
    "op_48_%=:\n"
    "s_add_u32 s85, s73, 65536\n"
    "buffer_load_dwordx4 v[32:35], v2, s[12:15], s85 offen offset:0 sc1\n"
    "buffer_load_dwordx4 v[36:39], v2, s[12:15], s85 offen offset:1024 sc1\n"
    "buffer_load_dwordx4 v[40:43], v2, s[12:15], s85 offen offset:2048 sc1\n"
    "buffer_load_dwordx4 v[44:47], v2, s[12:15], s85 offen offset:3072 sc1\n"
    "s_waitcnt vmcnt(3)\n"
    "v_max3_u32 v16, v32, v33, v34\n"
    "s_waitcnt vmcnt(2)\n"
    "v_max3_u32 v16, v35, v36, v16\n"
    "v_max3_u32 v16, v37, v38, v16\n"
    "s_waitcnt vmcnt(1)\n"
    "v_max3_u32 v16, v39, v40, v16\n"
    "v_max3_u32 v16, v41, v42, v16\n"
    "s_waitcnt vmcnt(0)\n"
    "v_max3_u32 v16, v43, v44, v16\n"
    "v_max3_u32 v16, v45, v46, v16\n"
    "v_max_u32 v16, v47, v16\n"
    "v_cmp_eq_u32 vcc, -1, v16\n"
    "s_cbranch_vccz og_50_%=\n"
    "s_sleep 2\n"
    "s_sub_i32 s0, s0, 1\n"
    "s_cmp_lt_i32 s0, 0\n"
    "s_cbranch_scc0 op_48_%=\n"
    "s_branch og_50_%=\n"
    "og_50_%=:\n"
    "v_mfma_f32_32x32x16_bf16 v[16:31], v[232:235], v[32:35], 0\n"
    "v_mfma_f32_32x32x16_bf16 v[16:31], v[236:239], v[36:39], v[16:31]\n"
    "v_mfma_f32_32x32x16_bf16 v[16:31], v[240:243], v[40:43], v[16:31]\n"
    "v_mfma_f32_32x32x16_bf16 v[16:31], v[244:247], v[44:47], v[16:31]\n"
    "s_nop 7\n"
    "s_nop 7\n"
    "s_branch ob_49_%=\n"
    "op_53_%=:\n"
    "s_add_u32 s85, s73, 98304\n"
    "buffer_load_dwordx4 v[32:35], v2, s[12:15], s85 offen offset:0 sc1\n"
    "buffer_load_dwordx4 v[36:39], v2, s[12:15], s85 offen offset:1024 sc1\n"
    "buffer_load_dwordx4 v[40:43], v2, s[12:15], s85 offen offset:2048 sc1\n"
    "buffer_load_dwordx4 v[44:47], v2, s[12:15], s85 offen offset:3072 sc1\n"
    "s_waitcnt vmcnt(3)\n"
    "v_max3_u32 v16, v32, v33, v34\n"
    "s_waitcnt vmcnt(2)\n"
    "v_max3_u32 v16, v35, v36, v16\n"
    "v_max3_u32 v16, v37, v38, v16\n"
    "s_waitcnt vmcnt(1)\n"
    "v_max3_u32 v16, v39, v40, v16\n"
    "v_max3_u32 v16, v41, v42, v16\n"
    "s_waitcnt vmcnt(0)\n"
    "v_max3_u32 v16, v43, v44, v16\n"
    "v_max3_u32 v16, v45, v46, v16\n"
    "v_max_u32 v16, v47, v16\n"
    "v_cmp_eq_u32 vcc, -1, v16\n"
    "s_cbranch_vccz og_55_%=\n"
    "s_sleep 2\n"
    "s_sub_i32 s0, s0, 1\n"
    "s_cmp_lt_i32 s0, 0\n"
    "s_cbranch_scc0 op_53_%=\n"
    "s_branch og_55_%=\n"
    "og_55_%=:\n"
    "v_mfma_f32_32x32x16_bf16 v[16:31], v[232:235], v[32:35], 0\n"
    "v_mfma_f32_32x32x16_bf16 v[16:31], v[236:239], v[36:39], v[16:31]\n"
    "v_mfma_f32_32x32x16_bf16 v[16:31], v[240:243], v[40:43], v[16:31]\n"
    "v_mfma_f32_32x32x16_bf16 v[16:31], v[244:247], v[44:47], v[16:31]\n"
    "s_nop 7\n"
    "s_nop 7\n"
    "s_branch ob_54_%=\n"
    "hp_56_%=:\n"
    "s_add_u32 m0, s78, 32768\n"
    "s_add_u32 s87, s74, 32768\n"
    "buffer_load_dwordx4 v2, s[12:15], s87 offen offset:0 sc1 lds\n"
    "buffer_load_dwordx4 v2, s[12:15], s87 offen offset:1024 sc1 lds\n"
    "buffer_load_dwordx4 v2, s[12:15], s87 offen offset:2048 sc1 lds\n"
    "buffer_load_dwordx4 v2, s[12:15], s87 offen offset:3072 sc1 lds\n"
    "s_waitcnt vmcnt(0)\n"
    "ds_read_b128 v[136:139], v3 offset:32768\n"
    "ds_read_b128 v[140:143], v3 offset:33792\n"
    "ds_read_b128 v[144:147], v3 offset:34816\n"
    "ds_read_b128 v[148:151], v3 offset:35840\n"
    "s_waitcnt lgkmcnt(3)\n"
    "v_max3_u32 v176, v136, v137, v138\n"
    "s_waitcnt lgkmcnt(2)\n"
    "v_max3_u32 v176, v139, v140, v176\n"
    "v_max3_u32 v176, v141, v142, v176\n"
    "s_waitcnt lgkmcnt(1)\n"
    "v_max3_u32 v176, v143, v144, v176\n"
    "v_max3_u32 v176, v145, v146, v176\n"
    "s_waitcnt lgkmcnt(0)\n"
    "v_max3_u32 v176, v147, v148, v176\n"
    "v_max3_u32 v176, v149, v150, v176\n"
    "v_max_u32 v176, v151, v176\n"
    "v_cmp_eq_u32 vcc, -1, v176\n"
    "s_cbranch_vccz hg_58_%=\n"
    "s_sleep 2\n"
    "s_sub_i32 s0, s0, 1\n"
    "s_cmp_lt_i32 s0, 0\n"
    "s_cbranch_scc0 hp_56_%=\n"
    "s_branch hg_58_%=\n"
    "hg_58_%=:\n"
    "v_mfma_f32_4x4x4_16b_bf16 v[176:179], v[136:137], v[136:137], 0\n"
    "v_mfma_f32_16x16x32_bf16 v[180:183], v[124:127], v[136:139], 0\n"
    "s_nop 0\n"
    "v_mfma_f32_4x4x4_16b_bf16 v[176:179], v[138:139], v[138:139], v[176:179]\n"
    "s_nop 1\n"
    "v_mfma_f32_4x4x4_16b_bf16 v[176:179], v[140:141], v[140:141], v[176:179]\n"
    "v_mfma_f32_16x16x32_bf16 v[180:183], v[124:127], v[140:143], v[180:183]\n"
    "s_nop 0\n"
    "v_mfma_f32_4x4x4_16b_bf16 v[176:179], v[142:143], v[142:143], v[176:179]\n"
    "s_nop 1\n"
    "v_mfma_f32_4x4x4_16b_bf16 v[176:179], v[144:145], v[144:145], v[176:179]\n"
    "v_mfma_f32_16x16x32_bf16 v[180:183], v[124:127], v[144:147], v[180:183]\n"
    "s_nop 0\n"
    "v_mfma_f32_4x4x4_16b_bf16 v[176:179], v[146:147], v[146:147], v[176:179]\n"
    "s_nop 1\n"
    "v_mfma_f32_4x4x4_16b_bf16 v[176:179], v[148:149], v[148:149], v[176:179]\n"
    "v_mfma_f32_16x16x32_bf16 v[180:183], v[124:127], v[148:151], v[180:183]\n"
    "s_nop 0\n"
    "v_mfma_f32_4x4x4_16b_bf16 v[176:179], v[150:151], v[150:151], v[176:179]\n"
    "v_mfma_f32_32x32x16_bf16 v[152:167], v[184:187], v[136:139], 0\n"
    "s_nop 7\n"
    "s_nop 7\n"
    "s_branch hb_57_%=\n"
    "hp_59_%=:\n"
    "s_add_u32 m0, s78, 0\n"
    "s_add_u32 s87, s74, 65536\n"
    "buffer_load_dwordx4 v2, s[12:15], s87 offen offset:0 sc1 lds\n"
    "buffer_load_dwordx4 v2, s[12:15], s87 offen offset:1024 sc1 lds\n"
    "buffer_load_dwordx4 v2, s[12:15], s87 offen offset:2048 sc1 lds\n"
    "buffer_load_dwordx4 v2, s[12:15], s87 offen offset:3072 sc1 lds\n"
    "s_waitcnt vmcnt(0)\n"
    "ds_read_b128 v[136:139], v3 offset:0\n"
    "ds_read_b128 v[140:143], v3 offset:1024\n"
    "ds_read_b128 v[144:147], v3 offset:2048\n"
    "ds_read_b128 v[148:151], v3 offset:3072\n"
    "s_waitcnt lgkmcnt(3)\n"
    "v_max3_u32 v176, v136, v137, v138\n"
    "s_waitcnt lgkmcnt(2)\n"
    "v_max3_u32 v176, v139, v140, v176\n"
    "v_max3_u32 v176, v141, v142, v176\n"
    "s_waitcnt lgkmcnt(1)\n"
    "v_max3_u32 v176, v143, v144, v176\n"
    "v_max3_u32 v176, v145, v146, v176\n"
    "s_waitcnt lgkmcnt(0)\n"
    "v_max3_u32 v176, v147, v148, v176\n"
    "v_max3_u32 v176, v149, v150, v176\n"
    "v_max_u32 v176, v151, v176\n"
    "v_cmp_eq_u32 vcc, -1, v176\n"
    "s_cbranch_vccz hg_61_%=\n"
    "s_sleep 2\n"
    "s_sub_i32 s0, s0, 1\n"
    "s_cmp_lt_i32 s0, 0\n"
    "s_cbranch_scc0 hp_59_%=\n"
    "s_branch hg_61_%=\n"
    "hg_61_%=:\n"
    "v_mfma_f32_4x4x4_16b_bf16 v[176:179], v[136:137], v[136:137], 0\n"
    "v_mfma_f32_16x16x32_bf16 v[180:183], v[124:127], v[136:139], 0\n"
    "s_nop 0\n"
    "v_mfma_f32_4x4x4_16b_bf16 v[176:179], v[138:139], v[138:139], v[176:179]\n"
    "s_nop 1\n"
    "v_mfma_f32_4x4x4_16b_bf16 v[176:179], v[140:141], v[140:141], v[176:179]\n"
    "v_mfma_f32_16x16x32_bf16 v[180:183], v[124:127], v[140:143], v[180:183]\n"
    "s_nop 0\n"
    "v_mfma_f32_4x4x4_16b_bf16 v[176:179], v[142:143], v[142:143], v[176:179]\n"
    "s_nop 1\n"
    "v_mfma_f32_4x4x4_16b_bf16 v[176:179], v[144:145], v[144:145], v[176:179]\n"
    "v_mfma_f32_16x16x32_bf16 v[180:183], v[124:127], v[144:147], v[180:183]\n"
    "s_nop 0\n"
    "v_mfma_f32_4x4x4_16b_bf16 v[176:179], v[146:147], v[146:147], v[176:179]\n"
    "s_nop 1\n"
    "v_mfma_f32_4x4x4_16b_bf16 v[176:179], v[148:149], v[148:149], v[176:179]\n"
    "v_mfma_f32_16x16x32_bf16 v[180:183], v[124:127], v[148:151], v[180:183]\n"
    "s_nop 0\n"
    "v_mfma_f32_4x4x4_16b_bf16 v[176:179], v[150:151], v[150:151], v[176:179]\n"
    "s_nop 7\n"
    "s_nop 7\n"
    "s_branch hb_60_%=\n"
    "hp_62_%=:\n"
    "s_add_u32 m0, s78, 32768\n"
    "s_add_u32 s87, s74, 98304\n"
    "buffer_load_dwordx4 v2, s[12:15], s87 offen offset:0 sc1 lds\n"
    "buffer_load_dwordx4 v2, s[12:15], s87 offen offset:1024 sc1 lds\n"
    "buffer_load_dwordx4 v2, s[12:15], s87 offen offset:2048 sc1 lds\n"
    "buffer_load_dwordx4 v2, s[12:15], s87 offen offset:3072 sc1 lds\n"
    "s_waitcnt vmcnt(0)\n"
    "ds_read_b128 v[136:139], v3 offset:32768\n"
    "ds_read_b128 v[140:143], v3 offset:33792\n"
    "ds_read_b128 v[144:147], v3 offset:34816\n"
    "ds_read_b128 v[148:151], v3 offset:35840\n"
    "s_waitcnt lgkmcnt(3)\n"
    "v_max3_u32 v176, v136, v137, v138\n"
    "s_waitcnt lgkmcnt(2)\n"
    "v_max3_u32 v176, v139, v140, v176\n"
    "v_max3_u32 v176, v141, v142, v176\n"
    "s_waitcnt lgkmcnt(1)\n"
    "v_max3_u32 v176, v143, v144, v176\n"
    "v_max3_u32 v176, v145, v146, v176\n"
    "s_waitcnt lgkmcnt(0)\n"
    "v_max3_u32 v176, v147, v148, v176\n"
    "v_max3_u32 v176, v149, v150, v176\n"
    "v_max_u32 v176, v151, v176\n"
    "v_cmp_eq_u32 vcc, -1, v176\n"
    "s_cbranch_vccz hg_64_%=\n"
    "s_sleep 2\n"
    "s_sub_i32 s0, s0, 1\n"
    "s_cmp_lt_i32 s0, 0\n"
    "s_cbranch_scc0 hp_62_%=\n"
    "s_branch hg_64_%=\n"
    "hg_64_%=:\n"
    "v_mfma_f32_4x4x4_16b_bf16 v[176:179], v[136:137], v[136:137], 0\n"
    "v_mfma_f32_16x16x32_bf16 v[180:183], v[124:127], v[136:139], 0\n"
    "s_nop 0\n"
    "v_mfma_f32_4x4x4_16b_bf16 v[176:179], v[138:139], v[138:139], v[176:179]\n"
    "s_nop 1\n"
    "v_mfma_f32_4x4x4_16b_bf16 v[176:179], v[140:141], v[140:141], v[176:179]\n"
    "v_mfma_f32_16x16x32_bf16 v[180:183], v[124:127], v[140:143], v[180:183]\n"
    "s_nop 0\n"
    "v_mfma_f32_4x4x4_16b_bf16 v[176:179], v[142:143], v[142:143], v[176:179]\n"
    "s_nop 1\n"
    "v_mfma_f32_4x4x4_16b_bf16 v[176:179], v[144:145], v[144:145], v[176:179]\n"
    "v_mfma_f32_16x16x32_bf16 v[180:183], v[124:127], v[144:147], v[180:183]\n"
    "s_nop 0\n"
    "v_mfma_f32_4x4x4_16b_bf16 v[176:179], v[146:147], v[146:147], v[176:179]\n"
    "s_nop 1\n"
    "v_mfma_f32_4x4x4_16b_bf16 v[176:179], v[148:149], v[148:149], v[176:179]\n"
    "v_mfma_f32_16x16x32_bf16 v[180:183], v[124:127], v[148:151], v[180:183]\n"
    "s_nop 0\n"
    "v_mfma_f32_4x4x4_16b_bf16 v[176:179], v[150:151], v[150:151], v[176:179]\n"
    "s_nop 7\n"
    "s_nop 7\n"
    "s_branch hb_63_%=\n"
    "gp_65_%=:\n"
    "s_add_u32 s86, s75, 0\n"
    "buffer_load_dwordx4 v[184:187], v248, s[12:15], s86 offen sc1\n"
    "s_add_u32 s86, s75, 2048\n"
    "buffer_load_dwordx4 v[188:191], v248, s[12:15], s86 offen sc1\n"
    "s_add_u32 s86, s75, 4096\n"
    "buffer_load_dwordx4 v[192:195], v248, s[12:15], s86 offen sc1\n"
    "s_add_u32 s86, s75, 6144\n"
    "buffer_load_dwordx4 v[196:199], v248, s[12:15], s86 offen sc1\n"
    "s_add_u32 s86, s75, 8192\n"
    "buffer_load_dwordx4 v[200:203], v248, s[12:15], s86 offen sc1\n"
    "s_add_u32 s86, s75, 10240\n"
    "buffer_load_dwordx4 v[204:207], v248, s[12:15], s86 offen sc1\n"
    "s_add_u32 s86, s75, 12288\n"
    "buffer_load_dwordx4 v[208:211], v248, s[12:15], s86 offen sc1\n"
    "s_waitcnt vmcnt(6)\n"
    "v_max3_u32 v13, v184, v185, v186\n"
    "s_waitcnt vmcnt(5)\n"
    "v_max3_u32 v13, v187, v188, v13\n"
    "v_max3_u32 v13, v189, v190, v13\n"
    "s_waitcnt vmcnt(4)\n"
    "v_max3_u32 v13, v191, v192, v13\n"
    "v_max3_u32 v13, v193, v194, v13\n"
    "s_waitcnt vmcnt(3)\n"
    "v_max3_u32 v13, v195, v196, v13\n"
    "v_max3_u32 v13, v197, v198, v13\n"
    "s_waitcnt vmcnt(2)\n"
    "v_max3_u32 v13, v199, v200, v13\n"
    "v_max3_u32 v13, v201, v202, v13\n"
    "s_waitcnt vmcnt(1)\n"
    "v_max3_u32 v13, v203, v204, v13\n"
    "v_max3_u32 v13, v205, v206, v13\n"
    "s_waitcnt vmcnt(0)\n"
    "v_max3_u32 v13, v207, v208, v13\n"
    "v_max3_u32 v13, v209, v210, v13\n"
    "v_max_u32 v13, v211, v13\n"
    "v_cmp_eq_u32 vcc, -1, v13\n"
    "s_cbranch_vccz gg_67_%=\n"
    "s_sleep 2\n"
    "s_sub_i32 s0, s0, 1\n"
    "s_cmp_lt_i32 s0, 0\n"
    "s_cbranch_scc0 gp_65_%=\n"
    "s_branch gg_67_%=\n"
    "gg_67_%=:\n"
    "v_mfma_f32_16x16x32_bf16 v[168:171], v[72:75], v[184:187], 0\n"
    "v_mfma_f32_16x16x32_bf16 v[172:175], v[76:79], v[184:187], 0\n"
    "v_mfma_f32_16x16x32_bf16 v[176:179], v[112:115], v[184:187], 0\n"
    "v_mfma_f32_16x16x32_bf16 v[180:183], v[116:119], v[184:187], 0\n"
    "v_mfma_f32_16x16x32_bf16 v[168:171], v[32:35], v[188:191], v[168:171]\n"
    "v_mfma_f32_16x16x32_bf16 v[172:175], v[36:39], v[188:191], v[172:175]\n"
    "v_mfma_f32_16x16x32_bf16 v[176:179], v[40:43], v[188:191], v[176:179]\n"
    "v_mfma_f32_16x16x32_bf16 v[180:183], v[44:47], v[188:191], v[180:183]\n"
    "v_mfma_f32_16x16x32_bf16 v[168:171], v[232:235], v[192:195], v[168:171]\n"
    "v_mfma_f32_16x16x32_bf16 v[172:175], v[236:239], v[192:195], v[172:175]\n"
    "v_mfma_f32_16x16x32_bf16 v[176:179], v[240:243], v[192:195], v[176:179]\n"
    "v_mfma_f32_16x16x32_bf16 v[180:183], v[244:247], v[192:195], v[180:183]\n"
    "v_mfma_f32_16x16x32_bf16 v[168:171], v[16:19], v[196:199], v[168:171]\n"
    "v_mfma_f32_16x16x32_bf16 v[172:175], v[20:23], v[196:199], v[172:175]\n"
    "v_mfma_f32_16x16x32_bf16 v[176:179], v[24:27], v[196:199], v[176:179]\n"
    "v_mfma_f32_16x16x32_bf16 v[180:183], v[28:31], v[196:199], v[180:183]\n"
    "v_mfma_f32_16x16x32_bf16 v[168:171], v[136:139], v[200:203], v[168:171]\n"
    "v_mfma_f32_16x16x32_bf16 v[172:175], v[140:143], v[200:203], v[172:175]\n"
    "v_mfma_f32_16x16x32_bf16 v[176:179], v[144:147], v[200:203], v[176:179]\n"
    "v_mfma_f32_16x16x32_bf16 v[180:183], v[148:151], v[200:203], v[180:183]\n"
    "v_mfma_f32_16x16x32_bf16 v[168:171], v[80:83], v[204:207], v[168:171]\n"
    "v_mfma_f32_16x16x32_bf16 v[172:175], v[84:87], v[204:207], v[172:175]\n"
    "v_mfma_f32_16x16x32_bf16 v[176:179], v[88:91], v[204:207], v[176:179]\n"
    "v_mfma_f32_16x16x32_bf16 v[180:183], v[92:95], v[204:207], v[180:183]\n"
    "v_mfma_f32_16x16x32_bf16 v[168:171], v[96:99], v[208:211], v[168:171]\n"
    "v_mfma_f32_16x16x32_bf16 v[172:175], v[100:103], v[208:211], v[172:175]\n"
    "v_mfma_f32_16x16x32_bf16 v[176:179], v[104:107], v[208:211], v[176:179]\n"
    "v_mfma_f32_16x16x32_bf16 v[180:183], v[108:111], v[208:211], v[180:183]\n"
    "s_nop 7\n"
    "s_nop 7\n"
    "s_branch gb_66_%=\n"
    "gp_68_%=:\n"
    "s_add_u32 s86, s75, 114688\n"
    "buffer_load_dwordx4 v[212:215], v248, s[12:15], s86 offen sc1\n"
    "s_add_u32 s86, s75, 116736\n"
    "buffer_load_dwordx4 v[216:219], v248, s[12:15], s86 offen sc1\n"
    "s_add_u32 s86, s75, 118784\n"
    "buffer_load_dwordx4 v[220:223], v248, s[12:15], s86 offen sc1\n"
    "s_add_u32 s86, s75, 120832\n"
    "buffer_load_dwordx4 v[224:227], v248, s[12:15], s86 offen sc1\n"
    "s_add_u32 s86, s75, 122880\n"
    "buffer_load_dwordx4 v[228:231], v248, s[12:15], s86 offen sc1\n"
    "s_add_u32 s86, s75, 124928\n"
    "buffer_load_dwordx4 v[48:51], v248, s[12:15], s86 offen sc1\n"
    "s_add_u32 s86, s75, 126976\n"
    "buffer_load_dwordx4 v[52:55], v248, s[12:15], s86 offen sc1\n"
    "s_waitcnt vmcnt(6)\n"
    "v_max3_u32 v13, v212, v213, v214\n"
    "s_waitcnt vmcnt(5)\n"
    "v_max3_u32 v13, v215, v216, v13\n"
    "v_max3_u32 v13, v217, v218, v13\n"
    "s_waitcnt vmcnt(4)\n"
    "v_max3_u32 v13, v219, v220, v13\n"
    "v_max3_u32 v13, v221, v222, v13\n"
    "s_waitcnt vmcnt(3)\n"
    "v_max3_u32 v13, v223, v224, v13\n"
    "v_max3_u32 v13, v225, v226, v13\n"
    "s_waitcnt vmcnt(2)\n"
    "v_max3_u32 v13, v227, v228, v13\n"
    "v_max3_u32 v13, v229, v230, v13\n"
    "s_waitcnt vmcnt(1)\n"
    "v_max3_u32 v13, v231, v48, v13\n"
    "v_max3_u32 v13, v49, v50, v13\n"
    "s_waitcnt vmcnt(0)\n"
    "v_max3_u32 v13, v51, v52, v13\n"
    "v_max3_u32 v13, v53, v54, v13\n"
    "v_max_u32 v13, v55, v13\n"
    "v_cmp_eq_u32 vcc, -1, v13\n"
    "s_cbranch_vccz gg_70_%=\n"
    "s_sleep 2\n"
    "s_sub_i32 s0, s0, 1\n"
    "s_cmp_lt_i32 s0, 0\n"
    "s_cbranch_scc0 gp_68_%=\n"
    "s_branch gg_70_%=\n"
    "gg_70_%=:\n"
    "v_mfma_f32_16x16x32_bf16 v[152:155], v[72:75], v[212:215], 0\n"
    "v_mfma_f32_16x16x32_bf16 v[156:159], v[76:79], v[212:215], 0\n"
    "v_mfma_f32_16x16x32_bf16 v[160:163], v[112:115], v[212:215], 0\n"
    "v_mfma_f32_16x16x32_bf16 v[164:167], v[116:119], v[212:215], 0\n"
    "v_mfma_f32_16x16x32_bf16 v[152:155], v[32:35], v[216:219], v[152:155]\n"
    "v_mfma_f32_16x16x32_bf16 v[156:159], v[36:39], v[216:219], v[156:159]\n"
    "v_mfma_f32_16x16x32_bf16 v[160:163], v[40:43], v[216:219], v[160:163]\n"
    "v_mfma_f32_16x16x32_bf16 v[164:167], v[44:47], v[216:219], v[164:167]\n"
    "v_mfma_f32_16x16x32_bf16 v[152:155], v[232:235], v[220:223], v[152:155]\n"
    "v_mfma_f32_16x16x32_bf16 v[156:159], v[236:239], v[220:223], v[156:159]\n"
    "v_mfma_f32_16x16x32_bf16 v[160:163], v[240:243], v[220:223], v[160:163]\n"
    "v_mfma_f32_16x16x32_bf16 v[164:167], v[244:247], v[220:223], v[164:167]\n"
    "v_mfma_f32_16x16x32_bf16 v[152:155], v[16:19], v[224:227], v[152:155]\n"
    "v_mfma_f32_16x16x32_bf16 v[156:159], v[20:23], v[224:227], v[156:159]\n"
    "v_mfma_f32_16x16x32_bf16 v[160:163], v[24:27], v[224:227], v[160:163]\n"
    "v_mfma_f32_16x16x32_bf16 v[164:167], v[28:31], v[224:227], v[164:167]\n"
    "v_mfma_f32_16x16x32_bf16 v[152:155], v[136:139], v[228:231], v[152:155]\n"
    "v_mfma_f32_16x16x32_bf16 v[156:159], v[140:143], v[228:231], v[156:159]\n"
    "v_mfma_f32_16x16x32_bf16 v[160:163], v[144:147], v[228:231], v[160:163]\n"
    "v_mfma_f32_16x16x32_bf16 v[164:167], v[148:151], v[228:231], v[164:167]\n"
    "v_mfma_f32_16x16x32_bf16 v[152:155], v[80:83], v[48:51], v[152:155]\n"
    "v_mfma_f32_16x16x32_bf16 v[156:159], v[84:87], v[48:51], v[156:159]\n"
    "v_mfma_f32_16x16x32_bf16 v[160:163], v[88:91], v[48:51], v[160:163]\n"
    "v_mfma_f32_16x16x32_bf16 v[164:167], v[92:95], v[48:51], v[164:167]\n"
    "v_mfma_f32_16x16x32_bf16 v[152:155], v[96:99], v[52:55], v[152:155]\n"
    "v_mfma_f32_16x16x32_bf16 v[156:159], v[100:103], v[52:55], v[156:159]\n"
    "v_mfma_f32_16x16x32_bf16 v[160:163], v[104:107], v[52:55], v[160:163]\n"
    "v_mfma_f32_16x16x32_bf16 v[164:167], v[108:111], v[52:55], v[164:167]\n"
    "s_nop 7\n"
    "s_nop 7\n"
    "s_branch gb_69_%=\n"
    "gp_71_%=:\n"
    "s_add_u32 s86, s75, 229376\n"
    "buffer_load_dwordx4 v[184:187], v248, s[12:15], s86 offen sc1\n"
    "s_add_u32 s86, s75, 231424\n"
    "buffer_load_dwordx4 v[188:191], v248, s[12:15], s86 offen sc1\n"
    "s_add_u32 s86, s75, 233472\n"
    "buffer_load_dwordx4 v[192:195], v248, s[12:15], s86 offen sc1\n"
    "s_add_u32 s86, s75, 235520\n"
    "buffer_load_dwordx4 v[196:199], v248, s[12:15], s86 offen sc1\n"
    "s_add_u32 s86, s75, 237568\n"
    "buffer_load_dwordx4 v[200:203], v248, s[12:15], s86 offen sc1\n"
    "s_add_u32 s86, s75, 239616\n"
    "buffer_load_dwordx4 v[204:207], v248, s[12:15], s86 offen sc1\n"
    "s_add_u32 s86, s75, 241664\n"
    "buffer_load_dwordx4 v[208:211], v248, s[12:15], s86 offen sc1\n"
    "s_waitcnt vmcnt(6)\n"
    "v_max3_u32 v13, v184, v185, v186\n"
    "s_waitcnt vmcnt(5)\n"
    "v_max3_u32 v13, v187, v188, v13\n"
    "v_max3_u32 v13, v189, v190, v13\n"
    "s_waitcnt vmcnt(4)\n"
    "v_max3_u32 v13, v191, v192, v13\n"
    "v_max3_u32 v13, v193, v194, v13\n"
    "s_waitcnt vmcnt(3)\n"
    "v_max3_u32 v13, v195, v196, v13\n"
    "v_max3_u32 v13, v197, v198, v13\n"
    "s_waitcnt vmcnt(2)\n"
    "v_max3_u32 v13, v199, v200, v13\n"
    "v_max3_u32 v13, v201, v202, v13\n"
    "s_waitcnt vmcnt(1)\n"
    "v_max3_u32 v13, v203, v204, v13\n"
    "v_max3_u32 v13, v205, v206, v13\n"
    "s_waitcnt vmcnt(0)\n"
    "v_max3_u32 v13, v207, v208, v13\n"
    "v_max3_u32 v13, v209, v210, v13\n"
    "v_max_u32 v13, v211, v13\n"
    "v_cmp_eq_u32 vcc, -1, v13\n"
    "s_cbranch_vccz gg_73_%=\n"
    "s_sleep 2\n"
    "s_sub_i32 s0, s0, 1\n"
    "s_cmp_lt_i32 s0, 0\n"
    "s_cbranch_scc0 gp_71_%=\n"
    "s_branch gg_73_%=\n"
    "gg_73_%=:\n"
    "v_mfma_f32_16x16x32_bf16 v[168:171], v[72:75], v[184:187], 0\n"
    "v_mfma_f32_16x16x32_bf16 v[172:175], v[76:79], v[184:187], 0\n"
    "v_mfma_f32_16x16x32_bf16 v[176:179], v[112:115], v[184:187], 0\n"
    "v_mfma_f32_16x16x32_bf16 v[180:183], v[116:119], v[184:187], 0\n"
    "v_mfma_f32_16x16x32_bf16 v[168:171], v[32:35], v[188:191], v[168:171]\n"
    "v_mfma_f32_16x16x32_bf16 v[172:175], v[36:39], v[188:191], v[172:175]\n"
    "v_mfma_f32_16x16x32_bf16 v[176:179], v[40:43], v[188:191], v[176:179]\n"
    "v_mfma_f32_16x16x32_bf16 v[180:183], v[44:47], v[188:191], v[180:183]\n"
    "v_mfma_f32_16x16x32_bf16 v[168:171], v[232:235], v[192:195], v[168:171]\n"
    "v_mfma_f32_16x16x32_bf16 v[172:175], v[236:239], v[192:195], v[172:175]\n"
    "v_mfma_f32_16x16x32_bf16 v[176:179], v[240:243], v[192:195], v[176:179]\n"
    "v_mfma_f32_16x16x32_bf16 v[180:183], v[244:247], v[192:195], v[180:183]\n"
    "v_mfma_f32_16x16x32_bf16 v[168:171], v[16:19], v[196:199], v[168:171]\n"
    "v_mfma_f32_16x16x32_bf16 v[172:175], v[20:23], v[196:199], v[172:175]\n"
    "v_mfma_f32_16x16x32_bf16 v[176:179], v[24:27], v[196:199], v[176:179]\n"
    "v_mfma_f32_16x16x32_bf16 v[180:183], v[28:31], v[196:199], v[180:183]\n"
    "v_mfma_f32_16x16x32_bf16 v[168:171], v[136:139], v[200:203], v[168:171]\n"
    "v_mfma_f32_16x16x32_bf16 v[172:175], v[140:143], v[200:203], v[172:175]\n"
    "v_mfma_f32_16x16x32_bf16 v[176:179], v[144:147], v[200:203], v[176:179]\n"
    "v_mfma_f32_16x16x32_bf16 v[180:183], v[148:151], v[200:203], v[180:183]\n"
    "v_mfma_f32_16x16x32_bf16 v[168:171], v[80:83], v[204:207], v[168:171]\n"
    "v_mfma_f32_16x16x32_bf16 v[172:175], v[84:87], v[204:207], v[172:175]\n"
    "v_mfma_f32_16x16x32_bf16 v[176:179], v[88:91], v[204:207], v[176:179]\n"
    "v_mfma_f32_16x16x32_bf16 v[180:183], v[92:95], v[204:207], v[180:183]\n"
    "v_mfma_f32_16x16x32_bf16 v[168:171], v[96:99], v[208:211], v[168:171]\n"
    "v_mfma_f32_16x16x32_bf16 v[172:175], v[100:103], v[208:211], v[172:175]\n"
    "v_mfma_f32_16x16x32_bf16 v[176:179], v[104:107], v[208:211], v[176:179]\n"
    "v_mfma_f32_16x16x32_bf16 v[180:183], v[108:111], v[208:211], v[180:183]\n"
    "s_nop 7\n"
    "s_nop 7\n"
    "s_branch gb_72_%=\n"
    "gp_74_%=:\n"
    "s_add_u32 s86, s75, 344064\n"
    "buffer_load_dwordx4 v[212:215], v248, s[12:15], s86 offen sc1\n"
    "s_add_u32 s86, s75, 346112\n"
    "buffer_load_dwordx4 v[216:219], v248, s[12:15], s86 offen sc1\n"
    "s_add_u32 s86, s75, 348160\n"
    "buffer_load_dwordx4 v[220:223], v248, s[12:15], s86 offen sc1\n"
    "s_add_u32 s86, s75, 350208\n"
    "buffer_load_dwordx4 v[224:227], v248, s[12:15], s86 offen sc1\n"
    "s_add_u32 s86, s75, 352256\n"
    "buffer_load_dwordx4 v[228:231], v248, s[12:15], s86 offen sc1\n"
    "s_add_u32 s86, s75, 354304\n"
    "buffer_load_dwordx4 v[48:51], v248, s[12:15], s86 offen sc1\n"
    "s_add_u32 s86, s75, 356352\n"
    "buffer_load_dwordx4 v[52:55], v248, s[12:15], s86 offen sc1\n"
    "s_waitcnt vmcnt(6)\n"
    "v_max3_u32 v13, v212, v213, v214\n"
    "s_waitcnt vmcnt(5)\n"
    "v_max3_u32 v13, v215, v216, v13\n"
    "v_max3_u32 v13, v217, v218, v13\n"
    "s_waitcnt vmcnt(4)\n"
    "v_max3_u32 v13, v219, v220, v13\n"
    "v_max3_u32 v13, v221, v222, v13\n"
    "s_waitcnt vmcnt(3)\n"
    "v_max3_u32 v13, v223, v224, v13\n"
    "v_max3_u32 v13, v225, v226, v13\n"
    "s_waitcnt vmcnt(2)\n"
    "v_max3_u32 v13, v227, v228, v13\n"
    "v_max3_u32 v13, v229, v230, v13\n"
    "s_waitcnt vmcnt(1)\n"
    "v_max3_u32 v13, v231, v48, v13\n"
    "v_max3_u32 v13, v49, v50, v13\n"
    "s_waitcnt vmcnt(0)\n"
    "v_max3_u32 v13, v51, v52, v13\n"
    "v_max3_u32 v13, v53, v54, v13\n"
    "v_max_u32 v13, v55, v13\n"
    "v_cmp_eq_u32 vcc, -1, v13\n"
    "s_cbranch_vccz gg_76_%=\n"
    "s_sleep 2\n"
    "s_sub_i32 s0, s0, 1\n"
    "s_cmp_lt_i32 s0, 0\n"
    "s_cbranch_scc0 gp_74_%=\n"
    "s_branch gg_76_%=\n"
    "gg_76_%=:\n"
    "v_mfma_f32_16x16x32_bf16 v[152:155], v[72:75], v[212:215], 0\n"
    "v_mfma_f32_16x16x32_bf16 v[156:159], v[76:79], v[212:215], 0\n"
    "v_mfma_f32_16x16x32_bf16 v[160:163], v[112:115], v[212:215], 0\n"
    "v_mfma_f32_16x16x32_bf16 v[164:167], v[116:119], v[212:215], 0\n"
    "v_mfma_f32_16x16x32_bf16 v[152:155], v[32:35], v[216:219], v[152:155]\n"
    "v_mfma_f32_16x16x32_bf16 v[156:159], v[36:39], v[216:219], v[156:159]\n"
    "v_mfma_f32_16x16x32_bf16 v[160:163], v[40:43], v[216:219], v[160:163]\n"
    "v_mfma_f32_16x16x32_bf16 v[164:167], v[44:47], v[216:219], v[164:167]\n"
    "v_mfma_f32_16x16x32_bf16 v[152:155], v[232:235], v[220:223], v[152:155]\n"
    "v_mfma_f32_16x16x32_bf16 v[156:159], v[236:239], v[220:223], v[156:159]\n"
    "v_mfma_f32_16x16x32_bf16 v[160:163], v[240:243], v[220:223], v[160:163]\n"
    "v_mfma_f32_16x16x32_bf16 v[164:167], v[244:247], v[220:223], v[164:167]\n"
    "v_mfma_f32_16x16x32_bf16 v[152:155], v[16:19], v[224:227], v[152:155]\n"
    "v_mfma_f32_16x16x32_bf16 v[156:159], v[20:23], v[224:227], v[156:159]\n"
    "v_mfma_f32_16x16x32_bf16 v[160:163], v[24:27], v[224:227], v[160:163]\n"
    "v_mfma_f32_16x16x32_bf16 v[164:167], v[28:31], v[224:227], v[164:167]\n"
    "v_mfma_f32_16x16x32_bf16 v[152:155], v[136:139], v[228:231], v[152:155]\n"
    "v_mfma_f32_16x16x32_bf16 v[156:159], v[140:143], v[228:231], v[156:159]\n"
    "v_mfma_f32_16x16x32_bf16 v[160:163], v[144:147], v[228:231], v[160:163]\n"
    "v_mfma_f32_16x16x32_bf16 v[164:167], v[148:151], v[228:231], v[164:167]\n"
    "v_mfma_f32_16x16x32_bf16 v[152:155], v[80:83], v[48:51], v[152:155]\n"
    "v_mfma_f32_16x16x32_bf16 v[156:159], v[84:87], v[48:51], v[156:159]\n"
    "v_mfma_f32_16x16x32_bf16 v[160:163], v[88:91], v[48:51], v[160:163]\n"
    "v_mfma_f32_16x16x32_bf16 v[164:167], v[92:95], v[48:51], v[164:167]\n"
    "v_mfma_f32_16x16x32_bf16 v[152:155], v[96:99], v[52:55], v[152:155]\n"
    "v_mfma_f32_16x16x32_bf16 v[156:159], v[100:103], v[52:55], v[156:159]\n"
    "v_mfma_f32_16x16x32_bf16 v[160:163], v[104:107], v[52:55], v[160:163]\n"
    "v_mfma_f32_16x16x32_bf16 v[164:167], v[108:111], v[52:55], v[164:167]\n"
    "s_nop 7\n"
    "s_nop 7\n"
    "s_branch gb_75_%=\n"
    "op_77_%=:\n"
    "s_add_u32 s85, s73, 98304\n"
    "buffer_load_dwordx4 v[32:35], v2, s[12:15], s85 offen offset:0 sc1\n"
    "buffer_load_dwordx4 v[36:39], v2, s[12:15], s85 offen offset:1024 sc1\n"
    "buffer_load_dwordx4 v[40:43], v2, s[12:15], s85 offen offset:2048 sc1\n"
    "buffer_load_dwordx4 v[44:47], v2, s[12:15], s85 offen offset:3072 sc1\n"
    "s_waitcnt vmcnt(3)\n"
    "v_max3_u32 v16, v32, v33, v34\n"
    "s_waitcnt vmcnt(2)\n"
    "v_max3_u32 v16, v35, v36, v16\n"
    "v_max3_u32 v16, v37, v38, v16\n"
    "s_waitcnt vmcnt(1)\n"
    "v_max3_u32 v16, v39, v40, v16\n"
    "v_max3_u32 v16, v41, v42, v16\n"
    "s_waitcnt vmcnt(0)\n"
    "v_max3_u32 v16, v43, v44, v16\n"
    "v_max3_u32 v16, v45, v46, v16\n"
    "v_max_u32 v16, v47, v16\n"
    "v_cmp_eq_u32 vcc, -1, v16\n"
    "s_cbranch_vccz og_79_%=\n"
    "s_sleep 2\n"
    "s_sub_i32 s0, s0, 1\n"
    "s_cmp_lt_i32 s0, 0\n"
    "s_cbranch_scc0 op_77_%=\n"
    "s_branch og_79_%=\n"
    "og_79_%=:\n"
    "v_mfma_f32_32x32x16_bf16 v[16:31], v[232:235], v[32:35], 0\n"
    "v_mfma_f32_32x32x16_bf16 v[16:31], v[236:239], v[36:39], v[16:31]\n"
    "v_mfma_f32_32x32x16_bf16 v[16:31], v[240:243], v[40:43], v[16:31]\n"
    "v_mfma_f32_32x32x16_bf16 v[16:31], v[244:247], v[44:47], v[16:31]\n"
    "s_nop 7\n"
    "s_nop 7\n"
    "s_branch ob_78_%=\n"
    "hp_80_%=:\n"
    "s_add_u32 m0, s78, 32768\n"
    "s_add_u32 s87, s74, 32768\n"
    "buffer_load_dwordx4 v2, s[12:15], s87 offen offset:0 sc1 lds\n"
    "buffer_load_dwordx4 v2, s[12:15], s87 offen offset:1024 sc1 lds\n"
    "buffer_load_dwordx4 v2, s[12:15], s87 offen offset:2048 sc1 lds\n"
    "buffer_load_dwordx4 v2, s[12:15], s87 offen offset:3072 sc1 lds\n"
    "s_waitcnt vmcnt(0)\n"
    "ds_read_b128 v[136:139], v3 offset:32768\n"
    "ds_read_b128 v[140:143], v3 offset:33792\n"
    "ds_read_b128 v[144:147], v3 offset:34816\n"
    "ds_read_b128 v[148:151], v3 offset:35840\n"
    "s_waitcnt lgkmcnt(3)\n"
    "v_max3_u32 v176, v136, v137, v138\n"
    "s_waitcnt lgkmcnt(2)\n"
    "v_max3_u32 v176, v139, v140, v176\n"
    "v_max3_u32 v176, v141, v142, v176\n"
    "s_waitcnt lgkmcnt(1)\n"
    "v_max3_u32 v176, v143, v144, v176\n"
    "v_max3_u32 v176, v145, v146, v176\n"
    "s_waitcnt lgkmcnt(0)\n"
    "v_max3_u32 v176, v147, v148, v176\n"
    "v_max3_u32 v176, v149, v150, v176\n"
    "v_max_u32 v176, v151, v176\n"
    "v_cmp_eq_u32 vcc, -1, v176\n"
    "s_cbranch_vccz hg_82_%=\n"
    "s_sleep 2\n"
    "s_sub_i32 s0, s0, 1\n"
    "s_cmp_lt_i32 s0, 0\n"
    "s_cbranch_scc0 hp_80_%=\n"
    "s_branch hg_82_%=\n"
    "hg_82_%=:\n"
    "v_mfma_f32_4x4x4_16b_bf16 v[176:179], v[136:137], v[136:137], 0\n"
    "v_mfma_f32_16x16x32_bf16 v[180:183], v[124:127], v[136:139], 0\n"
    "s_nop 0\n"
    "v_mfma_f32_4x4x4_16b_bf16 v[176:179], v[138:139], v[138:139], v[176:179]\n"
    "s_nop 1\n"
    "v_mfma_f32_4x4x4_16b_bf16 v[176:179], v[140:141], v[140:141], v[176:179]\n"
    "v_mfma_f32_16x16x32_bf16 v[180:183], v[124:127], v[140:143], v[180:183]\n"
    "s_nop 0\n"
    "v_mfma_f32_4x4x4_16b_bf16 v[176:179], v[142:143], v[142:143], v[176:179]\n"
    "s_nop 1\n"
    "v_mfma_f32_4x4x4_16b_bf16 v[176:179], v[144:145], v[144:145], v[176:179]\n"
    "v_mfma_f32_16x16x32_bf16 v[180:183], v[124:127], v[144:147], v[180:183]\n"
    "s_nop 0\n"
    "v_mfma_f32_4x4x4_16b_bf16 v[176:179], v[146:147], v[146:147], v[176:179]\n"
    "s_nop 1\n"
    "v_mfma_f32_4x4x4_16b_bf16 v[176:179], v[148:149], v[148:149], v[176:179]\n"
    "v_mfma_f32_16x16x32_bf16 v[180:183], v[124:127], v[148:151], v[180:183]\n"
    "s_nop 0\n"
    "v_mfma_f32_4x4x4_16b_bf16 v[176:179], v[150:151], v[150:151], v[176:179]\n"
    "v_mfma_f32_32x32x16_bf16 v[152:167], v[184:187], v[136:139], 0\n"
    "s_nop 7\n"
    "s_nop 7\n"
    "s_branch hb_81_%=\n"
    "hp_83_%=:\n"
    "s_add_u32 m0, s78, 0\n"
    "s_add_u32 s87, s74, 65536\n"
    "buffer_load_dwordx4 v2, s[12:15], s87 offen offset:0 sc1 lds\n"
    "buffer_load_dwordx4 v2, s[12:15], s87 offen offset:1024 sc1 lds\n"
    "buffer_load_dwordx4 v2, s[12:15], s87 offen offset:2048 sc1 lds\n"
    "buffer_load_dwordx4 v2, s[12:15], s87 offen offset:3072 sc1 lds\n"
    "s_waitcnt vmcnt(0)\n"
    "ds_read_b128 v[136:139], v3 offset:0\n"
    "ds_read_b128 v[140:143], v3 offset:1024\n"
    "ds_read_b128 v[144:147], v3 offset:2048\n"
    "ds_read_b128 v[148:151], v3 offset:3072\n"
    "s_waitcnt lgkmcnt(3)\n"
    "v_max3_u32 v176, v136, v137, v138\n"
    "s_waitcnt lgkmcnt(2)\n"
    "v_max3_u32 v176, v139, v140, v176\n"
    "v_max3_u32 v176, v141, v142, v176\n"
    "s_waitcnt lgkmcnt(1)\n"
    "v_max3_u32 v176, v143, v144, v176\n"
    "v_max3_u32 v176, v145, v146, v176\n"
    "s_waitcnt lgkmcnt(0)\n"
    "v_max3_u32 v176, v147, v148, v176\n"
    "v_max3_u32 v176, v149, v150, v176\n"
    "v_max_u32 v176, v151, v176\n"
    "v_cmp_eq_u32 vcc, -1, v176\n"
    "s_cbranch_vccz hg_85_%=\n"
    "s_sleep 2\n"
    "s_sub_i32 s0, s0, 1\n"
    "s_cmp_lt_i32 s0, 0\n"
    "s_cbranch_scc0 hp_83_%=\n"
    "s_branch hg_85_%=\n"
    "hg_85_%=:\n"
    "v_mfma_f32_4x4x4_16b_bf16 v[176:179], v[136:137], v[136:137], 0\n"
    "v_mfma_f32_16x16x32_bf16 v[180:183], v[124:127], v[136:139], 0\n"
    "s_nop 0\n"
    "v_mfma_f32_4x4x4_16b_bf16 v[176:179], v[138:139], v[138:139], v[176:179]\n"
    "s_nop 1\n"
    "v_mfma_f32_4x4x4_16b_bf16 v[176:179], v[140:141], v[140:141], v[176:179]\n"
    "v_mfma_f32_16x16x32_bf16 v[180:183], v[124:127], v[140:143], v[180:183]\n"
    "s_nop 0\n"
    "v_mfma_f32_4x4x4_16b_bf16 v[176:179], v[142:143], v[142:143], v[176:179]\n"
    "s_nop 1\n"
    "v_mfma_f32_4x4x4_16b_bf16 v[176:179], v[144:145], v[144:145], v[176:179]\n"
    "v_mfma_f32_16x16x32_bf16 v[180:183], v[124:127], v[144:147], v[180:183]\n"
    "s_nop 0\n"
    "v_mfma_f32_4x4x4_16b_bf16 v[176:179], v[146:147], v[146:147], v[176:179]\n"
    "s_nop 1\n"
    "v_mfma_f32_4x4x4_16b_bf16 v[176:179], v[148:149], v[148:149], v[176:179]\n"
    "v_mfma_f32_16x16x32_bf16 v[180:183], v[124:127], v[148:151], v[180:183]\n"
    "s_nop 0\n"
    "v_mfma_f32_4x4x4_16b_bf16 v[176:179], v[150:151], v[150:151], v[176:179]\n"
    "s_nop 7\n"
    "s_nop 7\n"
    "s_branch hb_84_%=\n"
    "hp_86_%=:\n"
    "s_add_u32 m0, s78, 32768\n"
    "s_add_u32 s87, s74, 98304\n"
    "buffer_load_dwordx4 v2, s[12:15], s87 offen offset:0 sc1 lds\n"
    "buffer_load_dwordx4 v2, s[12:15], s87 offen offset:1024 sc1 lds\n"
    "buffer_load_dwordx4 v2, s[12:15], s87 offen offset:2048 sc1 lds\n"
    "buffer_load_dwordx4 v2, s[12:15], s87 offen offset:3072 sc1 lds\n"
    "s_waitcnt vmcnt(0)\n"
    "ds_read_b128 v[136:139], v3 offset:32768\n"
    "ds_read_b128 v[140:143], v3 offset:33792\n"
    "ds_read_b128 v[144:147], v3 offset:34816\n"
    "ds_read_b128 v[148:151], v3 offset:35840\n"
    "s_waitcnt lgkmcnt(3)\n"
    "v_max3_u32 v176, v136, v137, v138\n"
    "s_waitcnt lgkmcnt(2)\n"
    "v_max3_u32 v176, v139, v140, v176\n"
    "v_max3_u32 v176, v141, v142, v176\n"
    "s_waitcnt lgkmcnt(1)\n"
    "v_max3_u32 v176, v143, v144, v176\n"
    "v_max3_u32 v176, v145, v146, v176\n"
    "s_waitcnt lgkmcnt(0)\n"
    "v_max3_u32 v176, v147, v148, v176\n"
    "v_max3_u32 v176, v149, v150, v176\n"
    "v_max_u32 v176, v151, v176\n"
    "v_cmp_eq_u32 vcc, -1, v176\n"
    "s_cbranch_vccz hg_88_%=\n"
    "s_sleep 2\n"
    "s_sub_i32 s0, s0, 1\n"
    "s_cmp_lt_i32 s0, 0\n"
    "s_cbranch_scc0 hp_86_%=\n"
    "s_branch hg_88_%=\n"
    "hg_88_%=:\n"
    "v_mfma_f32_4x4x4_16b_bf16 v[176:179], v[136:137], v[136:137], 0\n"
    "v_mfma_f32_16x16x32_bf16 v[180:183], v[124:127], v[136:139], 0\n"
    "s_nop 0\n"
    "v_mfma_f32_4x4x4_16b_bf16 v[176:179], v[138:139], v[138:139], v[176:179]\n"
    "s_nop 1\n"
    "v_mfma_f32_4x4x4_16b_bf16 v[176:179], v[140:141], v[140:141], v[176:179]\n"
    "v_mfma_f32_16x16x32_bf16 v[180:183], v[124:127], v[140:143], v[180:183]\n"
    "s_nop 0\n"
    "v_mfma_f32_4x4x4_16b_bf16 v[176:179], v[142:143], v[142:143], v[176:179]\n"
    "s_nop 1\n"
    "v_mfma_f32_4x4x4_16b_bf16 v[176:179], v[144:145], v[144:145], v[176:179]\n"
    "v_mfma_f32_16x16x32_bf16 v[180:183], v[124:127], v[144:147], v[180:183]\n"
    "s_nop 0\n"
    "v_mfma_f32_4x4x4_16b_bf16 v[176:179], v[146:147], v[146:147], v[176:179]\n"
    "s_nop 1\n"
    "v_mfma_f32_4x4x4_16b_bf16 v[176:179], v[148:149], v[148:149], v[176:179]\n"
    "v_mfma_f32_16x16x32_bf16 v[180:183], v[124:127], v[148:151], v[180:183]\n"
    "s_nop 0\n"
    "v_mfma_f32_4x4x4_16b_bf16 v[176:179], v[150:151], v[150:151], v[176:179]\n"
    "s_nop 7\n"
    "s_nop 7\n"
    "s_branch hb_87_%=\n"
    "gp_89_%=:\n"
    "s_add_u32 s86, s75, 0\n"
    "buffer_load_dwordx4 v[184:187], v248, s[12:15], s86 offen sc1\n"
    "s_add_u32 s86, s75, 2048\n"
    "buffer_load_dwordx4 v[188:191], v248, s[12:15], s86 offen sc1\n"
    "s_add_u32 s86, s75, 4096\n"
    "buffer_load_dwordx4 v[192:195], v248, s[12:15], s86 offen sc1\n"
    "s_add_u32 s86, s75, 6144\n"
    "buffer_load_dwordx4 v[196:199], v248, s[12:15], s86 offen sc1\n"
    "s_add_u32 s86, s75, 8192\n"
    "buffer_load_dwordx4 v[200:203], v248, s[12:15], s86 offen sc1\n"
    "s_add_u32 s86, s75, 10240\n"
    "buffer_load_dwordx4 v[204:207], v248, s[12:15], s86 offen sc1\n"
    "s_add_u32 s86, s75, 12288\n"
    "buffer_load_dwordx4 v[208:211], v248, s[12:15], s86 offen sc1\n"
    "s_waitcnt vmcnt(6)\n"
    "v_max3_u32 v13, v184, v185, v186\n"
    "s_waitcnt vmcnt(5)\n"
    "v_max3_u32 v13, v187, v188, v13\n"
    "v_max3_u32 v13, v189, v190, v13\n"
    "s_waitcnt vmcnt(4)\n"
    "v_max3_u32 v13, v191, v192, v13\n"
    "v_max3_u32 v13, v193, v194, v13\n"
    "s_waitcnt vmcnt(3)\n"
    "v_max3_u32 v13, v195, v196, v13\n"
    "v_max3_u32 v13, v197, v198, v13\n"
    "s_waitcnt vmcnt(2)\n"
    "v_max3_u32 v13, v199, v200, v13\n"
    "v_max3_u32 v13, v201, v202, v13\n"
    "s_waitcnt vmcnt(1)\n"
    "v_max3_u32 v13, v203, v204, v13\n"
    "v_max3_u32 v13, v205, v206, v13\n"
    "s_waitcnt vmcnt(0)\n"
    "v_max3_u32 v13, v207, v208, v13\n"
    "v_max3_u32 v13, v209, v210, v13\n"
    "v_max_u32 v13, v211, v13\n"
    "v_cmp_eq_u32 vcc, -1, v13\n"
    "s_cbranch_vccz gg_91_%=\n"
    "s_sleep 2\n"
    "s_sub_i32 s0, s0, 1\n"
    "s_cmp_lt_i32 s0, 0\n"
    "s_cbranch_scc0 gp_89_%=\n"
    "s_branch gg_91_%=\n"
    "gg_91_%=:\n"
    "v_mfma_f32_16x16x32_bf16 v[168:171], v[72:75], v[184:187], 0\n"
    "v_mfma_f32_16x16x32_bf16 v[168:171], v[32:35], v[188:191], v[168:171]\n"
    "v_mfma_f32_16x16x32_bf16 v[168:171], v[232:235], v[192:195], v[168:171]\n"
    "v_mfma_f32_16x16x32_bf16 v[168:171], v[16:19], v[196:199], v[168:171]\n"
    "v_mfma_f32_16x16x32_bf16 v[168:171], v[136:139], v[200:203], v[168:171]\n"
    "v_mfma_f32_16x16x32_bf16 v[168:171], v[80:83], v[204:207], v[168:171]\n"
    "v_mfma_f32_16x16x32_bf16 v[168:171], v[96:99], v[208:211], v[168:171]\n"
    "v_mfma_f32_16x16x32_bf16 v[172:175], v[76:79], v[184:187], 0\n"
    "v_mfma_f32_16x16x32_bf16 v[172:175], v[36:39], v[188:191], v[172:175]\n"
    "s_nop 7\n"
    "s_nop 7\n"
    "s_branch gb_90_%=\n"
    "gp_92_%=:\n"
    "s_add_u32 s86, s75, 114688\n"
    "buffer_load_dwordx4 v[212:215], v248, s[12:15], s86 offen sc1\n"
    "s_add_u32 s86, s75, 116736\n"
    "buffer_load_dwordx4 v[216:219], v248, s[12:15], s86 offen sc1\n"
    "s_add_u32 s86, s75, 118784\n"
    "buffer_load_dwordx4 v[220:223], v248, s[12:15], s86 offen sc1\n"
    "s_add_u32 s86, s75, 120832\n"
    "buffer_load_dwordx4 v[224:227], v248, s[12:15], s86 offen sc1\n"
    "s_add_u32 s86, s75, 122880\n"
    "buffer_load_dwordx4 v[228:231], v248, s[12:15], s86 offen sc1\n"
    "s_add_u32 s86, s75, 124928\n"
    "buffer_load_dwordx4 v[48:51], v248, s[12:15], s86 offen sc1\n"
    "s_add_u32 s86, s75, 126976\n"
    "buffer_load_dwordx4 v[52:55], v248, s[12:15], s86 offen sc1\n"
    "s_waitcnt vmcnt(6)\n"
    "v_max3_u32 v13, v212, v213, v214\n"
    "s_waitcnt vmcnt(5)\n"
    "v_max3_u32 v13, v215, v216, v13\n"
    "v_max3_u32 v13, v217, v218, v13\n"
    "s_waitcnt vmcnt(4)\n"
    "v_max3_u32 v13, v219, v220, v13\n"
    "v_max3_u32 v13, v221, v222, v13\n"
    "s_waitcnt vmcnt(3)\n"
    "v_max3_u32 v13, v223, v224, v13\n"
    "v_max3_u32 v13, v225, v226, v13\n"
    "s_waitcnt vmcnt(2)\n"
    "v_max3_u32 v13, v227, v228, v13\n"
    "v_max3_u32 v13, v229, v230, v13\n"
    "s_waitcnt vmcnt(1)\n"
    "v_max3_u32 v13, v231, v48, v13\n"
    "v_max3_u32 v13, v49, v50, v13\n"
    "s_waitcnt vmcnt(0)\n"
    "v_max3_u32 v13, v51, v52, v13\n"
    "v_max3_u32 v13, v53, v54, v13\n"
    "v_max_u32 v13, v55, v13\n"
    "v_cmp_eq_u32 vcc, -1, v13\n"
    "s_cbranch_vccz gg_94_%=\n"
    "s_sleep 2\n"
    "s_sub_i32 s0, s0, 1\n"
    "s_cmp_lt_i32 s0, 0\n"
    "s_cbranch_scc0 gp_92_%=\n"
    "s_branch gg_94_%=\n"
    "gg_94_%=:\n"
    "v_mfma_f32_16x16x32_bf16 v[152:155], v[72:75], v[212:215], 0\n"
    "v_mfma_f32_16x16x32_bf16 v[152:155], v[32:35], v[216:219], v[152:155]\n"
    "v_mfma_f32_16x16x32_bf16 v[152:155], v[232:235], v[220:223], v[152:155]\n"
    "v_mfma_f32_16x16x32_bf16 v[152:155], v[16:19], v[224:227], v[152:155]\n"
    "v_mfma_f32_16x16x32_bf16 v[152:155], v[136:139], v[228:231], v[152:155]\n"
    "v_mfma_f32_16x16x32_bf16 v[152:155], v[80:83], v[48:51], v[152:155]\n"
    "v_mfma_f32_16x16x32_bf16 v[152:155], v[96:99], v[52:55], v[152:155]\n"
    "v_mfma_f32_16x16x32_bf16 v[156:159], v[76:79], v[212:215], 0\n"
    "v_mfma_f32_16x16x32_bf16 v[156:159], v[36:39], v[216:219], v[156:159]\n"
    "s_nop 7\n"
    "s_nop 7\n"
    "s_branch gb_93_%=\n"
    "gp_95_%=:\n"
    "s_add_u32 s86, s75, 229376\n"
    "buffer_load_dwordx4 v[184:187], v248, s[12:15], s86 offen sc1\n"
    "s_add_u32 s86, s75, 231424\n"
    "buffer_load_dwordx4 v[188:191], v248, s[12:15], s86 offen sc1\n"
    "s_add_u32 s86, s75, 233472\n"
    "buffer_load_dwordx4 v[192:195], v248, s[12:15], s86 offen sc1\n"
    "s_add_u32 s86, s75, 235520\n"
    "buffer_load_dwordx4 v[196:199], v248, s[12:15], s86 offen sc1\n"
    "s_add_u32 s86, s75, 237568\n"
    "buffer_load_dwordx4 v[200:203], v248, s[12:15], s86 offen sc1\n"
    "s_add_u32 s86, s75, 239616\n"
    "buffer_load_dwordx4 v[204:207], v248, s[12:15], s86 offen sc1\n"
    "s_add_u32 s86, s75, 241664\n"
    "buffer_load_dwordx4 v[208:211], v248, s[12:15], s86 offen sc1\n"
    "s_waitcnt vmcnt(6)\n"
    "v_max3_u32 v13, v184, v185, v186\n"
    "s_waitcnt vmcnt(5)\n"
    "v_max3_u32 v13, v187, v188, v13\n"
    "v_max3_u32 v13, v189, v190, v13\n"
    "s_waitcnt vmcnt(4)\n"
    "v_max3_u32 v13, v191, v192, v13\n"
    "v_max3_u32 v13, v193, v194, v13\n"
    "s_waitcnt vmcnt(3)\n"
    "v_max3_u32 v13, v195, v196, v13\n"
    "v_max3_u32 v13, v197, v198, v13\n"
    "s_waitcnt vmcnt(2)\n"
    "v_max3_u32 v13, v199, v200, v13\n"
    "v_max3_u32 v13, v201, v202, v13\n"
    "s_waitcnt vmcnt(1)\n"
    "v_max3_u32 v13, v203, v204, v13\n"
    "v_max3_u32 v13, v205, v206, v13\n"
    "s_waitcnt vmcnt(0)\n"
    "v_max3_u32 v13, v207, v208, v13\n"
    "v_max3_u32 v13, v209, v210, v13\n"
    "v_max_u32 v13, v211, v13\n"
    "v_cmp_eq_u32 vcc, -1, v13\n"
    "s_cbranch_vccz gg_97_%=\n"
    "s_sleep 2\n"
    "s_sub_i32 s0, s0, 1\n"
    "s_cmp_lt_i32 s0, 0\n"
    "s_cbranch_scc0 gp_95_%=\n"
    "s_branch gg_97_%=\n"
    "gg_97_%=:\n"
    "v_mfma_f32_16x16x32_bf16 v[168:171], v[72:75], v[184:187], 0\n"
    "v_mfma_f32_16x16x32_bf16 v[168:171], v[32:35], v[188:191], v[168:171]\n"
    "v_mfma_f32_16x16x32_bf16 v[168:171], v[232:235], v[192:195], v[168:171]\n"
    "v_mfma_f32_16x16x32_bf16 v[168:171], v[16:19], v[196:199], v[168:171]\n"
    "v_mfma_f32_16x16x32_bf16 v[168:171], v[136:139], v[200:203], v[168:171]\n"
    "v_mfma_f32_16x16x32_bf16 v[168:171], v[80:83], v[204:207], v[168:171]\n"
    "v_mfma_f32_16x16x32_bf16 v[168:171], v[96:99], v[208:211], v[168:171]\n"
    "v_mfma_f32_16x16x32_bf16 v[172:175], v[76:79], v[184:187], 0\n"
    "v_mfma_f32_16x16x32_bf16 v[172:175], v[36:39], v[188:191], v[172:175]\n"
    "s_nop 7\n"
    "s_nop 7\n"
    "s_branch gb_96_%=\n"
    "gp_98_%=:\n"
    "s_add_u32 s86, s75, 344064\n"
    "buffer_load_dwordx4 v[212:215], v248, s[12:15], s86 offen sc1\n"
    "s_add_u32 s86, s75, 346112\n"
    "buffer_load_dwordx4 v[216:219], v248, s[12:15], s86 offen sc1\n"
    "s_add_u32 s86, s75, 348160\n"
    "buffer_load_dwordx4 v[220:223], v248, s[12:15], s86 offen sc1\n"
    "s_add_u32 s86, s75, 350208\n"
    "buffer_load_dwordx4 v[224:227], v248, s[12:15], s86 offen sc1\n"
    "s_add_u32 s86, s75, 352256\n"
    "buffer_load_dwordx4 v[228:231], v248, s[12:15], s86 offen sc1\n"
    "s_add_u32 s86, s75, 354304\n"
    "buffer_load_dwordx4 v[48:51], v248, s[12:15], s86 offen sc1\n"
    "s_add_u32 s86, s75, 356352\n"
    "buffer_load_dwordx4 v[52:55], v248, s[12:15], s86 offen sc1\n"
    "s_waitcnt vmcnt(6)\n"
    "v_max3_u32 v13, v212, v213, v214\n"
    "s_waitcnt vmcnt(5)\n"
    "v_max3_u32 v13, v215, v216, v13\n"
    "v_max3_u32 v13, v217, v218, v13\n"
    "s_waitcnt vmcnt(4)\n"
    "v_max3_u32 v13, v219, v220, v13\n"
    "v_max3_u32 v13, v221, v222, v13\n"
    "s_waitcnt vmcnt(3)\n"
    "v_max3_u32 v13, v223, v224, v13\n"
    "v_max3_u32 v13, v225, v226, v13\n"
    "s_waitcnt vmcnt(2)\n"
    "v_max3_u32 v13, v227, v228, v13\n"
    "v_max3_u32 v13, v229, v230, v13\n"
    "s_waitcnt vmcnt(1)\n"
    "v_max3_u32 v13, v231, v48, v13\n"
    "v_max3_u32 v13, v49, v50, v13\n"
    "s_waitcnt vmcnt(0)\n"
    "v_max3_u32 v13, v51, v52, v13\n"
    "v_max3_u32 v13, v53, v54, v13\n"
    "v_max_u32 v13, v55, v13\n"
    "v_cmp_eq_u32 vcc, -1, v13\n"
    "s_cbranch_vccz gg_100_%=\n"
    "s_sleep 2\n"
    "s_sub_i32 s0, s0, 1\n"
    "s_cmp_lt_i32 s0, 0\n"
    "s_cbranch_scc0 gp_98_%=\n"
    "s_branch gg_100_%=\n"
    "gg_100_%=:\n"
    "v_mfma_f32_16x16x32_bf16 v[152:155], v[72:75], v[212:215], 0\n"
    "v_mfma_f32_16x16x32_bf16 v[152:155], v[32:35], v[216:219], v[152:155]\n"
    "v_mfma_f32_16x16x32_bf16 v[152:155], v[232:235], v[220:223], v[152:155]\n"
    "v_mfma_f32_16x16x32_bf16 v[152:155], v[16:19], v[224:227], v[152:155]\n"
    "v_mfma_f32_16x16x32_bf16 v[152:155], v[136:139], v[228:231], v[152:155]\n"
    "v_mfma_f32_16x16x32_bf16 v[152:155], v[80:83], v[48:51], v[152:155]\n"
    "v_mfma_f32_16x16x32_bf16 v[152:155], v[96:99], v[52:55], v[152:155]\n"
    "v_mfma_f32_16x16x32_bf16 v[156:159], v[76:79], v[212:215], 0\n"
    "v_mfma_f32_16x16x32_bf16 v[156:159], v[36:39], v[216:219], v[156:159]\n"
    "s_nop 7\n"
    "s_nop 7\n"
    "s_branch gb_99_%=\n"
    "Lend_%=:\n"
    "s_waitcnt vmcnt(0) lgkmcnt(0)\n"
    :
    : "{s[0:1]}"(__builtin_amdgcn_kernarg_segment_ptr()), "{s2}"(__builtin_amdgcn_workgroup_id_x()), "{v0}"(__builtin_amdgcn_workitem_id_x())
    : "v1", "v2", "v3", "v4", "v5", "v6", "v7", "v8", "v9", "v10", "v11", "v12", "v13", "v14", "v15", "v16", "v17", "v18", "v19", "v20", "v21", "v22", "v23", "v24", "v25", "v26", "v27", "v28", "v29", "v30", "v31", "v32", "v33", "v34", "v35", "v36", "v37", "v38", "v39", "v40", "v41", "v42", "v43", "v44", "v45", "v46", "v47", "v48", "v49", "v50", "v51", "v52", "v53", "v54", "v55", "v56", "v57", "v58", "v59", "v60", "v61", "v62", "v63", "v64", "v65", "v66", "v67", "v68", "v69", "v70", "v71", "v72", "v73", "v74", "v75", "v76", "v77", "v78", "v79", "v80", "v81", "v82", "v83", "v84", "v85", "v86", "v87", "v88", "v89", "v90", "v91", "v92", "v93", "v94", "v95", "v96", "v97", "v98", "v99", "v100", "v101", "v102", "v103", "v104", "v105", "v106", "v107", "v108", "v109", "v110", "v111", "v112", "v113", "v114", "v115", "v116", "v117", "v118", "v119", "v120", "v121", "v122", "v123", "v124", "v125", "v126", "v127", "v128", "v129", "v130", "v131", "v132", "v133", "v134", "v135", "v136", "v137", "v138", "v139", "v140", "v141", "v142", "v143", "v144", "v145", "v146", "v147", "v148", "v149", "v150", "v151", "v152", "v153", "v154", "v155", "v156", "v157", "v158", "v159", "v160", "v161", "v162", "v163", "v164", "v165", "v166", "v167", "v168", "v169", "v170", "v171", "v172", "v173", "v174", "v175", "v176", "v177", "v178", "v179", "v180", "v181", "v182", "v183", "v184", "v185", "v186", "v187", "v188", "v189", "v190", "v191", "v192", "v193", "v194", "v195", "v196", "v197", "v198", "v199", "v200", "v201", "v202", "v203", "v204", "v205", "v206", "v207", "v208", "v209", "v210", "v211", "v212", "v213", "v214", "v215", "v216", "v217", "v218", "v219", "v220", "v221", "v222", "v223", "v224", "v225", "v226", "v227", "v228", "v229", "v230", "v231", "v232", "v233", "v234", "v235", "v236", "v237", "v238", "v239", "v240", "v241", "v242", "v243", "v244", "v245", "v246", "v247", "v248", "v249", "v250", "v251", "v252", "v253", "v254", "v255", "s3", "s4", "s5", "s6", "s7", "s8", "s9", "s10", "s11", "s12", "s13", "s14", "s15", "s16", "s17", "s18", "s19", "s20", "s21", "s22", "s23", "s24", "s25", "s26", "s27", "s28", "s29", "s30", "s31", "s32", "s33", "s34", "s35", "s36", "s37", "s38", "s39", "s40", "s41", "s42", "s43", "s44", "s45", "s46", "s47", "s48", "s49", "s50", "s51", "s52", "s53", "s54", "s55", "s56", "s57", "s58", "s59", "s60", "s61", "s62", "s63", "s64", "s65", "s66", "s67", "s68", "s69", "s70", "s71", "s72", "s73", "s74", "s75", "s76", "s77", "s78", "s79", "s80", "s81", "s82", "s83", "s84", "s85", "s86", "s87", "s88", "s89", "s90", "s91", "s92", "s93", "s94", "s95", "s96", "s97", "s98", "s99", "s100", "s101", "vcc", "m0", "scc", "memory");
}

extern "C" void kernel_launch(void* const* d_in, const int* in_sizes, int n_in,
                              void* d_out, int out_size, void* d_ws, size_t ws_size,
                              hipStream_t stream) {
  (void)hipFuncSetAttribute((const void*)mega, hipFuncAttributeMaxDynamicSharedMemorySize, 163840);
  mega<<<256, 512, 163840, stream>>>(d_in[0], d_in[1], d_in[2], d_in[4], d_in[5], d_in[6], d_in[7], d_ws, d_out);
}
```
